```python
import torch

def _softmax(x, axis=-1):
    return torch.softmax(x, dim=axis)

def _gelu(x):
    return torch.nn.functional.gelu(x, approximate='tanh')

def _silu(x):
    return torch.nn.functional.silu(x)

def _sigmoid(x):
    return torch.sigmoid(x)

def _layernorm(x, axis=-1, eps=1e-05):
    m = x.mean(dim=axis, keepdim=True)
    v = x.var(dim=axis, keepdim=True, unbiased=False)
    return (x - m) / torch.sqrt(v + eps)

def _scatter_add(tgt, idx, src, axis=0):
    return tgt.clone().index_add_(axis, idx, src)

def _segment_sum(src, seg, n):
    out = torch.zeros((n,) + src.shape[1:], dtype=src.dtype)
    return out.index_add_(0, seg, src)

def _pad(x, pw):
    flat = tuple((v for pair in reversed(pw) for v in pair))
    return torch.nn.functional.pad(x, flat)
kernel_name = 'adaLN_cond__gqa_block_cached__glu_gated_geglu__decode'

def setup_inputs():

    _w = lambda a: (lambda t: t.to(torch.bfloat16) if t.is_floating_point() else t)(torch.as_tensor(a))

    def _A_setup():
        B, Sq, D = (8, 1, 576)
        Hq, Hkv, Sk, Dh = (32, 16, 8192, 128)
        assert Hq % Hkv == 0, (Hq, Hkv)
        return {'x': _w((lambda *s: torch.randn(*s, dtype=torch.bfloat16) * 0.1)(B, Sq, D)), 'k_cache': _w((lambda *s: torch.randn(*s, dtype=torch.bfloat16) * 0.8)(B, Hkv, Sk, Dh)), 'v_cache': _w((lambda *s: torch.randn(*s, dtype=torch.bfloat16) * 3.2)(B, Hkv, Sk, Dh)), 'wq': _w((lambda *s: torch.randn(*s, dtype=torch.bfloat16) * 0.8)(D, Hq * Dh) * D ** (-0.5)), 'wo': _w((lambda *s: torch.randn(*s, dtype=torch.bfloat16) * 3.2)(Hq * Dh, D) * (Hq * Dh) ** (-0.5))}
    _A_in = _A_setup()

    def _B_setup():
        B, S, Dm, Df = (8, 1, 576, 4608)
        return {'x': _w((lambda *s: torch.randn(*s, dtype=torch.bfloat16) * 0.1)(B, S, Dm)), 'w_gate': _w((lambda *s: torch.randn(*s, dtype=torch.bfloat16) * 3.2)(Dm, Df) * Dm ** (-0.5)), 'w_up': _w((lambda *s: torch.randn(*s, dtype=torch.bfloat16) * 3.2)(Dm, Df) * Dm ** (-0.5)), 'w_down': _w((lambda *s: torch.randn(*s, dtype=torch.bfloat16) * 3.2)(Df, Dm) * Df ** (-0.5))}
    _B_in = _B_setup()
    return {'A_x': _A_in['x'], 'A_k_cache': _A_in['k_cache'], 'A_v_cache': _A_in['v_cache'], 'A_wq': _A_in['wq'], 'A_wo': _A_in['wo'], 'B_x': _B_in['x'], 'B_w_gate': _B_in['w_gate'], 'B_w_up': _B_in['w_up'], 'B_w_down': _B_in['w_down'], 'c': _w((lambda *s: torch.randn(*s, dtype=torch.bfloat16) * 0.4)(8, 576) * 8.0), 'w_mod': _w((lambda *s: torch.randn(*s, dtype=torch.bfloat16) * 0.4)(576, 6 * 576) * 576 ** (-0.5) * 8.0)}

def reference(A_x, A_k_cache, A_v_cache, A_wq, A_wo, B_x, B_w_gate, B_w_up, B_w_down, c, w_mod):

    def _A_ref(x, k_cache, v_cache, wq, wo):
        B, Sq, D = x.shape
        Hq, Dh = (32, 128)
        Hkv = k_cache.shape[1]
        rep = Hq // Hkv
        q = (lambda x, *d: x.permute(*d))((x @ wq).reshape(B, Sq, Hq, Dh), 0, 2, 1, 3)
        k = (lambda x, n, axis: torch.repeat_interleave(x, n, dim=axis))(k_cache, rep, axis=1)
        v = (lambda x, n, axis: torch.repeat_interleave(x, n, dim=axis))(v_cache, rep, axis=1)
        scores = torch.einsum('bhqd,bhkd->bhqk', q, k) * Dh ** (-0.5)
        p = _softmax(scores, axis=-1)
        o = torch.einsum('bhqk,bhkd->bhqd', p, v)
        return (lambda x, *d: x.permute(*d))(o, 0, 2, 1, 3).reshape(B, Sq, Hq * Dh) @ wo

    def _B_ref(x, w_gate, w_up, w_down):
        g = torch.einsum('bsd,df->bsf', x, w_gate)
        u = torch.einsum('bsd,df->bsf', x, w_up)
        h = _gelu(g) * u
        return torch.einsum('bsf,fd->bsd', h, w_down)
    x = A_x
    mod = (_silu(c) @ w_mod).reshape(c.shape[0], 6, 1, -1)
    sa, ca, ga, sb, cb, gb = (mod[:, 0].clone(), mod[:, 1].clone(), mod[:, 2].clone(), mod[:, 3].clone(), mod[:, 4].clone(), mod[:, 5].clone())
    h = _layernorm(x, axis=-1) * (1 + ca) + sa
    x = x + ga * _A_ref(h, A_k_cache, A_v_cache, A_wq, A_wo)
    h = _layernorm(x, axis=-1) * (1 + cb) + sb
    x = x + gb * _B_ref(h, B_w_gate, B_w_up, B_w_down)
    return x
```

```cpp
#include <hip/hip_runtime.h>
#include <hip/hip_bf16.h>
#include <stdint.h>

#define MIMIC 1

typedef unsigned short u16;
__device__ __forceinline__ float bf2f(u16 v) { return __uint_as_float(((unsigned)v) << 16); }
__device__ __forceinline__ u16 f2bf(float f) { unsigned u = __float_as_uint(f); u += 0x7FFFu + ((u >> 16) & 1u); return (u16)(u >> 16); }
__device__ __forceinline__ float rb(float f) {
  return bf2f(f2bf(f));
}

constexpr int B = 8, D = 576, HQ = 32, HKV = 16, SK = 8192, DH = 128, DF = 4608;

__global__ void k_mod(const u16* c, const u16* wmod, float* mod) {
  __shared__ float sc[B * D];
  for (int i = threadIdx.x; i < B * D; i += blockDim.x) { float v = bf2f(c[i]); sc[i] = rb(v / (1.f + expf(-v))); }
  __syncthreads();
  int j = blockIdx.x * blockDim.x + threadIdx.x;
  if (j >= 6 * D) return;
  float acc[B];
  for (int b = 0; b < B; ++b) acc[b] = 0.f;
  for (int k = 0; k < D; ++k) { float w = bf2f(wmod[(size_t)k * 6 * D + j]); for (int b = 0; b < B; ++b) acc[b] += sc[b * D + k] * w; }
  for (int b = 0; b < B; ++b) mod[b * 6 * D + j] = rb(acc[b]);
}

__device__ void ln_mod(const float* x, const float* sh, const float* scl, float* h, int tid, int nthr, float* red) {
  if (tid == 0) {
    float s = 0.f; for (int i = 0; i < D; ++i) s += x[i];
    float m = s / D; float v = 0.f; for (int i = 0; i < D; ++i) { float d = x[i] - m; v += d * d; }
    v /= D;
    red[0] = rb(m); red[1] = rb(sqrtf(rb(rb(v) + 1e-5f)));
  }
  __syncthreads();
  float m = red[0], sd = red[1];
  for (int i = tid; i < D; i += nthr) { float n = rb(rb(x[i] - m) / sd); h[i] = rb(rb(n * rb(1.f + scl[i])) + sh[i]); }
  __syncthreads();
}

__global__ void k_q(const u16* x, const float* mod, const u16* wq, float* q) {
  __shared__ float xs[D], h[D], red[2];
  int b = blockIdx.y;
  for (int i = threadIdx.x; i < D; i += blockDim.x) xs[i] = bf2f(x[b * D + i]);
  __syncthreads();
  ln_mod(xs, mod + b * 6 * D + 0 * D, mod + b * 6 * D + 1 * D, h, threadIdx.x, blockDim.x, red);
  int j = blockIdx.x * blockDim.x + threadIdx.x;
  float acc = 0.f;
  for (int k = 0; k < D; ++k) acc += h[k] * bf2f(wq[(size_t)k * HQ * DH + j]);
  q[b * HQ * DH + j] = rb(acc);
}

__global__ void __launch_bounds__(256) k_attn(const float* q, const u16* kc, const u16* vc, float* o) {
  __shared__ float s[SK];
  __shared__ float qs[DH];
  __shared__ float red[256];
  int h = blockIdx.x, b = blockIdx.y, hk = h / 2, tid = threadIdx.x;
  if (tid < DH) qs[tid] = q[b * HQ * DH + h * DH + tid];
  __syncthreads();
  const u16* kb = kc + ((size_t)(b * HKV + hk) * SK) * DH;
  const u16* vb = vc + ((size_t)(b * HKV + hk) * SK) * DH;
  float mx = -INFINITY;
  for (int k = tid; k < SK; k += 256) {
    const uint4* kr = (const uint4*)(kb + (size_t)k * DH);
    float acc = 0.f;
#pragma unroll 2
    for (int i = 0; i < 16; ++i) { uint4 w = kr[i];
      acc += qs[i * 8 + 0] * __uint_as_float(w.x << 16); acc += qs[i * 8 + 1] * __uint_as_float(w.x & 0xffff0000u);
      acc += qs[i * 8 + 2] * __uint_as_float(w.y << 16); acc += qs[i * 8 + 3] * __uint_as_float(w.y & 0xffff0000u);
      acc += qs[i * 8 + 4] * __uint_as_float(w.z << 16); acc += qs[i * 8 + 5] * __uint_as_float(w.z & 0xffff0000u);
      acc += qs[i * 8 + 6] * __uint_as_float(w.w << 16); acc += qs[i * 8 + 7] * __uint_as_float(w.w & 0xffff0000u);
    }
    float sc = rb(rb(acc) * 0.08838834764831845f);
    s[k] = sc; mx = fmaxf(mx, sc);
  }
  red[tid] = mx; __syncthreads();
  for (int st = 128; st > 0; st >>= 1) { if (tid < st) red[tid] = fmaxf(red[tid], red[tid + st]); __syncthreads(); }
  mx = red[0]; __syncthreads();
  float sum = 0.f;
  for (int k = tid; k < SK; k += 256) { float e = expf(s[k] - mx); s[k] = e; sum += e; }
  red[tid] = sum; __syncthreads();
  for (int st = 128; st > 0; st >>= 1) { if (tid < st) red[tid] += red[tid + st]; __syncthreads(); }
  sum = red[0]; __syncthreads();
  for (int k = tid; k < SK; k += 256) s[k] = rb(s[k] / sum);
  __syncthreads();
  int d = tid & 127, half = tid >> 7;
  float acc = 0.f;
  for (int k = half * (SK / 2); k < (half + 1) * (SK / 2); ++k) acc += s[k] * bf2f(vb[(size_t)k * DH + d]);
  red[tid] = acc; __syncthreads();
  if (tid < 128) o[b * HQ * DH + h * DH + tid] = rb(red[tid] + red[tid + 128]);
}

__global__ void k_wo(const u16* x, const float* mod, const float* o, const u16* wo, float* x1) {
  __shared__ float os[HQ * DH];
  int b = blockIdx.y;
  for (int i = threadIdx.x; i < HQ * DH; i += blockDim.x) os[i] = o[b * HQ * DH + i];
  __syncthreads();
  int j = blockIdx.x * blockDim.x + threadIdx.x;
  float acc = 0.f;
  for (int k = 0; k < HQ * DH; ++k) acc += os[k] * bf2f(wo[(size_t)k * D + j]);
  float a = rb(acc);
  x1[b * D + j] = rb(bf2f(x[b * D + j]) + rb(mod[b * 6 * D + 2 * D + j] * a));
}

__global__ void k_gu(const float* x1, const float* mod, const u16* wg, const u16* wu, float* hh) {
  __shared__ float xs[D], h[D], red[2];
  int b = blockIdx.y;
  for (int i = threadIdx.x; i < D; i += blockDim.x) xs[i] = x1[b * D + i];
  __syncthreads();
  ln_mod(xs, mod + b * 6 * D + 3 * D, mod + b * 6 * D + 4 * D, h, threadIdx.x, blockDim.x, red);
  int j = blockIdx.x * blockDim.x + threadIdx.x;
  float g = 0.f, u = 0.f;
  for (int k = 0; k < D; ++k) { g += h[k] * bf2f(wg[(size_t)k * DF + j]); u += h[k] * bf2f(wu[(size_t)k * DF + j]); }
  g = rb(g); u = rb(u);
  float inner = 0.7978845608028654f * (g + 0.044715f * g * g * g);
  float ge = rb(0.5f * g * (1.f + tanhf(inner)));
  hh[b * DF + j] = rb(ge * u);
}

__global__ void k_down(const float* x1, const float* mod, const float* hh, const u16* wd, u16* out) {
  __shared__ float hs[DF];
  int b = blockIdx.y;
  for (int i = threadIdx.x; i < DF; i += blockDim.x) hs[i] = hh[b * DF + i];
  __syncthreads();
  int j = blockIdx.x * blockDim.x + threadIdx.x;
  float acc = 0.f;
  for (int k = 0; k < DF; ++k) acc += hs[k] * bf2f(wd[(size_t)k * D + j]);
  float a = rb(acc);
  out[b * D + j] = f2bf(x1[b * D + j] + rb(mod[b * 6 * D + 5 * D + j] * a));
}

extern "C" void kernel_launch(void* const* d_in, const int* in_sizes, int n_in, void* d_out, int out_size, void* d_ws, size_t ws_size, hipStream_t stream) {
  const u16* x = (const u16*)d_in[0];
  const u16* kc = (const u16*)d_in[1];
  const u16* vc = (const u16*)d_in[2];
  const u16* wq = (const u16*)d_in[3];
  const u16* wo = (const u16*)d_in[4];
  const u16* wg = (const u16*)d_in[6];
  const u16* wu = (const u16*)d_in[7];
  const u16* wd = (const u16*)d_in[8];
  const u16* c = (const u16*)d_in[9];
  const u16* wmod = (const u16*)d_in[10];
  float* ws = (float*)d_ws;
  float* mod = ws; float* q = mod + B * 6 * D; float* o = q + B * HQ * DH; float* x1 = o + B * HQ * DH; float* hh = x1 + B * D;
  k_mod<<<(6 * D + 63) / 64, 64, 0, stream>>>(c, wmod, mod);
  k_q<<<dim3(16, B), 256, 0, stream>>>(x, mod, wq, q);
  k_attn<<<dim3(HQ, B), 256, 0, stream>>>(q, kc, vc, o);
  k_wo<<<dim3(9, B), 64, 0, stream>>>(x, mod, o, wo, x1);
  k_gu<<<dim3(DF / 256, B), 256, 0, stream>>>(x1, mod, wg, wu, hh);
  k_down<<<dim3(9, B), 64, 0, stream>>>(x1, mod, hh, wd, (u16*)d_out);
}
```

```cpp
#include <hip/hip_runtime.h>
#include <stdint.h>
#pragma clang fp contract(off)

#define INFL 24
#define AUX_K "nt"
#define AUX_V "nt"
#define AUX_W "nt"
#define STAMPS 0

#define LAS __attribute__((address_space(3)))
#define GAS __attribute__((address_space(1)))
typedef unsigned u32;
typedef unsigned long long u64;
typedef unsigned short u16;
typedef u32 u32x4 __attribute__((ext_vector_type(4)));
typedef u32 u32x2 __attribute__((ext_vector_type(2)));
typedef float f32x4 __attribute__((ext_vector_type(4)));
typedef __bf16 bf16x8 __attribute__((ext_vector_type(8)));
typedef short s16x4 __attribute__((ext_vector_type(4)));

namespace {

constexpr int NB = 8, DM = 576, HKV = 16, SK = 8192, DH = 128, DF = 4608;
constexpr int NCU = 256, NT = 512;

constexpr int WS_CTRL = 0;
constexpr int WS_ALIVE = 4096;
constexpr int WS_H = 8192;
constexpr int WS_X1 = WS_H + 9216;
constexpr int WS_ST = WS_X1 + 9216;
constexpr int WS_MODT = WS_ST + 4096;
constexpr int WS_Q = WS_MODT + 36864;
constexpr int WS_OP = WS_Q + 65536;
constexpr int WS_WP = WS_OP + 262144;
constexpr int WS_FP = WS_WP + 147456;
constexpr int WS_END = WS_FP + 144 * 18432;
constexpr int WS_DBG = 4 << 20;

constexpr int L_RING = 0;
constexpr int L_SC = 131072;
constexpr int L_ACT = L_SC + 16384;
constexpr int L_QS = L_ACT + 9344;
constexpr int L_RED = L_QS + 512;
constexpr int L_STAT = L_RED + 3072;
constexpr int L_FLG = L_STAT + 64;
constexpr int LDS_BYTES = 163840;
static_assert(L_FLG + 256 <= LDS_BYTES, "lds");
constexpr int ACT_STRIDE = 1168;
constexpr int F_LANDED = 0, F_FREE = 1  , F_WREL = 9  , F_SC = 12, F_P1 = 13, F_INIT = 14, F_Q = 15, F_ABORT = 16;

constexpr u32 SENT = 0xFFFFFFFFu;
constexpr u32 LDS_SPIN_CAP = 1u << 21;
constexpr u32 GLB_SPIN_CAP = 1u << 17;
constexpr float LOG2E = 1.4426950408889634f;

struct Params {
  const u16* x; const unsigned char* kc; const unsigned char* vc; const unsigned char* wq; const unsigned char* wo;
  const unsigned char* wg; const unsigned char* wu; const unsigned char* wd; const u16* c; const unsigned char* wmod;
  u16* out; unsigned char* ws;
};

__device__ __forceinline__ float bf2f(u32 v16) { return __uint_as_float(v16 << 16); }
__device__ __forceinline__ float bflo(u32 d) { return __uint_as_float(d << 16); }
__device__ __forceinline__ float bfhi(u32 d) { return __uint_as_float(d & 0xffff0000u); }
__device__ __forceinline__ float rb(float f) { return (float)(__bf16)f; }
__device__ __forceinline__ u32 f2bf(float f) { return (u32)__builtin_bit_cast(u16, (__bf16)f); }
__device__ __forceinline__ u32 pack2(float lo, float hi) { return f2bf(lo) | (f2bf(hi) << 16); }
__device__ __forceinline__ u32 nosent(u32 p) { return p == SENT ? 0xFFFEFFFFu : p; }
__device__ __forceinline__ u32 vld(float f) { return __float_as_uint(f) | 1u; }
__device__ __forceinline__ f32x4 mfma(u32x4 a, u32x4 b, f32x4 c) {
  return __builtin_amdgcn_mfma_f32_16x16x32_bf16(__builtin_bit_cast(bf16x8, a), __builtin_bit_cast(bf16x8, b), c, 0, 0, 0);
}
__device__ __forceinline__ u32x4 mk_srd(const void* p, u32 bytes) {
  const u64 a = (u64)p;
  u32x4 s;
  s.x = __builtin_amdgcn_readfirstlane((u32)a);
  s.y = __builtin_amdgcn_readfirstlane((u32)(a >> 32) & 0xffffu);
  s.z = __builtin_amdgcn_readfirstlane(bytes);
  s.w = 0x00020000u;
  return s;
}
#define MEMBAR() asm volatile("" ::: "memory")
#define WAIT_LGKM0() asm volatile("s_waitcnt lgkmcnt(0)" ::: "memory")
#define WAIT_VM0() asm volatile("s_waitcnt vmcnt(0)" ::: "memory")

#define DMA1(AUX, srd, voff, soff, ldsaddr)                                                                      \
  do { u32 keep_;                                                                                                \
    asm volatile("s_mov_b32 %0, m0\n\ts_mov_b32 m0, %1\n\ts_nop 0\n\t"                                           \
                 "buffer_load_dwordx4 %4, %2, %3 offen " AUX " lds\n\ts_mov_b32 m0, %0"                          \
                 : "=&s"(keep_) : "s"(ldsaddr), "s"(srd), "s"(soff), "v"(voff) : "memory"); } while (0)
#define DMA4(AUX, srd, v0, v1, v2, v3, soff, ldsaddr)                                                            \
  do { u32 keep_;                                                                                                \
    asm volatile("s_mov_b32 %0, m0\n\ts_mov_b32 m0, %1\n\ts_nop 0\n\t"                                           \
                 "buffer_load_dwordx4 %4, %2, %3 offen " AUX " lds\n\t"                                          \
                 "buffer_load_dwordx4 %5, %2, %3 offen offset:1024 " AUX " lds\n\t"                              \
                 "buffer_load_dwordx4 %6, %2, %3 offen offset:2048 " AUX " lds\n\t"                              \
                 "buffer_load_dwordx4 %7, %2, %3 offen offset:3072 " AUX " lds\n\ts_mov_b32 m0, %0"              \
                 : "=&s"(keep_) : "s"(ldsaddr), "s"(srd), "s"(soff), "v"(v0), "v"(v1), "v"(v2), "v"(v3) : "memory"); } while (0)

template <int CTRL> __device__ __forceinline__ float dpp(float x) {
  return __builtin_bit_cast(float, __builtin_amdgcn_mov_dpp(__builtin_bit_cast(int, x), CTRL, 0xf, 0xf, true));
}
constexpr int ROR8 = 0x128, ROR4 = 0x124, ROR2 = 0x122, ROR1 = 0x121;
__device__ __forceinline__ float xrow_max(float x) {
  auto s = __builtin_amdgcn_permlane16_swap(__float_as_uint(x), __float_as_uint(x), false, false);
  x = fmaxf(__uint_as_float(s[0]), __uint_as_float(s[1]));
  auto t = __builtin_amdgcn_permlane32_swap(__float_as_uint(x), __float_as_uint(x), false, false);
  return fmaxf(__uint_as_float(t[0]), __uint_as_float(t[1]));
}
__device__ __forceinline__ float xrow_sum(float x) {
  auto s = __builtin_amdgcn_permlane16_swap(__float_as_uint(x), __float_as_uint(x), false, false);
  x = __uint_as_float(s[0]) + __uint_as_float(s[1]);
  auto t = __builtin_amdgcn_permlane32_swap(__float_as_uint(x), __float_as_uint(x), false, false);
  return __uint_as_float(t[0]) + __uint_as_float(t[1]);
}
__device__ __forceinline__ float wave_max(float x) {
  x = fmaxf(x, dpp<ROR8>(x)); x = fmaxf(x, dpp<ROR4>(x)); x = fmaxf(x, dpp<ROR2>(x)); x = fmaxf(x, dpp<ROR1>(x));
  return xrow_max(x);
}
__device__ __forceinline__ float wave_sum(float x) {
  x += dpp<ROR8>(x); x += dpp<ROR4>(x); x += dpp<ROR2>(x); x += dpp<ROR1>(x);
  return xrow_sum(x);
}
__device__ __forceinline__ float sum_x8_16_32(float x) {
  x += dpp<ROR8>(x);
  return xrow_sum(x);
}

typedef volatile LAS u32* flagp;
__device__ __forceinline__ void lds_wait_ge(flagp F, int idx, u32 need) {
  u32 spins = 0;
  while (F[idx] < need) {
    __builtin_amdgcn_s_sleep(1);
    if ((++spins & 63u) == 0u) { if (F[F_ABORT] != 0u) break; if (spins > LDS_SPIN_CAP) { F[F_ABORT] = 1u; break; } }
  }
  MEMBAR();
}
__device__ __forceinline__ void lds_signal(LAS unsigned char* L, int idx, int lane) {
  WAIT_LGKM0();
  if (lane == 0) __hip_atomic_fetch_add((LAS u32*)(L + L_FLG) + idx, 1u, __ATOMIC_RELAXED, __HIP_MEMORY_SCOPE_WORKGROUP);
}
__device__ __forceinline__ bool spin_ok(flagp F, u32& spins) {
  if (F[F_ABORT] != 0u) return false;
  if (++spins > GLB_SPIN_CAP) { F[F_ABORT] = 2u; return false; }
  return true;
}

constexpr int SC1 = 16;
typedef __amdgpu_buffer_rsrc_t rsrc_t;
__device__ __forceinline__ u32x4 ld128(rsrc_t r, int off) { return __builtin_amdgcn_raw_buffer_load_b128(r, off, 0, SC1); }
__device__ __forceinline__ u32x2 ld64(rsrc_t r, int off) { return __builtin_amdgcn_raw_buffer_load_b64(r, off, 0, SC1); }
__device__ __forceinline__ u32 ld32(rsrc_t r, int off) { return __builtin_amdgcn_raw_buffer_load_b32(r, off, 0, SC1); }
__device__ __forceinline__ void st128(rsrc_t r, int off, u32x4 v) { __builtin_amdgcn_raw_buffer_store_b128(v, r, off, 0, SC1); }
__device__ __forceinline__ void st64(rsrc_t r, int off, u32x2 v) { __builtin_amdgcn_raw_buffer_store_b64(v, r, off, 0, SC1); }

__device__ __forceinline__ u32x4 tr_frag(LAS unsigned char* L, u32 addr, u32 stride4) {
  const s16x4 a = __builtin_amdgcn_ds_read_tr16_b64_v4i16((LAS s16x4*)(L + addr));
  const s16x4 b = __builtin_amdgcn_ds_read_tr16_b64_v4i16((LAS s16x4*)(L + addr + stride4));
  const u32x2 a2 = __builtin_bit_cast(u32x2, a), b2 = __builtin_bit_cast(u32x2, b);
  return u32x4{a2.x, a2.y, b2.x, b2.y};
}
__device__ __forceinline__ u32x4 lds128(LAS unsigned char* L, u32 addr) { return *(LAS u32x4*)(L + addr); }

#define STAMP(i) do {} while (0)

__device__ __forceinline__ void ln_stats(const float (&v)[10], bool tail, float& mean_r, float& sd_r) {
  float s = 0.f;
#pragma unroll
  for (int i = 0; i < 8; ++i) s += v[i];
  if (tail) s += v[8] + v[9];
  const float mean = wave_sum(s) * (1.0f / DM);
  float q = 0.f;
#pragma unroll
  for (int i = 0; i < 8; ++i) { const float d = v[i] - mean; q += d * d; }
  if (tail) { const float d0 = v[8] - mean, d1 = v[9] - mean; q += d0 * d0 + d1 * d1; }
  const float var = wave_sum(q) * (1.0f / DM);
  mean_r = rb(mean);
  sd_r = rb(sqrtf(rb(rb(var) + 1e-5f)));
}
__device__ __forceinline__ float ln_mod(float x, float mean_r, float sd_r, float scale, float shift) {
  const float n = rb(rb(x - mean_r) / sd_r);
  return rb(rb(n * rb(1.0f + scale)) + shift);
}

__global__ void __launch_bounds__(NT, 2) fused_block(Params p) {
  extern __shared__ __attribute__((aligned(16))) unsigned char smem[];
  LAS unsigned char* L = (LAS unsigned char*)smem;
  flagp F = (flagp)(L + L_FLG);
  const int tid = threadIdx.x, lane = tid & 63, w = __builtin_amdgcn_readfirstlane(tid >> 6);
  const int c = blockIdx.x, xg = c & 7, ci = c >> 3;
  const int hkv = 2 * xg + (ci >> 4), cj = ci & 15, bb = cj >> 1, half = cj & 1;
  const bool isF = ci < 18, isO = ci >= 18 && ci < 27;
  const int hu = xg * 5 + (ci - 27);
  const bool isH = ci >= 27 && hu < 36;
  const int f = xg * 18 + ci;
  const int u = ci - 18;
  const int mu = xg * 9 + u;
  const u32 Wtot = isF ? 128u : isO ? 120u : isH ? 56u : 20u;
  const int roff = (isF || isO) ? 0 : 4;
  const int g = lane >> 4, r16 = lane & 15, qp = (lane & 15) >> 2, pp = lane & 3;
  const rsrc_t rs = __builtin_amdgcn_make_buffer_rsrc((void*)p.ws, 0, WS_END, 0x00020000);

  STAMP(0);
  if (tid < 32) F[tid] = 0u;
  __syncthreads();

  u32x4 wf[18];
#pragma unroll
  for (int i = 0; i < 18; ++i) wf[i] = u32x4{0u, 0u, 0u, 0u};
  u32 cbp[5] = {0, 0, 0, 0, 0}, sbp[5] = {0, 0, 0, 0, 0};
  u32x2 gap = {0, 0}, gbp = {0, 0}, xp = {0, 0};

  auto load_cbsb = [&]() {
    u32 spins = 0;
    for (;;) {
      MEMBAR();
      const int base = WS_MODT + w * 4608;
#pragma unroll
      for (int i = 0; i < 4; ++i) { sbp[i] = ld32(rs, base + (288 + lane + 64 * i) * 4); cbp[i] = ld32(rs, base + (576 + lane + 64 * i) * 4); }
      sbp[4] = ld32(rs, base + (288 + 256 + (lane & 31)) * 4); cbp[4] = ld32(rs, base + (576 + 256 + (lane & 31)) * 4);
      bool ok = true;
#pragma unroll
      for (int i = 0; i < 5; ++i) ok = ok && sbp[i] != SENT && cbp[i] != SENT;
      if (__all(ok) || !spin_ok(F, spins)) break;
    }
  };

  if (w == 0) {
    __builtin_amdgcn_s_setprio(3);
    u32 n = 0;
    auto after_piece = [&]() {
      ++n;
      if ((n & 3u) == 0u) {
        asm volatile("s_waitcnt vmcnt(%0)" :: "i"(INFL - 4) : "memory");
        if (n > (u32)(INFL - 4)) F[F_LANDED] = n - (u32)(INFL - 4);
      }
    };
    const u32x4 srd_q = mk_srd(p.wq + (hkv * 256 + cj * 16) * 2, DM * 8192 - (hkv * 256 + cj * 16) * 2);
    const u32 vq = (lane >> 1) * 8192 + (lane & 1) * 16;
    auto issue_wq = [&]() {
      for (int s = 0; s < 18; ++s) { DMA1(AUX_W, srd_q, vq, (u32)(s * 32 * 8192), (u32)(L_RING + n * 1024)); after_piece(); }
    };
    auto pad_to = [&](u32 tot) { while (n < tot) { DMA1(AUX_W, srd_q, vq, 0u, (u32)(L_RING + n * 1024)); after_piece(); } };
    if (isF) {
      issue_wq();
      const u32x4 srd_g = mk_srd(p.wg + f * 64, DM * 9216 - f * 64), srd_u = mk_srd(p.wu + f * 64, DM * 9216 - f * 64);
      const u32 vg = (lane >> 2) * 9216 + (lane & 3) * 16;
      for (int s = 0; s < 36; ++s) { DMA1(AUX_W, srd_g, vg, (u32)(s * 16 * 9216), (u32)(L_RING + n * 1024)); after_piece(); }
      for (int s = 0; s < 36; ++s) { DMA1(AUX_W, srd_u, vg, (u32)(s * 16 * 9216), (u32)(L_RING + n * 1024)); after_piece(); }
      const u32x4 srd_d = mk_srd(p.wd + f * 36864, 36864);
      const u32 vd = lane * 16;
      for (int s = 0; s < 36; ++s) { DMA1(AUX_W, srd_d, vd, (u32)(s * 1024), (u32)(L_RING + n * 1024)); after_piece(); }
    } else if (isO) {
      issue_wq();
      const u32x4 srd_m = mk_srd(p.wmod + (1152 + mu * 32) * 2, DM * 6912 - (1152 + mu * 32) * 2);
      const u32 vm = (lane >> 2) * 6912 + (lane & 3) * 16;
      for (int s = 0; s < 36; ++s) { DMA1(AUX_W, srd_m, vm, (u32)(s * 16 * 6912), (u32)(L_RING + n * 1024)); after_piece(); }
      const u32x4 srd_o = mk_srd(p.wo + xg * 512 * 1152 + u * 128, 512 * 1152 - u * 128);
      const u32 vo = (lane >> 3) * 1152 + (lane & 7) * 16;
      for (int s = 0; s < 64; ++s) { DMA1(AUX_W, srd_o, vo, (u32)(s * 8 * 1152), (u32)(L_RING + n * 1024)); after_piece(); }
    } else {
      if (isH) {
        const u32x4 srd_a = mk_srd(p.wmod + hu * 32, DM * 6912 - hu * 32), srd_c = mk_srd(p.wmod + 1152 + hu * 32, DM * 6912 - 1152 - hu * 32);
        const u32 vm = (lane >> 1) * 6912 + (lane & 1) * 16;
        for (int s = 0; s < 18; ++s) { DMA1(AUX_W, srd_a, vm, (u32)(s * 32 * 6912), (u32)(L_RING + n * 1024)); after_piece(); }
        for (int s = 0; s < 18; ++s) { DMA1(AUX_W, srd_c, vm, (u32)(s * 32 * 6912), (u32)(L_RING + n * 1024)); after_piece(); }
      }
      issue_wq();
    }
    pad_to(Wtot);
    STAMP(1);
    const size_t kvoff = ((size_t)(bb * HKV + hkv) * SK + (size_t)half * 4096) * 256;
    const u32x4 srd_k = mk_srd(p.kc + kvoff, 1u << 20), srd_v = mk_srd(p.vc + kvoff, 1u << 20);
    u32 vk[4], vv[2];
#pragma unroll
    for (int j = 0; j < 4; ++j) vk[j] = g * 256 + ((r16 ^ (4 * j + g)) << 4);
#pragma unroll
    for (int j = 0; j < 2; ++j) vv[j] = g * 256 + ((r16 ^ ((g | (j << 2)) << 1)) << 4);
    auto wait_free = [&](int q, int pos) {
      int idx; u32 need;
      if (q >= 8) { idx = F_FREE + pos; need = (u32)(q - 7); }
      else if (roff == 0) { idx = F_WREL + (pos < 3 ? 0 : pos < 5 ? 1 : 2); need = 7u; }
      else { if (pos >= 4) return; idx = F_WREL; need = 7u; }
      if (F[idx] < need) { WAIT_VM0(); F[F_LANDED] = n; lds_wait_ge(F, idx, need); }
    };
    for (int q = 0; q < 64; ++q) {
      const int pos = (q + roff) & 7;
      wait_free(q, pos);
#pragma unroll
      for (int gq = 0; gq < 4; ++gq) {
        DMA4(AUX_K, srd_k, vk[0], vk[1], vk[2], vk[3], (u32)(q * 16384 + gq * 4096), (u32)(L_RING + pos * 16384 + gq * 4096));
        n += 4;
        asm volatile("s_waitcnt vmcnt(%0)" :: "i"(INFL - 4) : "memory");
        F[F_LANDED] = n - (u32)(INFL - 4);
      }
    }
    STAMP(2);
    for (int q = 64; q < 128; ++q) {
      const int pos = (q + roff) & 7;
      wait_free(q, pos);
#pragma unroll
      for (int gq = 0; gq < 4; ++gq) {
        DMA4(AUX_V, srd_v, vv[0], vv[0], vv[1], vv[1], (u32)((q - 64) * 16384 + gq * 4096), (u32)(L_RING + pos * 16384 + gq * 4096));
        n += 4;
        asm volatile("s_waitcnt vmcnt(%0)" :: "i"(INFL - 4) : "memory");
        F[F_LANDED] = n - (u32)(INFL - 4);
      }
    }
    WAIT_VM0();
    F[F_LANDED] = n;
    STAMP(3);
    __builtin_amdgcn_s_setprio(0);
    if (isF) load_cbsb();
  } else if (w == 7) {
    const u64 epoch = __hip_atomic_load((GAS u64*)(p.ws + WS_CTRL), __ATOMIC_RELAXED, __HIP_MEMORY_SCOPE_AGENT) + 1ull;
    const u32x4 z4 = {0u, 0u, 0u, 0u};
    const u32x2 s2 = {SENT, SENT};
    st128(rs, WS_OP + c * 1024 + lane * 16, z4);
    if (lane == 0) st128(rs, WS_ST + c * 16, z4);
    if (r16 < 8) st64(rs, WS_Q + (hkv * 8 + r16) * 512 + cj * 32 + g * 8, s2);
    if (isH && r16 < 8) st64(rs, WS_H + r16 * 1152 + hu * 32 + g * 8, s2);
    if (isO) {
#pragma unroll
      for (int i = 0; i < 2; ++i) st128(rs, WS_WP + (xg * 8 + i * 4 + g) * 2304 + u * 256 + r16 * 16, z4);
      if (r16 < 8) { st64(rs, WS_MODT + r16 * 4608 + mu * 64 + g * 8, s2); st64(rs, WS_MODT + r16 * 4608 + mu * 64 + 32 + g * 8, s2); }
    }
    if (isF) {
      if (lane < 8) st64(rs, WS_X1 + (f / 18) * 1152 + (f % 18) * 64 + lane * 8, s2);
#pragma unroll
      for (int i = 0; i < 18; ++i) st128(rs, WS_FP + f * 18432 + i * 1024 + lane * 16, z4);
    }
    WAIT_VM0();
    F[F_INIT] = 1u;
    if (lane == 0) __hip_atomic_store((GAS u64*)(p.ws + WS_ALIVE) + c, epoch, __ATOMIC_RELAXED, __HIP_MEMORY_SCOPE_AGENT);
    STAMP(1);
    const u32 wq0 = isH ? 36u : 0u;
    lds_wait_ge(F, F_LANDED, wq0 + 18u);
    u32x4 qf[18];
    {
      const u32 a0 = L_RING + wq0 * 1024 + (8 * g + qp) * 32 + 8 * pp;
#pragma unroll
      for (int s = 0; s < 18; ++s) qf[s] = tr_frag(L, a0 + s * 1024, 4 * 32);
    }
    lds_signal(L, F_WREL + 0, lane);
    if (lane == 0) { __hip_atomic_fetch_add((LAS u32*)(L + L_FLG) + F_WREL + 1, 1u, __ATOMIC_RELAXED, __HIP_MEMORY_SCOPE_WORKGROUP);
                     __hip_atomic_fetch_add((LAS u32*)(L + L_FLG) + F_WREL + 2, 1u, __ATOMIC_RELAXED, __HIP_MEMORY_SCOPE_WORKGROUP); }
    {
      u32 spins = 0;
      const u32 elo = (u32)epoch, ehi = (u32)(epoch >> 32);
      for (;;) {
        MEMBAR();
        const u32x4 a = ld128(rs, WS_ALIVE + lane * 32), b2 = ld128(rs, WS_ALIVE + lane * 32 + 16);
        const bool ok = a.x == elo && a.y == ehi && a.z == elo && a.w == ehi && b2.x == elo && b2.y == ehi && b2.z == elo && b2.w == ehi;
        if (__all(ok) || !spin_ok(F, spins)) break;
      }
    }
    if (c == 0 && lane == 0) __hip_atomic_store((GAS u64*)(p.ws + WS_CTRL), epoch, __ATOMIC_RELAXED, __HIP_MEMORY_SCOPE_AGENT);
    STAMP(2);
    {
      u32x4 hv[9];
      u32 spins = 0;
      for (;;) {
        MEMBAR();
        bool ok = true;
#pragma unroll
        for (int i = 0; i < 9; ++i) hv[i] = ld128(rs, WS_H + (i * 64 + lane) * 16);
#pragma unroll
        for (int i = 0; i < 9; ++i) ok = ok && hv[i].x != SENT && hv[i].y != SENT && hv[i].z != SENT && hv[i].w != SENT;
        if (__all(ok) || !spin_ok(F, spins)) break;
      }
#pragma unroll
      for (int i = 0; i < 9; ++i) { const int e = i * 64 + lane, row = e / 72, col = e % 72; *(LAS u32x4*)(L + L_ACT + row * ACT_STRIDE + col * 16) = hv[i]; }
      WAIT_LGKM0();
    }
    STAMP(3);
    {
      f32x4 a0 = {0, 0, 0, 0}, a1 = {0, 0, 0, 0};
      const u32 ab = L_ACT + (lane & 7) * ACT_STRIDE + g * 16;
#pragma unroll
      for (int s = 0; s < 18; s += 2) { a0 = mfma(qf[s], lds128(L, ab + s * 64), a0); a1 = mfma(qf[s + 1], lds128(L, ab + (s + 1) * 64), a1); }
      const f32x4 a = a0 + a1;
      if (r16 < 8) st64(rs, WS_Q + (hkv * 8 + r16) * 512 + cj * 32 + g * 8, u32x2{nosent(pack2(a[0], a[1])), nosent(pack2(a[2], a[3]))});
    }
    STAMP(4);
    {
      u32x2 qv; u32 spins = 0;
      for (;;) {
        MEMBAR();
        qv = ld64(rs, WS_Q + (hkv * 8 + bb) * 512 + lane * 8);
        if (__all(qv.x != SENT && qv.y != SENT) || !spin_ok(F, spins)) break;
      }
      *(LAS u32x2*)(L + L_QS + lane * 8) = qv;
      WAIT_LGKM0();
      F[F_Q] = 1u;
    }
    STAMP(5);
    if (isF) {
      load_cbsb();
      const int br = f / 18, cu = f % 18, cg = lane & 7;
      u32 spins = 0;
      for (;;) {
        MEMBAR();
        gap = ld64(rs, WS_MODT + br * 4608 + (cu * 16 + cg * 2) * 4);
        gbp = ld64(rs, WS_MODT + br * 4608 + (864 + cu * 16 + cg * 2) * 4);
        if (__all(gap.x != SENT && gap.y != SENT && gbp.x != SENT && gbp.y != SENT) || !spin_ok(F, spins)) break;
      }
      xp = *(const GAS u32x2*)(p.x + br * DM + cu * 32 + cg * 4);
    }
  } else {
    const int cw = w - 1;
    if (isF) {
      const int m = cw & 1, kt = cw >> 1;
      const u32 a0 = L_RING + (18 + 36 * m) * 1024 + (kt * 192 + 8 * g + qp) * 64 + 8 * pp;
      if (m == 0) lds_wait_ge(F, F_LANDED, 54u);
      else { lds_signal(L, F_WREL + 0, lane); lds_wait_ge(F, F_LANDED, 90u); }
#pragma unroll
      for (int s = 0; s < 6; ++s) { wf[2 * s] = tr_frag(L, a0 + s * 2048, 4 * 64); wf[2 * s + 1] = tr_frag(L, a0 + s * 2048 + 32, 4 * 64); }
      if (m == 0) { lds_signal(L, F_WREL + 0, lane); lds_signal(L, F_WREL + 1, lane); } else lds_signal(L, F_WREL + 1, lane);
      lds_wait_ge(F, F_LANDED, 126u);
      const u32 d0 = L_RING + 90 * 1024 + (8 * g + qp) * 1152 + cw * 192 + 8 * pp;
#pragma unroll
      for (int t = 0; t < 6; ++t) wf[12 + t] = tr_frag(L, d0 + t * 32, 4 * 1152);
      lds_signal(L, F_WREL + 2, lane);
    } else if (isO || isH) {
      for (int e = cw * 64 + lane; e < 8 * 288; e += 384) {
        const u32 d = ((const GAS u32*)p.c)[e];
        const float a = bflo(d), b2 = bfhi(d);
        const float sa_ = a / (1.0f + __expf(-a)), sb_ = b2 / (1.0f + __expf(-b2));
        *(LAS u32*)(L + L_SC + (e / 288) * ACT_STRIDE + (e % 288) * 4) = pack2(sa_, sb_);
      }
      if (isH) {
        for (int row = cw; row < 8; row += 6) {
          float v[10];
          const GAS u32* xr = (const GAS u32*)p.x + row * 288;
#pragma unroll
          for (int i = 0; i < 4; ++i) { const u32 d = xr[lane + 64 * i]; v[2 * i] = bflo(d); v[2 * i + 1] = bfhi(d); }
          { const u32 d = xr[256 + (lane & 31)]; v[8] = bflo(d); v[9] = bfhi(d); }
          float mean_r, sd_r; ln_stats(v, lane < 32, mean_r, sd_r);
          if (lane == 0) { *(LAS float*)(L + L_STAT + row * 8) = mean_r; *(LAS float*)(L + L_STAT + row * 8 + 4) = sd_r; }
        }
      }
      lds_signal(L, F_SC, lane);
      const u32 sb0 = L_SC + (lane & 7) * ACT_STRIDE + g * 16;
      if (isH) {
        const int m = cw & 1, kt = cw >> 1;
        u32x2 xq = {0, 0};
        if (cw == 0) xq = *(const GAS u32x2*)(p.x + (lane & 7) * DM + hu * 16 + g * 4);
        lds_wait_ge(F, F_LANDED, 36u);
        lds_wait_ge(F, F_SC, 6u);
        const u32 a0 = L_RING + (m * 18 + kt * 6) * 1024 + (8 * g + qp) * 32 + 8 * pp;
        f32x4 acc = {0, 0, 0, 0};
#pragma unroll
        for (int s = 0; s < 6; ++s) acc = mfma(tr_frag(L, a0 + s * 1024, 4 * 32), lds128(L, sb0 + (kt * 6 + s) * 64), acc);
        if (r16 < 8) *(LAS f32x4*)(L + L_RED + cw * 512 + (g * 8 + r16) * 16) = acc;
        lds_signal(L, F_WREL + 0, lane);
        lds_signal(L, F_P1, lane);
        if (cw == 0) {
          lds_wait_ge(F, F_P1, 6u);
          const int ls = (g * 8 + (lane & 7)) * 16;
          const f32x4 sa = *(LAS f32x4*)(L + L_RED + 0 * 512 + ls) + *(LAS f32x4*)(L + L_RED + 2 * 512 + ls) + *(LAS f32x4*)(L + L_RED + 4 * 512 + ls);
          const f32x4 ca = *(LAS f32x4*)(L + L_RED + 1 * 512 + ls) + *(LAS f32x4*)(L + L_RED + 3 * 512 + ls) + *(LAS f32x4*)(L + L_RED + 5 * 512 + ls);
          const float mean_r = *(LAS float*)(L + L_STAT + (lane & 7) * 8), sd_r = *(LAS float*)(L + L_STAT + (lane & 7) * 8 + 4);
          const float h0 = ln_mod(bflo(xq.x), mean_r, sd_r, rb(ca[0]), rb(sa[0])), h1 = ln_mod(bfhi(xq.x), mean_r, sd_r, rb(ca[1]), rb(sa[1]));
          const float h2 = ln_mod(bflo(xq.y), mean_r, sd_r, rb(ca[2]), rb(sa[2])), h3 = ln_mod(bfhi(xq.y), mean_r, sd_r, rb(ca[3]), rb(sa[3]));
          lds_wait_ge(F, F_INIT, 1u);
          if (r16 < 8) st64(rs, WS_H + r16 * 1152 + hu * 32 + g * 8, u32x2{nosent(pack2(h0, h1)), nosent(pack2(h2, h3))});
          STAMP(6);
        }
        lds_signal(L, F_WREL + 1, lane); lds_signal(L, F_WREL + 2, lane);
      } else {
        if (cw >= 4) {
          const int ct = cw - 4;
          lds_wait_ge(F, F_LANDED, 54u);
          lds_wait_ge(F, F_SC, 6u);
          const u32 a0 = L_RING + 18 * 1024 + (8 * g + qp) * 64 + ct * 32 + 8 * pp;
          f32x4 a0c = {0, 0, 0, 0}, a1c = {0, 0, 0, 0};
#pragma unroll
          for (int s = 0; s < 18; s += 2) {
            a0c = mfma(tr_frag(L, a0 + s * 2048, 4 * 64), lds128(L, sb0 + s * 64), a0c);
            a1c = mfma(tr_frag(L, a0 + (s + 1) * 2048, 4 * 64), lds128(L, sb0 + (s + 1) * 64), a1c);
          }
          const f32x4 a = a0c + a1c;
          lds_signal(L, F_WREL + 0, lane);
          lds_wait_ge(F, F_INIT, 1u);
          if (r16 < 8) st64(rs, WS_MODT + r16 * 4608 + mu * 64 + ct * 32 + g * 8, u32x2{nosent(pack2(a[0], a[1])), nosent(pack2(a[2], a[3]))});
          lds_signal(L, F_WREL + 1, lane); lds_signal(L, F_WREL + 2, lane);
        } else {
          lds_signal(L, F_WREL + 0, lane);
          const u32 a0 = L_RING + 54 * 1024 + (8 * g + qp) * 128 + cw * 32 + 8 * pp;
          lds_wait_ge(F, F_LANDED, 86u);
#pragma unroll
          for (int s = 0; s < 8; ++s) wf[s] = tr_frag(L, a0 + s * 4096, 4 * 128);
          lds_signal(L, F_WREL + 1, lane);
          lds_wait_ge(F, F_LANDED, 118u);
#pragma unroll
          for (int s = 8; s < 16; ++s) wf[s] = tr_frag(L, a0 + s * 4096, 4 * 128);
          lds_signal(L, F_WREL + 2, lane);
        }
      }
    } else {
      lds_signal(L, F_WREL + 0, lane); lds_signal(L, F_WREL + 1, lane); lds_signal(L, F_WREL + 2, lane);
    }
    STAMP(1);
    lds_wait_ge(F, F_Q, 1u);
    if (isO) lds_wait_ge(F, F_WREL + 0, 7u);
    STAMP(2);
    u32x4 qf[4];
#pragma unroll
    for (int s = 0; s < 4; ++s) qf[s] = lds128(L, L_QS + (lane & 1) * 256 + s * 64 + g * 16);
    u32 ka[4];
#pragma unroll
    for (int s = 0; s < 4; ++s) ka[s] = r16 * 256 + (((4 * s + g) ^ r16) << 4);
    float mx0 = -INFINITY, mx1 = -INFINITY;
    for (int q = cw; q < 64; q += 6) {
      const int pos = (q + roff) & 7;
      lds_wait_ge(F, F_LANDED, Wtot + 16u * (u32)(q + 1));
      const u32 sb = L_RING + pos * 16384;
      u32x4 kf[16];
#pragma unroll
      for (int t = 0; t < 4; ++t)
#pragma unroll
        for (int s = 0; s < 4; ++s) kf[t * 4 + s] = lds128(L, sb + ka[s] + t * 4096);
      WAIT_LGKM0();
      if (lane == 0) F[F_FREE + pos] = (u32)(q + 1);
      f32x4 acc[4];
#pragma unroll
      for (int t = 0; t < 4; ++t) {
        acc[t] = f32x4{0, 0, 0, 0};
#pragma unroll
        for (int s = 0; s < 4; ++s) acc[t] = mfma(qf[s], kf[t * 4 + s], acc[t]);
      }
      float s0 = g == 0 ? acc[0][0] : g == 1 ? acc[1][0] : g == 2 ? acc[2][0] : acc[3][0];
      float s1 = g == 0 ? acc[0][1] : g == 1 ? acc[1][1] : g == 2 ? acc[2][1] : acc[3][1];
      s0 = rb(rb(s0) * 0.08838834764831845f);
      s1 = rb(rb(s1) * 0.08838834764831845f);
      mx0 = fmaxf(mx0, s0); mx1 = fmaxf(mx1, s1);
      *(LAS u16*)(L + L_SC + q * 256 + lane * 2) = (u16)(__float_as_uint(s0) >> 16);
      *(LAS u16*)(L + L_SC + q * 256 + 128 + lane * 2) = (u16)(__float_as_uint(s1) >> 16);
    }
    mx0 = wave_max(mx0); mx1 = wave_max(mx1);
    STAMP(3);
    if (isF) load_cbsb();
    f32x4 o[8];
#pragma unroll
    for (int cb = 0; cb < 8; ++cb) o[cb] = f32x4{0, 0, 0, 0};
    float l0 = 0.f, l1 = 0.f;
    const u32 va0 = (8 * g + qp) * 256 + ((pp >> 1) << 4) + 8 * (pp & 1) + (((qp | ((g & 1) << 2)) << 1) << 4);
    for (int q = cw; q < 64; q += 6) {
      const int pos = (q + 64 + roff) & 7;
      const float s0 = bf2f(*(LAS u16*)(L + L_SC + q * 256 + lane * 2)), s1 = bf2f(*(LAS u16*)(L + L_SC + q * 256 + 128 + lane * 2));
      const float p0 = __builtin_amdgcn_exp2f((s0 - mx0) * LOG2E), p1 = __builtin_amdgcn_exp2f((s1 - mx1) * LOG2E);
      l0 += p0; l1 += p1;
      *(LAS u16*)(L + L_SC + q * 256 + lane * 2) = (u16)f2bf(p0);
      *(LAS u16*)(L + L_SC + q * 256 + 128 + lane * 2) = (u16)f2bf(p1);
      WAIT_LGKM0();
      const u32x4 pf0 = lds128(L, L_SC + q * 256 + (lane & 1) * 128 + g * 16), pf1 = lds128(L, L_SC + q * 256 + (lane & 1) * 128 + 64 + g * 16);
      lds_wait_ge(F, F_LANDED, Wtot + 16u * (u32)(q + 65));
      const u32 sb = L_RING + pos * 16384 + va0;
      u32x4 vf[8];
#pragma unroll
      for (int cb = 0; cb < 8; ++cb) vf[cb] = tr_frag(L, sb ^ (cb << 5), 1024);
#pragma unroll
      for (int cb = 0; cb < 8; ++cb) o[cb] = mfma(pf0, vf[cb], o[cb]);
#pragma unroll
      for (int cb = 0; cb < 8; ++cb) vf[cb] = tr_frag(L, (sb ^ (cb << 5)) + 8192, 1024);
      WAIT_LGKM0();
      if (lane == 0) F[F_FREE + pos] = (u32)(q + 65);
#pragma unroll
      for (int cb = 0; cb < 8; ++cb) o[cb] = mfma(pf1, vf[cb], o[cb]);
    }
    l0 = wave_sum(l0); l1 = wave_sum(l1);
    STAMP(4);
    const u32 mb = L_ACT + cw * 1040;
    if (g == 0) {
#pragma unroll
      for (int cb = 0; cb < 8; ++cb) { *(LAS float*)(L + mb + (cb * 16 + r16) * 4) = o[cb][0]; *(LAS float*)(L + mb + 512 + (cb * 16 + r16) * 4) = o[cb][1]; }
      if (lane == 0) *(LAS f32x4*)(L + mb + 1024) = f32x4{mx0, l0, mx1, l1};
    }
  }
  __syncthreads();
  STAMP(8);
  if (w == 7) {
    const int head = lane >> 5;
    float mw[6], lw[6];
#pragma unroll
    for (int i = 0; i < 6; ++i) { mw[i] = *(LAS float*)(L + L_ACT + i * 1040 + 1024 + head * 8); lw[i] = *(LAS float*)(L + L_ACT + i * 1040 + 1028 + head * 8); }
    float M = mw[0];
#pragma unroll
    for (int i = 1; i < 6; ++i) M = fmaxf(M, mw[i]);
    f32x4 O = {0, 0, 0, 0}; float Ls = 0.f;
#pragma unroll
    for (int i = 0; i < 6; ++i) {
      const float e = __builtin_amdgcn_exp2f((mw[i] - M) * LOG2E);
      O += e * *(LAS f32x4*)(L + L_ACT + i * 1040 + lane * 16);
      Ls += e * lw[i];
    }
    st128(rs, WS_OP + c * 1024 + lane * 16, u32x4{vld(O[0]), vld(O[1]), vld(O[2]), vld(O[3])});
    if ((lane & 31) == 0) st64(rs, WS_ST + c * 16 + head * 8, u32x2{vld(M), vld(Ls)});
  }
  STAMP(9);
  if (isO) {
    constexpr int L_OS = L_RING;
    const int head = lane >> 5;
    u32x4 ov[4]; u32x2 sv[4];
    bool got[4] = {false, false, false, false};
    u32 spins = 0;
    for (;;) {
      MEMBAR();
#pragma unroll
      for (int i = 0; i < 4; ++i) if (!got[i]) {
        const int cp = xg + 8 * ((i >> 1) * 16 + 2 * w + (i & 1));
        ov[i] = ld128(rs, WS_OP + cp * 1024 + lane * 16);
        sv[i] = ld64(rs, WS_ST + cp * 16 + head * 8);
      }
      bool all = true;
#pragma unroll
      for (int i = 0; i < 4; ++i) if (!got[i]) {
        got[i] = __all((ov[i].x & ov[i].y & ov[i].z & ov[i].w & sv[i].x & sv[i].y & 1u) != 0u);
        all = all && got[i];
      }
      if (all || !spin_ok(F, spins)) break;
    }
#pragma unroll
    for (int hk = 0; hk < 2; ++hk) {
      const float m0 = __uint_as_float(sv[2 * hk].x), m1 = __uint_as_float(sv[2 * hk + 1].x);
      const float M = fmaxf(m0, m1);
      const float e0 = __builtin_amdgcn_exp2f((m0 - M) * LOG2E), e1 = __builtin_amdgcn_exp2f((m1 - M) * LOG2E);
      const float inv = 1.0f / (e0 * __uint_as_float(sv[2 * hk].y) + e1 * __uint_as_float(sv[2 * hk + 1].y));
      const u32x4 a = ov[2 * hk], b2 = ov[2 * hk + 1];
      const float o0 = (e0 * __uint_as_float(a.x) + e1 * __uint_as_float(b2.x)) * inv, o1 = (e0 * __uint_as_float(a.y) + e1 * __uint_as_float(b2.y)) * inv;
      const float o2 = (e0 * __uint_as_float(a.z) + e1 * __uint_as_float(b2.z)) * inv, o3 = (e0 * __uint_as_float(a.w) + e1 * __uint_as_float(b2.w)) * inv;
      *(LAS u32x2*)(L + L_OS + w * 1040 + hk * 512 + lane * 8) = u32x2{pack2(o0, o1), pack2(o2, o3)};
    }
    __syncthreads();
    STAMP(10);
    if (w >= 1 && w <= 4) {
      const int ct = w - 1;
      f32x4 a0 = {0, 0, 0, 0}, a1 = {0, 0, 0, 0};
      const u32 ob = L_OS + (lane & 7) * 1040 + g * 16;
#pragma unroll
      for (int s = 0; s < 16; s += 2) { a0 = mfma(wf[s], lds128(L, ob + s * 64), a0); a1 = mfma(wf[s + 1], lds128(L, ob + (s + 1) * 64), a1); }
      const f32x4 a = a0 + a1;
      if (r16 < 8) st128(rs, WS_WP + (xg * 8 + r16) * 2304 + (u * 64 + ct * 16 + g * 4) * 4, u32x4{vld(a[0]), vld(a[1]), vld(a[2]), vld(a[3])});
    }
    STAMP(11);
  } else if (isF) {
    const int br = f / 18, cu = f % 18;
    float x1r[4] = {0, 0, 0, 0};
    if (w == 7) {
      u32x4 pv; u32 spins = 0;
      for (;;) {
        MEMBAR();
        pv = ld128(rs, WS_WP + ((lane >> 3) * 8 + br) * 2304 + (cu * 32 + (lane & 7) * 4) * 4);
        if (__all((pv.x & pv.y & pv.z & pv.w & 1u) != 0u) || !spin_ok(F, spins)) break;
      }
      const float a0 = rb(sum_x8_16_32(__uint_as_float(pv.x))), a1 = rb(sum_x8_16_32(__uint_as_float(pv.y)));
      const float a2 = rb(sum_x8_16_32(__uint_as_float(pv.z))), a3 = rb(sum_x8_16_32(__uint_as_float(pv.w)));
      x1r[0] = rb(bflo(xp.x) + rb(bflo(gap.x) * a0)); x1r[1] = rb(bfhi(xp.x) + rb(bfhi(gap.x) * a1));
      x1r[2] = rb(bflo(xp.y) + rb(bflo(gap.y) * a2)); x1r[3] = rb(bfhi(xp.y) + rb(bfhi(gap.y) * a3));
      if (lane < 8) st64(rs, WS_X1 + br * 1152 + cu * 64 + lane * 8, u32x2{nosent(pack2(x1r[0], x1r[1])), nosent(pack2(x1r[2], x1r[3]))});
    }
    STAMP(10);
    {
      u32 xv[5]; u32 spins = 0;
      for (;;) {
        MEMBAR();
#pragma unroll
        for (int i = 0; i < 4; ++i) xv[i] = ld32(rs, WS_X1 + w * 1152 + (lane + 64 * i) * 4);
        xv[4] = ld32(rs, WS_X1 + w * 1152 + (256 + (lane & 31)) * 4);
        if (__all(xv[0] != SENT && xv[1] != SENT && xv[2] != SENT && xv[3] != SENT && xv[4] != SENT) || !spin_ok(F, spins)) break;
      }
      float v[10];
#pragma unroll
      for (int i = 0; i < 5; ++i) { v[2 * i] = bflo(xv[i]); v[2 * i + 1] = bfhi(xv[i]); }
      float mean_r, sd_r; ln_stats(v, lane < 32, mean_r, sd_r);
#pragma unroll
      for (int i = 0; i < 5; ++i) {
        const u32 hp = pack2(ln_mod(v[2 * i], mean_r, sd_r, bflo(cbp[i]), bflo(sbp[i])), ln_mod(v[2 * i + 1], mean_r, sd_r, bfhi(cbp[i]), bfhi(sbp[i])));
        if (i < 4) *(LAS u32*)(L + L_ACT + w * ACT_STRIDE + (lane + 64 * i) * 4) = hp;
        else if (lane < 32) *(LAS u32*)(L + L_ACT + w * ACT_STRIDE + (256 + lane) * 4) = hp;
      }
    }
    __syncthreads();
    STAMP(11);
    constexpr int L_R2 = L_RING;
    constexpr int L_HH = L_RING + 8192;
    constexpr int L_R3 = L_RING + 12288;
    if (w >= 1 && w <= 6) {
      const int cw = w - 1, kt = cw >> 1;
      f32x4 a0 = {0, 0, 0, 0}, a1 = {0, 0, 0, 0};
      const u32 hb = L_ACT + (lane & 7) * ACT_STRIDE + kt * 384 + g * 16;
#pragma unroll
      for (int s = 0; s < 6; ++s) { const u32x4 bfr = lds128(L, hb + s * 64); a0 = mfma(wf[2 * s], bfr, a0); a1 = mfma(wf[2 * s + 1], bfr, a1); }
      if (r16 < 8) { *(LAS f32x4*)(L + L_R2 + cw * 1024 + (g * 8 + r16) * 16) = a0; *(LAS f32x4*)(L + L_R2 + cw * 1024 + 512 + (g * 8 + r16) * 16) = a1; }
    }
    __syncthreads();
    if (tid < 256) {
      const int ct = tid >> 7, e = tid & 127;
      const u32 a = L_R2 + ct * 512 + e * 4;
      const float gg = rb(*(LAS float*)(L + a) + *(LAS float*)(L + a + 2048) + *(LAS float*)(L + a + 4096));
      const float uu = rb(*(LAS float*)(L + a + 1024) + *(LAS float*)(L + a + 3072) + *(LAS float*)(L + a + 5120));
      const float inner = 0.7978845608028654f * (gg + 0.044715f * gg * gg * gg);
      const float th = 1.0f - 2.0f / (1.0f + __expf(2.0f * inner));
      const float ge = rb(0.5f * gg * (1.0f + th));
      const int ls = e >> 2, rr = e & 3, b_ = ls & 7, g_ = ls >> 3;
      *(LAS u16*)(L + L_HH + b_ * 80 + (ct * 16 + g_ * 4 + rr) * 2) = (u16)f2bf(ge * uu);
    }
    __syncthreads();
    if (w >= 1 && w <= 6) {
      const int cw = w - 1;
      const u32x4 bfr = lds128(L, L_HH + (lane & 7) * 80 + g * 16);
#pragma unroll
      for (int t = 0; t < 6; ++t) {
        const f32x4 a = mfma(wf[12 + t], bfr, f32x4{0, 0, 0, 0});
        if (r16 < 8) st128(rs, WS_FP + f * 18432 + r16 * 2304 + ((cw * 6 + t) * 16 + g * 4) * 4, u32x4{vld(a[0]), vld(a[1]), vld(a[2]), vld(a[3])});
      }
    }
    STAMP(12);
    {
      const int p0 = tid >> 3, off = WS_FP + br * 2304 + (cu * 32 + (lane & 7) * 4) * 4;
      u32x4 e0, e1, e2 = {1u, 1u, 1u, 1u}; u32 spins = 0;
      for (;;) {
        MEMBAR();
        e0 = ld128(rs, off + p0 * 18432); e1 = ld128(rs, off + (p0 + 64) * 18432);
        if (w < 2) e2 = ld128(rs, off + (p0 + 128) * 18432);
        if (__all((e0.x & e0.y & e0.z & e0.w & e1.x & e1.y & e1.z & e1.w & e2.x & e2.y & e2.z & e2.w & 1u) != 0u) || !spin_ok(F, spins)) break;
      }
      f32x4 s = __builtin_bit_cast(f32x4, e0) + __builtin_bit_cast(f32x4, e1);
      if (w < 2) s += __builtin_bit_cast(f32x4, e2);
      s[0] = sum_x8_16_32(s[0]); s[1] = sum_x8_16_32(s[1]); s[2] = sum_x8_16_32(s[2]); s[3] = sum_x8_16_32(s[3]);
      if (lane < 8) *(LAS f32x4*)(L + L_R3 + w * 128 + lane * 16) = s;
    }
    __syncthreads();
    if (w == 7 && lane < 8) {
      f32x4 s = {0, 0, 0, 0};
#pragma unroll
      for (int i = 0; i < 8; ++i) s += *(LAS f32x4*)(L + L_R3 + i * 128 + lane * 16);
      const float o0 = x1r[0] + rb(bflo(gbp.x) * rb(s[0])), o1 = x1r[1] + rb(bfhi(gbp.x) * rb(s[1]));
      const float o2 = x1r[2] + rb(bflo(gbp.y) * rb(s[2])), o3 = x1r[3] + rb(bfhi(gbp.y) * rb(s[3]));
      *(GAS u32x2*)(p.out + br * DM + cu * 32 + lane * 4) = u32x2{pack2(o0, o1), pack2(o2, o3)};
    }
    STAMP(13);
  }
}

}

extern "C" void kernel_launch(void* const* d_in, const int* in_sizes, int n_in, void* d_out, int out_size, void* d_ws, size_t ws_size,
                              hipStream_t stream) {
  (void)in_sizes; (void)n_in; (void)out_size; (void)ws_size;
  Params p;
  p.x = (const u16*)d_in[0];
  p.kc = (const unsigned char*)d_in[1];
  p.vc = (const unsigned char*)d_in[2];
  p.wq = (const unsigned char*)d_in[3];
  p.wo = (const unsigned char*)d_in[4];
  p.wg = (const unsigned char*)d_in[6];
  p.wu = (const unsigned char*)d_in[7];
  p.wd = (const unsigned char*)d_in[8];
  p.c = (const u16*)d_in[9];
  p.wmod = (const unsigned char*)d_in[10];
  p.out = (u16*)d_out;
  p.ws = (unsigned char*)d_ws;
  (void)hipFuncSetAttribute((const void*)fused_block, hipFuncAttributeMaxDynamicSharedMemorySize, LDS_BYTES);
  hipLaunchKernelGGL(fused_block, dim3(NCU), dim3(NT), LDS_BYTES, stream, p);
}
```

```cpp
#include <hip/hip_runtime.h>
#include <stdint.h>
#include <type_traits>
#pragma clang fp contract(off)

#define INFL 24
#define INFLW 256
#define HPOLL_SLEEP 2
#define QPOLL_SLEEP 2
#define AUX_K "nt"
#define AUX_V "nt"
#define AUX_W "nt"
#define STAMPS 0

#define LAS __attribute__((address_space(3)))
#define GAS __attribute__((address_space(1)))
typedef unsigned u32;
typedef unsigned long long u64;
typedef unsigned short u16;
typedef u32 u32x4 __attribute__((ext_vector_type(4)));
typedef u32 u32x2 __attribute__((ext_vector_type(2)));
typedef float f32x4 __attribute__((ext_vector_type(4)));
typedef __bf16 bf16x8 __attribute__((ext_vector_type(8)));
typedef short s16x4 __attribute__((ext_vector_type(4)));

namespace {

constexpr int NB = 8, DM = 576, HKV = 16, SK = 8192, DH = 128, DF = 4608;
constexpr int NCU = 256, NT = 512;

constexpr int REP_H = 8, REP_X = 16;
constexpr int WS_CTRL = 0;
constexpr int WS_ALIVE = 4096;
constexpr int WS_ST = 8192;
constexpr int WS_MODT = WS_ST + 4096;
constexpr int WS_X1 = WS_MODT + 36864;
constexpr int WS_H = WS_X1 + REP_X * 9216;
constexpr int WS_Q = WS_H + REP_H * 18432;
constexpr int WS_OP = WS_Q + 131072;
constexpr int WS_WP = WS_OP + 262144;
constexpr int WS_FP = WS_WP + 147456;
constexpr int WS_END = WS_FP + 144 * 18432;
constexpr int WS_DBG = 4 << 20;
static_assert(WS_END <= WS_DBG, "ws");

constexpr int L_RING = 0;
constexpr int L_SC = 131072;
constexpr int L_ACT = L_SC + 16384;
constexpr int L_QS = L_ACT + 9344;
constexpr int L_RED = L_QS + 512;
constexpr int L_STAT = L_RED + 3072;
constexpr int L_MX = L_STAT + 64;
constexpr int L_LW = L_MX + 64;
constexpr int L_FLG = L_LW + 64;
constexpr int L_P2 = L_SC + 8192;
constexpr int LDS_BYTES = 163840;
static_assert(L_FLG + 256 <= LDS_BYTES, "lds");
constexpr int ACT_STRIDE = 1168;
constexpr int F_LANDED = 0, F_FREE = 1  , F_WREL = 9  , F_SC = 12, F_P1 = 13, F_INIT = 14, F_Q = 15, F_ABORT = 16, F_ALIVE = 17, F_P2 = 18, F_MX = 19, F_L = 20;

constexpr u32 SENT = 0xFFFFFFFFu;
constexpr u32 LDS_SPIN_CAP = 1u << 21;
constexpr u32 GLB_SPIN_CAP = 1u << 17;
constexpr float LOG2E = 1.4426950408889634f;

struct Params {
  const u16* x; const unsigned char* kc; const unsigned char* vc; const unsigned char* wq; const unsigned char* wo;
  const unsigned char* wg; const unsigned char* wu; const unsigned char* wd; const u16* c; const unsigned char* wmod;
  u16* out; unsigned char* ws;
};

__device__ __forceinline__ float bf2f(u32 v16) { return __uint_as_float(v16 << 16); }
__device__ __forceinline__ float bflo(u32 d) { return __uint_as_float(d << 16); }
__device__ __forceinline__ float bfhi(u32 d) { return __uint_as_float(d & 0xffff0000u); }
__device__ __forceinline__ float rb(float f) { return (float)(__bf16)f; }
__device__ __forceinline__ u32 f2bf(float f) { return (u32)__builtin_bit_cast(u16, (__bf16)f); }
__device__ __forceinline__ u32 pack2(float lo, float hi) { return f2bf(lo) | (f2bf(hi) << 16); }
__device__ __forceinline__ u32 nosent(u32 p) { return p == SENT ? 0xFFFEFFFFu : p; }
__device__ __forceinline__ u32 vld(float f) { return __float_as_uint(f) | 1u; }
__device__ __forceinline__ f32x4 mfma(u32x4 a, u32x4 b, f32x4 c) {
  return __builtin_amdgcn_mfma_f32_16x16x32_bf16(__builtin_bit_cast(bf16x8, a), __builtin_bit_cast(bf16x8, b), c, 0, 0, 0);
}
__device__ __forceinline__ u32x4 mk_srd(const void* p, u32 bytes) {
  const u64 a = (u64)p;
  u32x4 s;
  s.x = __builtin_amdgcn_readfirstlane((u32)a);
  s.y = __builtin_amdgcn_readfirstlane((u32)(a >> 32) & 0xffffu);
  s.z = __builtin_amdgcn_readfirstlane(bytes);
  s.w = 0x00020000u;
  return s;
}
#define MEMBAR() asm volatile("" ::: "memory")
#define WAIT_LGKM0() asm volatile("s_waitcnt lgkmcnt(0)" ::: "memory")
#define WAIT_VM0() asm volatile("s_waitcnt vmcnt(0)" ::: "memory")
#define BARRIER() do { asm volatile("s_waitcnt lgkmcnt(0)" ::: "memory"); __builtin_amdgcn_s_barrier(); asm volatile("" ::: "memory"); } while (0)

#define DMA1(AUX, srd, voff, soff, ldsaddr)                                                                      \
  do { u32 keep_;                                                                                                \
    asm volatile("s_mov_b32 %0, m0\n\ts_mov_b32 m0, %1\n\ts_nop 0\n\t"                                           \
                 "buffer_load_dwordx4 %4, %2, %3 offen " AUX " lds\n\ts_mov_b32 m0, %0"                          \
                 : "=&s"(keep_) : "s"(ldsaddr), "s"(srd), "s"(soff), "v"(voff) : "memory"); } while (0)
#define DMA4(AUX, srd, v0, v1, v2, v3, soff, ldsaddr)                                                            \
  do { u32 keep_;                                                                                                \
    asm volatile("s_mov_b32 %0, m0\n\ts_mov_b32 m0, %1\n\ts_nop 0\n\t"                                           \
                 "buffer_load_dwordx4 %4, %2, %3 offen " AUX " lds\n\t"                                          \
                 "buffer_load_dwordx4 %5, %2, %3 offen offset:1024 " AUX " lds\n\t"                              \
                 "buffer_load_dwordx4 %6, %2, %3 offen offset:2048 " AUX " lds\n\t"                              \
                 "buffer_load_dwordx4 %7, %2, %3 offen offset:3072 " AUX " lds\n\ts_mov_b32 m0, %0"              \
                 : "=&s"(keep_) : "s"(ldsaddr), "s"(srd), "s"(soff), "v"(v0), "v"(v1), "v"(v2), "v"(v3) : "memory"); } while (0)

template <int CTRL> __device__ __forceinline__ float dpp(float x) {
  return __builtin_bit_cast(float, __builtin_amdgcn_mov_dpp(__builtin_bit_cast(int, x), CTRL, 0xf, 0xf, true));
}
constexpr int ROR8 = 0x128, ROR4 = 0x124, ROR2 = 0x122, ROR1 = 0x121;
__device__ __forceinline__ float xrow_max(float x) {
  auto s = __builtin_amdgcn_permlane16_swap(__float_as_uint(x), __float_as_uint(x), false, false);
  x = fmaxf(__uint_as_float(s[0]), __uint_as_float(s[1]));
  auto t = __builtin_amdgcn_permlane32_swap(__float_as_uint(x), __float_as_uint(x), false, false);
  return fmaxf(__uint_as_float(t[0]), __uint_as_float(t[1]));
}
__device__ __forceinline__ float xrow_sum(float x) {
  auto s = __builtin_amdgcn_permlane16_swap(__float_as_uint(x), __float_as_uint(x), false, false);
  x = __uint_as_float(s[0]) + __uint_as_float(s[1]);
  auto t = __builtin_amdgcn_permlane32_swap(__float_as_uint(x), __float_as_uint(x), false, false);
  return __uint_as_float(t[0]) + __uint_as_float(t[1]);
}
__device__ __forceinline__ float wave_max(float x) {
  x = fmaxf(x, dpp<ROR8>(x)); x = fmaxf(x, dpp<ROR4>(x)); x = fmaxf(x, dpp<ROR2>(x)); x = fmaxf(x, dpp<ROR1>(x));
  return xrow_max(x);
}
__device__ __forceinline__ float wave_sum(float x) {
  x += dpp<ROR8>(x); x += dpp<ROR4>(x); x += dpp<ROR2>(x); x += dpp<ROR1>(x);
  return xrow_sum(x);
}
__device__ __forceinline__ float sum_x8_16_32(float x) {
  x += dpp<ROR8>(x);
  return xrow_sum(x);
}

typedef volatile LAS u32* flagp;
__device__ __forceinline__ void lds_wait_ge(flagp F, int idx, u32 need) {
  u32 spins = 0;
  while (F[idx] < need) {
    __builtin_amdgcn_s_sleep(1);
    if ((++spins & 63u) == 0u) { if (F[F_ABORT] != 0u) break; if (spins > LDS_SPIN_CAP) { F[F_ABORT] = 1u; break; } }
  }
  MEMBAR();
}
__device__ __forceinline__ void lds_add(int idx, u32 n) {
  const u32 a = L_FLG + idx * 4;
  asm volatile("s_waitcnt lgkmcnt(0)\n\ts_mov_b64 vcc, exec\n\ts_mov_b64 exec, 1\n\tds_add_u32 %0, %1\n\ts_mov_b64 exec, vcc"
               :: "v"(a), "v"(n) : "vcc", "memory");
}
__device__ __forceinline__ bool spin_ok(flagp F, u32& spins) {
  if (++spins > GLB_SPIN_CAP) { F[F_ABORT] = 2u; return false; }
  if ((spins & 255u) == 0u && F[F_ABORT] != 0u) return false;
  return true;
}

constexpr int SC1 = 16;
typedef __amdgpu_buffer_rsrc_t rsrc_t;
__device__ __forceinline__ u32x4 ld128(rsrc_t r, int off) { return __builtin_amdgcn_raw_buffer_load_b128(r, off, 0, SC1); }
__device__ __forceinline__ u32x2 ld64(rsrc_t r, int off) { return __builtin_amdgcn_raw_buffer_load_b64(r, off, 0, SC1); }
__device__ __forceinline__ u32 ld32(rsrc_t r, int off) { return __builtin_amdgcn_raw_buffer_load_b32(r, off, 0, SC1); }
__device__ __forceinline__ void st128(rsrc_t r, int off, u32x4 v) { __builtin_amdgcn_raw_buffer_store_b128(v, r, off, 0, SC1); }
__device__ __forceinline__ void st64(rsrc_t r, int off, u32x2 v) { __builtin_amdgcn_raw_buffer_store_b64(v, r, off, 0, SC1); }

__device__ __forceinline__ u32x4 tr_frag(LAS unsigned char* L, u32 addr, u32 stride4) {
  const s16x4 a = __builtin_amdgcn_ds_read_tr16_b64_v4i16((LAS s16x4*)(L + addr));
  const s16x4 b = __builtin_amdgcn_ds_read_tr16_b64_v4i16((LAS s16x4*)(L + addr + stride4));
  const u32x2 a2 = __builtin_bit_cast(u32x2, a), b2 = __builtin_bit_cast(u32x2, b);
  return u32x4{a2.x, a2.y, b2.x, b2.y};
}
__device__ __forceinline__ u32x4 lds128(LAS unsigned char* L, u32 addr) { return *(LAS u32x4*)(L + addr); }

#define STAMP(i) do {} while (0)

__device__ __forceinline__ void ln_stats(const float (&v)[10], bool tail, float& mean_r, float& sd_r) {
  float s = 0.f;
#pragma unroll
  for (int i = 0; i < 8; ++i) s += v[i];
  if (tail) s += v[8] + v[9];
  const float mean = wave_sum(s) * (1.0f / DM);
  float q = 0.f;
#pragma unroll
  for (int i = 0; i < 8; ++i) { const float d = v[i] - mean; q += d * d; }
  if (tail) { const float d0 = v[8] - mean, d1 = v[9] - mean; q += d0 * d0 + d1 * d1; }
  const float var = wave_sum(q) * (1.0f / DM);
  mean_r = rb(mean);
  sd_r = rb(sqrtf(rb(rb(var) + 1e-5f)));
}
__device__ __forceinline__ float ln_mod(float x, float mean_r, float sd_r, float scale, float shift) {
  const float n = rb(rb(x - mean_r) / sd_r);
  return rb(rb(n * rb(1.0f + scale)) + shift);
}

__global__ void __launch_bounds__(NT, 2) fused_block(Params p) {
  extern __shared__ __attribute__((aligned(16))) unsigned char smem[];
  LAS unsigned char* L = (LAS unsigned char*)smem;
  flagp F = (flagp)(L + L_FLG);
  const int tid = threadIdx.x, lane = tid & 63, w = __builtin_amdgcn_readfirstlane(tid >> 6);
  STAMP(0);
  const int c = blockIdx.x, xg = c & 7, ci = c >> 3;
  const int hkv = 2 * xg + (ci >> 4), cj = ci & 15, bb = cj >> 1, half = cj & 1;
  const bool isF = ci < 18, isO = ci >= 18 && ci < 27;
  const int hu = xg * 5 + (ci - 27);
  const bool isH = ci >= 27 && hu < 36;
  const int f = xg * 18 + ci;
  const int u = ci - 18;
  const bool isQ = isO && u < 8;
  const u32 Wtot = isF ? 108u : isO ? 136u : isH ? 108u : 0u;
  const int roff = (isF || isH) ? 7 : 0;
  const int g = lane >> 4, r16 = lane & 15, qp = (lane & 15) >> 2, pp = lane & 3;
  const rsrc_t rs = __builtin_amdgcn_make_buffer_rsrc((void*)p.ws, 0, WS_END, 0x00020000);

  auto read_epoch = [&]() {
    u64 e = *(const __attribute__((address_space(4))) u64*)(p.ws + WS_CTRL) + 1ull;
    asm volatile("" : "+s"(e));
    return e;
  };

  u32x4 wf[18];
  u32 cbp[5], sbp[5];
  u32x2 gap, gbp, xp;
  float osA = 0.f, osB = 0.f;

  auto load_cbsb = [&]() {
    u32 spins = 0;
    for (;;) {
      MEMBAR();
      const int base = WS_MODT + w * 4608;
#pragma unroll
      for (int i = 0; i < 4; ++i) { sbp[i] = ld32(rs, base + (288 + lane + 64 * i) * 4); cbp[i] = ld32(rs, base + (576 + lane + 64 * i) * 4); }
      sbp[4] = ld32(rs, base + (288 + 256 + (lane & 31)) * 4); cbp[4] = ld32(rs, base + (576 + 256 + (lane & 31)) * 4);
      bool ok = true;
#pragma unroll
      for (int i = 0; i < 5; ++i) ok = ok && sbp[i] != SENT && cbp[i] != SENT;
      if (__all(ok) || !spin_ok(F, spins)) break;
    }
#pragma unroll
    for (int i = 0; i < 5; ++i) cbp[i] = pack2(1.0f + bflo(cbp[i]), 1.0f + bfhi(cbp[i]));
  };
  auto q_partial = [&](int kt, u32 tag, f32x4 (&acc)[4]) {
    lds_wait_ge(F, F_LANDED, 72u);
    u32x4 pq[24];
    const u32 a0 = L_RING + (kt * 192 + 8 * g + qp) * 128 + 8 * pp;
#pragma unroll
    for (int s = 0; s < 6; ++s)
#pragma unroll
      for (int ct = 0; ct < 4; ++ct) pq[s * 4 + ct] = tr_frag(L, a0 + s * 4096 + ct * 32, 4 * 128);
    lds_add(F_WREL + 0, 1);
    STAMP(6);
    u32x4 hv[6];
    u32 spins = 0;
    const int hb = WS_H + (c % REP_H) * 18432 + kt * 768;
    int go[6], lo[6];
#pragma unroll
    for (int i = 0; i < 6; ++i) { const int e = i * 64 + lane, row = e / 48, j = e % 48; go[i] = hb + row * 2304 + j * 16; lo[i] = L_ACT + row * ACT_STRIDE + kt * 384 + j * 8; }
    for (;;) {
      MEMBAR();
#pragma unroll
      for (int i = 0; i < 6; ++i) hv[i] = ld128(rs, go[i]);
      bool ok = true;
#pragma unroll
      for (int i = 0; i < 6; ++i) ok = ok && hv[i].y == tag && hv[i].w == tag;
      if (__all(ok) || !spin_ok(F, spins)) break;
      __builtin_amdgcn_s_sleep(HPOLL_SLEEP);
    }
#pragma unroll
    for (int i = 0; i < 6; ++i) *(LAS u32x2*)(L + lo[i]) = u32x2{hv[i].x, hv[i].z};
    WAIT_LGKM0();
    STAMP(3);
    const u32 ab = L_ACT + (lane & 7) * ACT_STRIDE + kt * 384 + g * 16;
#pragma unroll
    for (int ct = 0; ct < 4; ++ct) acc[ct] = f32x4{0, 0, 0, 0};
#pragma unroll
    for (int s = 0; s < 6; ++s) {
      const u32x4 bfr = lds128(L, ab + s * 64);
#pragma unroll
      for (int ct = 0; ct < 4; ++ct) acc[ct] = mfma(pq[s * 4 + ct], bfr, acc[ct]);
    }
  };

  if (w == 0) {
    __builtin_amdgcn_s_setprio(3);
    if (lane < 32) F[lane] = 0u;
    u32 n = 0;
    auto run = [&](auto inflp, const u32x4& srd, u32 voff, u32 step, u32 first, u32 cnt) {
      constexpr int NP = decltype(inflp)::value;
#pragma unroll 1
      for (u32 s = first; s < cnt; ++s) {
        DMA1(AUX_W, srd, voff, s * step, (u32)(L_RING + n * 1024));
        ++n;
        asm volatile("s_waitcnt vmcnt(%0)" :: "i"(NP - 1) : "memory");
        if (n > (u32)(NP - 1)) F[F_LANDED] = n - (u32)(NP - 1);
      }
    };
    typedef std::integral_constant<int, INFLW / 32> P32;
    typedef std::integral_constant<int, INFLW / 16> P16;
    typedef std::integral_constant<int, (INFLW / 8 > 32 ? 32 : INFLW / 8)> P8;
    const u32 vg = (lane >> 2) * 9216 + (lane & 3) * 16, vm32 = (lane >> 1) * 6912 + (lane & 1) * 16;
    const u32 v128o = (lane >> 3) * 1152 + (lane & 7) * 16;
    u32x4 srd0; u32 v0, step0, cnt0;
    if (isF) { srd0 = mk_srd(p.wg + f * 64, DM * 9216 - f * 64); v0 = vg; step0 = 16 * 9216; cnt0 = 36; }
    else if (isQ) { srd0 = mk_srd(p.wq + (xg * 512 + u * 64) * 2, DM * 8192 - (xg * 512 + u * 64) * 2); v0 = (lane >> 3) * 8192 + (lane & 7) * 16; step0 = 8 * 8192; cnt0 = 72; }
    else if (isH) { srd0 = mk_srd(p.wmod + hu * 32, DM * 6912 - hu * 32); v0 = vm32; step0 = 32 * 6912; cnt0 = 18; }
    else { srd0 = mk_srd(p.wq, 0); v0 = 0; step0 = 0; cnt0 = 0; }
    const u32 pre = cnt0 < (u32)P32::value ? cnt0 : (u32)P32::value;
#pragma unroll 1
    for (u32 s = 0; s < pre; ++s) { DMA1(AUX_W, srd0, v0, s * step0, (u32)(L_RING + n * 1024)); ++n; }
    BARRIER();
    STAMP(4);
    if (isF) {
      run(P16(), srd0, v0, step0, pre, 36);
      run(P16(), mk_srd(p.wu + f * 64, DM * 9216 - f * 64), vg, 16 * 9216, 0, 36);
      run(P8(), mk_srd(p.wd + f * 36864, 36864), lane * 16, 1024, 0, 36);
    } else if (isO) {
      if (isQ) run(P8(), srd0, v0, step0, pre, 72); else n = 72;
      run(P8(), mk_srd(p.wo + xg * 512 * 1152 + u * 128, 512 * 1152 - u * 128), v128o, 8 * 1152, 0, 64);
    } else if (isH) {
      run(P32(), srd0, v0, step0, pre, 18);
      run(P32(), mk_srd(p.wmod + 1152 + hu * 32, DM * 6912 - 1152 - hu * 32), vm32, 32 * 6912, 0, 18);
      run(P8(), mk_srd(p.wmod + 2304 + hu * 128, DM * 6912 - 2304 - hu * 128), (lane >> 3) * 6912 + (lane & 7) * 16, 8 * 6912, 0, 72);
    }
    STAMP(1);
    const size_t kvoff = ((size_t)(bb * HKV + hkv) * SK + (size_t)half * 4096) * 256;
    const u32x4 srd_k = mk_srd(p.kc + kvoff, 1u << 20), srd_v = mk_srd(p.vc + kvoff, 1u << 20);
    u32 vk[4], vv[2];
#pragma unroll
    for (int j = 0; j < 4; ++j) vk[j] = g * 256 + ((r16 ^ (4 * j + g)) << 4);
#pragma unroll
    for (int j = 0; j < 2; ++j) vv[j] = g * 256 + ((r16 ^ ((g | (j << 2)) << 1)) << 4);
    const u32 wneed = isF ? 0x03332211u : isO ? 0x33221111u : isH ? 0x02222211u : 0u;
    auto wait_free = [&](int q, int pos) {
      if (q >= 8) {
        if (F[F_FREE + pos] < (u32)(q - 7)) {
          WAIT_VM0(); F[F_LANDED] = n; lds_wait_ge(F, F_FREE + pos, (u32)(q - 7));
        }
      } else {
        const int nb = (wneed >> (4 * pos)) & 15;
#pragma unroll 1
        for (int i = 0; i < nb; ++i)
          if (F[F_WREL + i] < 7u) { WAIT_VM0(); F[F_LANDED] = n; lds_wait_ge(F, F_WREL + i, 7u); }
      }
    };
#pragma unroll 1
    for (int q = 0; q < 64; ++q) {
      const int pos = (q + roff) & 7;
      wait_free(q, pos);
#pragma unroll
      for (int gq = 0; gq < 4; ++gq) {
        DMA4(AUX_K, srd_k, vk[0], vk[1], vk[2], vk[3], (u32)(q * 16384 + gq * 4096), (u32)(L_RING + pos * 16384 + gq * 4096));
        n += 4;
        asm volatile("s_waitcnt vmcnt(%0)" :: "i"(INFL - 4) : "memory");
        if (n > (u32)(INFL - 4)) F[F_LANDED] = n - (u32)(INFL - 4);
      }
    }
    STAMP(2);
#pragma unroll 1
    for (int q = 64; q < 128; ++q) {
      const int pos = (q + roff) & 7;
      wait_free(q, pos);
#pragma unroll
      for (int gq = 0; gq < 4; ++gq) {
        DMA4(AUX_V, srd_v, vv[0], vv[0], vv[1], vv[1], (u32)((q - 64) * 16384 + gq * 4096), (u32)(L_RING + pos * 16384 + gq * 4096));
        n += 4;
        asm volatile("s_waitcnt vmcnt(%0)" :: "i"(INFL - 4) : "memory");
        F[F_LANDED] = n - (u32)(INFL - 4);
      }
    }
    WAIT_VM0();
    F[F_LANDED] = n;
    STAMP(3);
    __builtin_amdgcn_s_setprio(0);
    if (isF) { lds_wait_ge(F, F_ALIVE, 1u); load_cbsb(); }
  } else if (w == 7) {
    const u64 epoch = read_epoch();
    const u32 tag = (u32)epoch;
    const u32x4 z4 = {0u, 0u, 0u, 0u};
    const u32x2 s2 = {SENT, SENT};
    if (isH) {
#pragma unroll
      for (int i = 0; i < 2; ++i) st64(rs, WS_MODT + (lane & 7) * 4608 + hu * 128 + (2 * i + (r16 >> 3)) * 32 + g * 8, s2);
    }
    BARRIER();
    st128(rs, WS_OP + c * 1024 + lane * 16, z4);
    if (lane == 0) st128(rs, WS_ST + c * 16, z4);
    if (isO) {
#pragma unroll
      for (int i = 0; i < 2; ++i) st128(rs, WS_WP + (xg * 144 + u * 16) * 128 + i * 1024 + lane * 16, z4);
    }
    if (isF) {
#pragma unroll
      for (int i = 0; i < REP_X / 8; ++i) st64(rs, WS_X1 + (i * 8 + (lane >> 3)) * 9216 + f * 64 + (lane & 7) * 8, s2);
#pragma unroll 1
      for (int i = 0; i < 18; ++i) st128(rs, WS_FP + f * 18432 + i * 1024 + lane * 16, z4);
    }
    WAIT_VM0();
    F[F_INIT] = 1u;
    if (lane == 0) __hip_atomic_store((GAS u64*)(p.ws + WS_ALIVE) + c, epoch, __ATOMIC_RELAXED, __HIP_MEMORY_SCOPE_AGENT);
    STAMP(1);
    if (isQ) {
      f32x4 acc[4];
      q_partial(2, tag, acc);
      lds_add(F_WREL + 1, 1); lds_add(F_WREL + 2, 1);
      lds_wait_ge(F, F_P2, 2u);
      const int ls = (g * 8 + (lane & 7)) * 16;
#pragma unroll
      for (int ct = 0; ct < 4; ++ct) {
        const f32x4 a = (*(LAS f32x4*)(L + L_P2 + ct * 512 + ls) + *(LAS f32x4*)(L + L_P2 + 2048 + ct * 512 + ls)) + acc[ct];
        if (r16 < 8) st128(rs, WS_Q + ((2 * xg + (u >> 2)) * 8 + r16) * 1024 + ((u & 3) * 16 + ct * 4 + g) * 16, u32x4{pack2(a[0], a[1]), tag, pack2(a[2], a[3]), tag});
      }
      STAMP(4);
    } else {
      lds_add(F_WREL + 0, 1); lds_add(F_WREL + 1, 1); lds_add(F_WREL + 2, 1);
    }
    {
      u32x4 qv; u32 spins = 0;
      for (;;) {
        MEMBAR();
        qv = ld128(rs, WS_Q + (hkv * 8 + bb) * 1024 + lane * 16);
        if (__all(qv.y == tag && qv.w == tag) || !spin_ok(F, spins)) break;
        __builtin_amdgcn_s_sleep(QPOLL_SLEEP);
      }
      *(LAS u32x2*)(L + L_QS + lane * 8) = u32x2{qv.x, qv.z};
      WAIT_LGKM0();
      F[F_Q] = 1u;
    }
    STAMP(5);
    lds_wait_ge(F, F_LANDED, Wtot + 16u * 56u);
    {
      const u32 ehi = (u32)(epoch >> 32);
      u32 spins = 0;
      for (;;) {
        MEMBAR();
        const u32x4 a = ld128(rs, WS_ALIVE + lane * 32), b2 = ld128(rs, WS_ALIVE + lane * 32 + 16);
        const bool ok = a.x == tag && a.y == ehi && a.z == tag && a.w == ehi && b2.x == tag && b2.y == ehi && b2.z == tag && b2.w == ehi;
        if (__all(ok) || !spin_ok(F, spins)) break;
        __builtin_amdgcn_s_sleep(64);
      }
    }
    F[F_ALIVE] = 1u;
    if (c == 0 && lane == 0) __hip_atomic_store((GAS u64*)(p.ws + WS_CTRL), epoch, __ATOMIC_RELAXED, __HIP_MEMORY_SCOPE_AGENT);
    {
      lds_wait_ge(F, F_L, 6u);
      const int hd = lane & 1;
      float M = *(LAS float*)(L + L_MX + hd * 4), Ls = *(LAS float*)(L + L_LW + hd * 4);
#pragma unroll
      for (int i = 1; i < 6; ++i) { M = fmaxf(M, *(LAS float*)(L + L_MX + i * 8 + hd * 4)); Ls += *(LAS float*)(L + L_LW + i * 8 + hd * 4); }
      if (lane < 2) st64(rs, WS_ST + c * 16 + hd * 8, u32x2{vld(M), vld(Ls)});
      u32x2 o; u32 spins = 0;
      for (;;) {
        MEMBAR();
        o = ld64(rs, WS_ST + (c ^ 8) * 16 + hd * 8);
        if (__all((o.x & o.y & 1u) != 0u) || !spin_ok(F, spins)) break;
        __builtin_amdgcn_s_sleep(8);
      }
      const float m0 = __uint_as_float(vld(M)), l0 = __uint_as_float(vld(Ls)), m1 = __uint_as_float(o.x), l1 = __uint_as_float(o.y);
      const float Mg = fmaxf(m0, m1);
      const float e0 = __builtin_amdgcn_exp2f((m0 - Mg) * LOG2E), e1 = __builtin_amdgcn_exp2f((m1 - Mg) * LOG2E);
      const float sc = e0 / (e0 * l0 + e1 * l1);
      osA = __builtin_bit_cast(float, __builtin_amdgcn_readlane(__builtin_bit_cast(int, sc), 0));
      osB = __builtin_bit_cast(float, __builtin_amdgcn_readlane(__builtin_bit_cast(int, sc), 1));
    }
    if (isF) {
      load_cbsb();
      const int b_ = lane & 7;
      u32 spins = 0;
      for (;;) {
        MEMBAR();
        gap = ld64(rs, WS_MODT + b_ * 4608 + f * 8);
        gbp = ld64(rs, WS_MODT + b_ * 4608 + 864 * 4 + f * 8);
        if (__all(gap.x != SENT && gap.y != SENT && gbp.x != SENT && gbp.y != SENT) || !spin_ok(F, spins)) break;
      }
      xp = *(const GAS u32x2*)(p.x + b_ * DM + f * 4);
    }
    STAMP(7);
  } else {
    const int cw = w - 1;
    if (isF) {
      BARRIER();
      const int m = cw & 1, kt = cw >> 1;
      const u32 a0 = L_RING + 36 * m * 1024 + (kt * 192 + 8 * g + qp) * 64 + 8 * pp;
      if (m == 1) lds_add(F_WREL + 0, 1);
      lds_wait_ge(F, F_LANDED, m == 0 ? 36u : 72u);
#pragma unroll
      for (int s = 0; s < 6; ++s) { wf[2 * s] = tr_frag(L, a0 + s * 2048, 4 * 64); wf[2 * s + 1] = tr_frag(L, a0 + s * 2048 + 32, 4 * 64); }
      if (m == 0) lds_add(F_WREL + 0, 1);
      lds_add(F_WREL + 1, 1);
      lds_wait_ge(F, F_LANDED, 108u);
      const u32 d0 = L_RING + 72 * 1024 + (8 * g + qp) * 1152 + cw * 192 + 8 * pp;
#pragma unroll
      for (int t = 0; t < 6; ++t) wf[12 + t] = tr_frag(L, d0 + t * 32, 4 * 1152);
      lds_add(F_WREL + 2, 1);
    } else if (isO) {
      if (cw < 4) {
        BARRIER();
        lds_add(F_WREL + 0, 1);
        const u32 a0 = L_RING + 72 * 1024 + (8 * g + qp) * 128 + cw * 32 + 8 * pp;
        lds_wait_ge(F, F_LANDED, 104u);
#pragma unroll
        for (int s = 0; s < 8; ++s) wf[s] = tr_frag(L, a0 + s * 4096, 4 * 128);
        lds_add(F_WREL + 1, 1);
        lds_wait_ge(F, F_LANDED, 136u);
#pragma unroll
        for (int s = 8; s < 16; ++s) wf[s] = tr_frag(L, a0 + s * 4096, 4 * 128);
        lds_add(F_WREL + 2, 1);
      } else {
        const u32 tag = (u32)read_epoch();
        BARRIER();
        if (isQ) {
          f32x4 acc[4];
          q_partial(cw - 4, tag, acc);
          if (r16 < 8) {
#pragma unroll
            for (int ct = 0; ct < 4; ++ct) *(LAS f32x4*)(L + L_P2 + (cw - 4) * 2048 + ct * 512 + (g * 8 + r16) * 16) = acc[ct];
          }
          lds_add(F_P2, 1);
        } else lds_add(F_WREL + 0, 1);
        lds_add(F_WREL + 1, 1); lds_add(F_WREL + 2, 1);
      }
    } else if (isH) {
      u32 cv[6], xa[5], xb[5];
      const u32 tag = (u32)read_epoch();
#pragma unroll
      for (int i = 0; i < 6; ++i) cv[i] = ((const GAS u32*)p.c)[cw * 64 + lane + 384 * i];
      const int rowb = 6 + (cw & 1);
      const GAS u32* xr = (const GAS u32*)p.x + cw * 288;
      const GAS u32* xs = (const GAS u32*)p.x + rowb * 288;
#pragma unroll
      for (int i = 0; i < 4; ++i) { xa[i] = xr[lane + 64 * i]; xb[i] = xs[lane + 64 * i]; }
      xa[4] = xr[256 + (lane & 31)]; xb[4] = xs[256 + (lane & 31)];
      const u32x2 xq = *(const GAS u32x2*)(p.x + (lane & 7) * DM + hu * 16 + g * 4);
      BARRIER();
#pragma unroll
      for (int i = 0; i < 6; ++i) {
        const int e = cw * 64 + lane + 384 * i;
        const float a = bflo(cv[i]), b2 = bfhi(cv[i]);
        *(LAS u32*)(L + L_SC + (e / 288) * ACT_STRIDE + (e % 288) * 4) = pack2(a / (1.0f + __expf(-a)), b2 / (1.0f + __expf(-b2)));
      }
      {
        float v[10], mean_r, sd_r;
#pragma unroll
        for (int i = 0; i < 5; ++i) { v[2 * i] = bflo(xa[i]); v[2 * i + 1] = bfhi(xa[i]); }
        ln_stats(v, lane < 32, mean_r, sd_r);
        if (lane == 0) { *(LAS float*)(L + L_STAT + cw * 8) = mean_r; *(LAS float*)(L + L_STAT + cw * 8 + 4) = sd_r; }
        if (cw < 2) {
#pragma unroll
          for (int i = 0; i < 5; ++i) { v[2 * i] = bflo(xb[i]); v[2 * i + 1] = bfhi(xb[i]); }
          ln_stats(v, lane < 32, mean_r, sd_r);
          if (lane == 0) { *(LAS float*)(L + L_STAT + rowb * 8) = mean_r; *(LAS float*)(L + L_STAT + rowb * 8 + 4) = sd_r; }
        }
      }
      lds_add(F_SC, 1);
      STAMP(24);
      const u32 sb0 = L_SC + (lane & 7) * ACT_STRIDE + g * 16;
      {
        const int m = cw & 1, kt = cw >> 1;
        lds_wait_ge(F, F_SC, 6u);
        const float mean_r = *(LAS float*)(L + L_STAT + (lane & 7) * 8), sd_r = *(LAS float*)(L + L_STAT + (lane & 7) * 8 + 4);
        const float n0 = rb(rb(bflo(xq.x) - mean_r) / sd_r), n1 = rb(rb(bfhi(xq.x) - mean_r) / sd_r);
        const float n2 = rb(rb(bflo(xq.y) - mean_r) / sd_r), n3 = rb(rb(bfhi(xq.y) - mean_r) / sd_r);
        u32x4 bfr[6];
#pragma unroll
        for (int s = 0; s < 6; ++s) bfr[s] = lds128(L, sb0 + (kt * 6 + s) * 64);
        lds_wait_ge(F, F_LANDED, 36u);
        STAMP(25);
        const u32 a0 = L_RING + (m * 18 + kt * 6) * 1024 + (8 * g + qp) * 32 + 8 * pp;
        f32x4 acc = {0, 0, 0, 0}, acc2 = {0, 0, 0, 0};
#pragma unroll
        for (int s = 0; s < 6; s += 2) { acc = mfma(tr_frag(L, a0 + s * 1024, 4 * 32), bfr[s], acc); acc2 = mfma(tr_frag(L, a0 + (s + 1) * 1024, 4 * 32), bfr[s + 1], acc2); }
        acc += acc2;
        if (r16 < 8) *(LAS f32x4*)(L + L_RED + cw * 512 + (g * 8 + r16) * 16) = acc;
        lds_add(F_P1, 1);
        lds_add(F_WREL + 0, 1);
        if (cw == 5) {
          lds_wait_ge(F, F_P1, 6u);
          const int ls = (g * 8 + (lane & 7)) * 16;
          const f32x4 sa = *(LAS f32x4*)(L + L_RED + 0 * 512 + ls) + *(LAS f32x4*)(L + L_RED + 2 * 512 + ls) + *(LAS f32x4*)(L + L_RED + 4 * 512 + ls);
          const f32x4 ca = *(LAS f32x4*)(L + L_RED + 1 * 512 + ls) + *(LAS f32x4*)(L + L_RED + 3 * 512 + ls) + *(LAS f32x4*)(L + L_RED + 5 * 512 + ls);
          const float h0 = rb(rb(n0 * rb(1.0f + rb(ca[0]))) + rb(sa[0])), h1 = rb(rb(n1 * rb(1.0f + rb(ca[1]))) + rb(sa[1]));
          const float h2 = rb(rb(n2 * rb(1.0f + rb(ca[2]))) + rb(sa[2])), h3 = rb(rb(n3 * rb(1.0f + rb(ca[3]))) + rb(sa[3]));
          const u32x4 hu4 = {pack2(h0, h1), tag, pack2(h2, h3), tag};
#pragma unroll
          for (int i = 0; i < REP_H / 2; ++i)
            st128(rs, WS_H + (2 * i + (r16 >> 3)) * 18432 + ((lane & 7) * 144 + hu * 4 + g) * 16, hu4);
          STAMP(6);
        }
      }
      if (cw < 4) {
        lds_wait_ge(F, F_LANDED, 108u);
        const u32 a0 = L_RING + 36 * 1024 + (8 * g + qp) * 128 + cw * 32 + 8 * pp;
        f32x4 a0c = {0, 0, 0, 0}, a1c = {0, 0, 0, 0};
#pragma unroll 3
        for (int s = 0; s < 18; s += 2) {
          a0c = mfma(tr_frag(L, a0 + s * 4096, 4 * 128), lds128(L, sb0 + s * 64), a0c);
          a1c = mfma(tr_frag(L, a0 + (s + 1) * 4096, 4 * 128), lds128(L, sb0 + (s + 1) * 64), a1c);
        }
        const f32x4 a = a0c + a1c;
        lds_add(F_WREL + 1, 1);
        lds_wait_ge(F, F_INIT, 1u);
        if (r16 < 8) st64(rs, WS_MODT + r16 * 4608 + hu * 128 + cw * 32 + g * 8, u32x2{nosent(pack2(a[0], a[1])), nosent(pack2(a[2], a[3]))});
      } else lds_add(F_WREL + 1, 1);
      lds_add(F_WREL + 2, 1);
    } else {
      BARRIER();
      lds_add(F_WREL + 0, 1); lds_add(F_WREL + 1, 1); lds_add(F_WREL + 2, 1);
    }
    STAMP(1);
    u32x4 qf[4];
    u32 ka[4];
#pragma unroll
    for (int s = 0; s < 4; ++s) ka[s] = r16 * 256 + (((4 * s + g) ^ r16) << 4);
    float mx0 = -INFINITY, mx1 = -INFINITY;
#pragma unroll 1
    for (int q = cw; q < 64; q += 6) {
      const int pos = (q + roff) & 7;
      lds_wait_ge(F, F_LANDED, Wtot + 16u * (u32)(q + 1));
      const u32 sb = L_RING + pos * 16384;
      u32x4 kf[16];
#pragma unroll
      for (int t = 0; t < 4; ++t)
#pragma unroll
        for (int s = 0; s < 4; ++s) kf[t * 4 + s] = lds128(L, sb + ka[s] + t * 4096);
      WAIT_LGKM0();
      if (lane == 0) F[F_FREE + pos] = (u32)(q + 1);
      if (q < 6) {
        lds_wait_ge(F, F_Q, 1u);
        lds_wait_ge(F, F_WREL + 1, 7u); lds_wait_ge(F, F_WREL + 2, 7u);
        STAMP(2);
#pragma unroll
        for (int s = 0; s < 4; ++s) qf[s] = lds128(L, L_QS + (lane & 1) * 256 + s * 64 + g * 16);
      }
      f32x4 acc[4];
#pragma unroll
      for (int t = 0; t < 4; ++t) {
        acc[t] = f32x4{0, 0, 0, 0};
#pragma unroll
        for (int s = 0; s < 4; ++s) acc[t] = mfma(qf[s], kf[t * 4 + s], acc[t]);
      }
      float s0 = g == 0 ? acc[0][0] : g == 1 ? acc[1][0] : g == 2 ? acc[2][0] : acc[3][0];
      float s1 = g == 0 ? acc[0][1] : g == 1 ? acc[1][1] : g == 2 ? acc[2][1] : acc[3][1];
      s0 = rb(rb(s0) * 0.08838834764831845f);
      s1 = rb(rb(s1) * 0.08838834764831845f);
      mx0 = fmaxf(mx0, s0); mx1 = fmaxf(mx1, s1);
      *(LAS u16*)(L + L_SC + q * 256 + lane * 2) = (u16)(__float_as_uint(s0) >> 16);
      *(LAS u16*)(L + L_SC + q * 256 + 128 + lane * 2) = (u16)(__float_as_uint(s1) >> 16);
    }
    STAMP(3);
    {
      mx0 = wave_max(mx0); mx1 = wave_max(mx1);
      if (lane == 0) *(LAS u32x2*)(L + L_MX + cw * 8) = u32x2{__float_as_uint(mx0), __float_as_uint(mx1)};
      lds_add(F_MX, 1);
      lds_wait_ge(F, F_MX, 6u);
#pragma unroll
      for (int i = 0; i < 6; ++i) { mx0 = fmaxf(mx0, *(LAS float*)(L + L_MX + i * 8)); mx1 = fmaxf(mx1, *(LAS float*)(L + L_MX + i * 8 + 4)); }
      float l0 = 0.f, l1 = 0.f;
#pragma unroll 1
      for (int q = cw; q < 64; q += 6) {
        const float s0 = bf2f(*(LAS u16*)(L + L_SC + q * 256 + lane * 2)), s1 = bf2f(*(LAS u16*)(L + L_SC + q * 256 + 128 + lane * 2));
        const float p0 = __builtin_amdgcn_exp2f((s0 - mx0) * LOG2E), p1 = __builtin_amdgcn_exp2f((s1 - mx1) * LOG2E);
        l0 += p0; l1 += p1;
        *(LAS u16*)(L + L_SC + q * 256 + lane * 2) = (u16)f2bf(p0);
        *(LAS u16*)(L + L_SC + q * 256 + 128 + lane * 2) = (u16)f2bf(p1);
      }
      l0 = wave_sum(l0); l1 = wave_sum(l1);
      if (lane == 0) *(LAS u32x2*)(L + L_LW + cw * 8) = u32x2{__float_as_uint(l0), __float_as_uint(l1)};
      lds_add(F_L, 1);
    }
    f32x4 o[8];
#pragma unroll
    for (int cb = 0; cb < 8; ++cb) o[cb] = f32x4{0, 0, 0, 0};
    const u32 va0 = (8 * g + qp) * 256 + ((pp >> 1) << 4) + 8 * (pp & 1) + (((qp | ((g & 1) << 2)) << 1) << 4);
#pragma unroll 1
    for (int q = cw; q < 64; q += 6) {
      const int pos = (q + 64 + roff) & 7;
      if (isF && q >= 24 && q < 30) { lds_wait_ge(F, F_ALIVE, 1u); load_cbsb(); }
      const u32x4 pf0 = lds128(L, L_SC + q * 256 + (lane & 1) * 128 + g * 16), pf1 = lds128(L, L_SC + q * 256 + (lane & 1) * 128 + 64 + g * 16);
      lds_wait_ge(F, F_LANDED, Wtot + 16u * (u32)(q + 65));
      const u32 sb = L_RING + pos * 16384 + va0;
      u32x4 vf[8], vg2[8];
#pragma unroll
      for (int cb = 0; cb < 8; ++cb) vf[cb] = tr_frag(L, sb ^ (cb << 5), 1024);
#pragma unroll
      for (int cb = 0; cb < 8; ++cb) vg2[cb] = tr_frag(L, (sb ^ (cb << 5)) + 8192, 1024);
      WAIT_LGKM0();
      if (lane == 0) F[F_FREE + pos] = (u32)(q + 65);
#pragma unroll
      for (int cb = 0; cb < 8; ++cb) o[cb] = mfma(pf0, vf[cb], o[cb]);
#pragma unroll
      for (int cb = 0; cb < 8; ++cb) o[cb] = mfma(pf1, vg2[cb], o[cb]);
    }
    STAMP(4);
    if (g == 0) {
#pragma unroll
      for (int cb = 0; cb < 8; ++cb) *(LAS u32x2*)(L + L_ACT + cw * 1024 + (cb * 16 + r16) * 8) = u32x2{__float_as_uint(o[cb][0]), __float_as_uint(o[cb][1])};
    }
  }
  BARRIER();
  STAMP(8);
  if (w == 7) {
    f32x4 O = *(LAS f32x4*)(L + L_ACT + lane * 16);
#pragma unroll
    for (int i = 1; i < 6; ++i) O += *(LAS f32x4*)(L + L_ACT + i * 1024 + lane * 16);
    st128(rs, WS_OP + c * 1024 + lane * 16, u32x4{vld(O[0] * osA), vld(O[1] * osB), vld(O[2] * osA), vld(O[3] * osB)});
    STAMP(9);
  }
  if (isO) {
    constexpr int L_OS = L_RING;
    u32x4 ov[4];
    bool got[4] = {false, false, false, false};
    u32 spins = 0;
    for (;;) {
      MEMBAR();
#pragma unroll
      for (int i = 0; i < 4; ++i) if (!got[i]) ov[i] = ld128(rs, WS_OP + (xg + 8 * ((i >> 1) * 16 + 2 * w + (i & 1))) * 1024 + lane * 16);
      bool all = true;
#pragma unroll
      for (int i = 0; i < 4; ++i) if (!got[i]) {
        got[i] = __all((ov[i].x & ov[i].y & ov[i].z & ov[i].w & 1u) != 0u);
        all = all && got[i];
      }
      if (all || !spin_ok(F, spins)) break;
    }
#pragma unroll
    for (int hk = 0; hk < 2; ++hk) {
      const f32x4 s = __builtin_bit_cast(f32x4, ov[2 * hk]) + __builtin_bit_cast(f32x4, ov[2 * hk + 1]);
      *(LAS u32*)(L + L_OS + w * 1040 + hk * 512 + lane * 4) = pack2(s[0], s[2]);
      *(LAS u32*)(L + L_OS + w * 1040 + hk * 512 + 256 + lane * 4) = pack2(s[1], s[3]);
    }
    BARRIER();
    STAMP(10);
    if (w >= 1 && w <= 4) {
      const int ct = w - 1;
      const u32 ob = L_OS + (lane & 7) * 1040 + g * 16;
      u32x4 bfr[16];
#pragma unroll
      for (int s = 0; s < 16; ++s) bfr[s] = lds128(L, ob + s * 64);
      f32x4 a0 = {0, 0, 0, 0}, a1 = {0, 0, 0, 0}, a2 = {0, 0, 0, 0}, a3 = {0, 0, 0, 0};
#pragma unroll
      for (int s = 0; s < 16; s += 4) { a0 = mfma(wf[s], bfr[s], a0); a1 = mfma(wf[s + 1], bfr[s + 1], a1); a2 = mfma(wf[s + 2], bfr[s + 2], a2); a3 = mfma(wf[s + 3], bfr[s + 3], a3); }
      const f32x4 a = (a0 + a1) + (a2 + a3);
      if (r16 < 8) st128(rs, WS_WP + (xg * 144 + (u * 4 + ct) * 4 + g) * 128 + r16 * 16, u32x4{vld(a[0]), vld(a[1]), vld(a[2]), vld(a[3])});
      STAMP(11);
    }
  } else if (isF) {
    float x1r[4];
    if (w == 7) {
      u32x4 pv; u32 spins = 0;
      for (;;) {
        MEMBAR();
        pv = ld128(rs, WS_WP + ((lane >> 3) * 144 + f) * 128 + (lane & 7) * 16);
        if (__all((pv.x & pv.y & pv.z & pv.w & 1u) != 0u) || !spin_ok(F, spins)) break;
      }
      const float a0 = rb(sum_x8_16_32(__uint_as_float(pv.x))), a1 = rb(sum_x8_16_32(__uint_as_float(pv.y)));
      const float a2 = rb(sum_x8_16_32(__uint_as_float(pv.z))), a3 = rb(sum_x8_16_32(__uint_as_float(pv.w)));
      x1r[0] = rb(bflo(xp.x) + rb(bflo(gap.x) * a0)); x1r[1] = rb(bfhi(xp.x) + rb(bfhi(gap.x) * a1));
      x1r[2] = rb(bflo(xp.y) + rb(bflo(gap.y) * a2)); x1r[3] = rb(bfhi(xp.y) + rb(bfhi(gap.y) * a3));
      const u32x2 xo = {nosent(pack2(x1r[0], x1r[1])), nosent(pack2(x1r[2], x1r[3]))};
#pragma unroll
      for (int i = 0; i < REP_X / 8; ++i) st64(rs, WS_X1 + (i * 8 + (lane >> 3)) * 9216 + f * 64 + (lane & 7) * 8, xo);
      STAMP(10);
    }
    {
      const int xb = WS_X1 + (f % REP_X) * 9216;
      u32x4 xv, xw = {0u, 0u, 0u, 0u}; u32 spins = 0;
      for (;;) {
        MEMBAR();
        xv = ld128(rs, xb + tid * 16);
        if (w == 0) xw = ld128(rs, xb + 8192 + lane * 16);
        if (__all(xv.x != SENT && xv.y != SENT && xv.z != SENT && xv.w != SENT && xw.x != SENT && xw.y != SENT && xw.z != SENT && xw.w != SENT) || !spin_ok(F, spins)) break;
      }
      STAMP(16);
      {
        const u32 a = L_ACT + (tid & 3) * 2 * ACT_STRIDE + (tid >> 2) * 8;
        *(LAS u32x2*)(L + a) = u32x2{xv.x, xv.y}; *(LAS u32x2*)(L + a + ACT_STRIDE) = u32x2{xv.z, xv.w};
        if (w == 0) { *(LAS u32x2*)(L + a + 1024) = u32x2{xw.x, xw.y}; *(LAS u32x2*)(L + a + 1024 + ACT_STRIDE) = u32x2{xw.z, xw.w}; }
      }
      BARRIER();
      STAMP(17);
      u32 xr[5];
#pragma unroll
      for (int i = 0; i < 4; ++i) xr[i] = *(LAS u32*)(L + L_ACT + w * ACT_STRIDE + (lane + 64 * i) * 4);
      xr[4] = *(LAS u32*)(L + L_ACT + w * ACT_STRIDE + (256 + (lane & 31)) * 4);
      float v[10];
#pragma unroll
      for (int i = 0; i < 5; ++i) { v[2 * i] = bflo(xr[i]); v[2 * i + 1] = bfhi(xr[i]); }
      float s = (v[0] + v[1]) + (v[2] + v[3]) + ((v[4] + v[5]) + (v[6] + v[7]));
      if (lane < 32) s += v[8] + v[9];
      const float mean = wave_sum(s) * (1.0f / DM);
      float q2 = 0.f, q3 = 0.f;
#pragma unroll
      for (int i = 0; i < 4; ++i) { const float d0 = v[2 * i] - mean, d1 = v[2 * i + 1] - mean; q2 += d0 * d0; q3 += d1 * d1; }
      if (lane < 32) { const float d0 = v[8] - mean, d1 = v[9] - mean; q2 += d0 * d0; q3 += d1 * d1; }
      const float var = wave_sum(q2 + q3) * (1.0f / DM);
      const float mean_r = rb(mean);
      const float rinv = __builtin_amdgcn_rcpf(rb(__builtin_amdgcn_sqrtf(rb(rb(var) + 1e-5f))));
#pragma unroll
      for (int i = 0; i < 5; ++i) {
        const u32 dp = pack2(v[2 * i] - mean_r, v[2 * i + 1] - mean_r);
        const u32 np = pack2(bflo(dp) * rinv, bfhi(dp) * rinv);
        const u32 tp = pack2(bflo(np) * bflo(cbp[i]), bfhi(np) * bfhi(cbp[i]));
        const u32 hp = pack2(bflo(tp) + bflo(sbp[i]), bfhi(tp) + bfhi(sbp[i]));
        if (i < 4) *(LAS u32*)(L + L_ACT + w * ACT_STRIDE + (lane + 64 * i) * 4) = hp;
        else if (lane < 32) *(LAS u32*)(L + L_ACT + w * ACT_STRIDE + (256 + lane) * 4) = hp;
      }
      STAMP(18);
    }
    BARRIER();
    STAMP(11);
    constexpr int L_R2 = L_RING;
    constexpr int L_HH = L_RING + 8192;
    if (w == 7) {
      BARRIER();
      BARRIER();
      const int off = WS_FP + (lane >> 3) * 18432 + f * 128 + (lane & 7) * 16;
      u32x4 e[18]; u32 spins = 0;
      for (;;) {
        MEMBAR();
#pragma unroll
        for (int i = 0; i < 18; ++i) e[i] = ld128(rs, off + i * 8 * 18432);
        u32 a = 1u;
#pragma unroll
        for (int i = 0; i < 18; ++i) a &= e[i].x & e[i].y & e[i].z & e[i].w;
        if (__all(a != 0u) || !spin_ok(F, spins)) break;
      }
      STAMP(23);
      f32x4 s0 = __builtin_bit_cast(f32x4, e[0]), s1 = __builtin_bit_cast(f32x4, e[1]), s2 = __builtin_bit_cast(f32x4, e[2]);
#pragma unroll
      for (int i = 3; i < 18; i += 3) { s0 += __builtin_bit_cast(f32x4, e[i]); s1 += __builtin_bit_cast(f32x4, e[i + 1]); s2 += __builtin_bit_cast(f32x4, e[i + 2]); }
      const f32x4 s = (s0 + s1) + s2;
      const float d0 = rb(sum_x8_16_32(s[0])), d1 = rb(sum_x8_16_32(s[1])), d2 = rb(sum_x8_16_32(s[2])), d3 = rb(sum_x8_16_32(s[3]));
      const float o0 = x1r[0] + rb(bflo(gbp.x) * d0), o1 = x1r[1] + rb(bfhi(gbp.x) * d1);
      const float o2 = x1r[2] + rb(bflo(gbp.y) * d2), o3 = x1r[3] + rb(bfhi(gbp.y) * d3);
      if (lane < 8) *(GAS u32x2*)(p.out + lane * DM + f * 4) = u32x2{pack2(o0, o1), pack2(o2, o3)};
      STAMP(13);
    } else {
      const int cw = w - 1;
      if (w >= 1) {
        const int kt = cw >> 1;
        const u32 hb = L_ACT + (lane & 7) * ACT_STRIDE + kt * 384 + g * 16;
        u32x4 bfr[6];
#pragma unroll
        for (int s = 0; s < 6; ++s) bfr[s] = lds128(L, hb + s * 64);
        f32x4 a0 = {0, 0, 0, 0}, a1 = {0, 0, 0, 0}, a2 = {0, 0, 0, 0}, a3 = {0, 0, 0, 0};
#pragma unroll
        for (int s = 0; s < 6; s += 2) { a0 = mfma(wf[2 * s], bfr[s], a0); a1 = mfma(wf[2 * s + 1], bfr[s], a1); a2 = mfma(wf[2 * s + 2], bfr[s + 1], a2); a3 = mfma(wf[2 * s + 3], bfr[s + 1], a3); }
        a0 += a2; a1 += a3;
        if (r16 < 8) { *(LAS f32x4*)(L + L_R2 + cw * 1024 + (g * 8 + r16) * 16) = a0; *(LAS f32x4*)(L + L_R2 + cw * 1024 + 512 + (g * 8 + r16) * 16) = a1; }
      }
      STAMP(19);
      BARRIER();
      STAMP(20);
      if (w < 4) {
        const int ct = w >> 1, e = (w & 1) * 64 + lane;
        const u32 a = L_R2 + ct * 512 + e * 4;
        const float gg = rb(*(LAS float*)(L + a) + *(LAS float*)(L + a + 2048) + *(LAS float*)(L + a + 4096));
        const float uu = rb(*(LAS float*)(L + a + 1024) + *(LAS float*)(L + a + 3072) + *(LAS float*)(L + a + 5120));
        const float inner = 0.7978845608028654f * (gg + 0.044715f * gg * gg * gg);
        const float th = 1.0f - 2.0f * __builtin_amdgcn_rcpf(1.0f + __expf(2.0f * inner));
        const float ge = rb(0.5f * gg * (1.0f + th));
        const int ls = e >> 2, rr = e & 3, b_ = ls & 7, g_ = ls >> 3;
        *(LAS u16*)(L + L_HH + b_ * 80 + (ct * 16 + g_ * 4 + rr) * 2) = (u16)f2bf(ge * uu);
      }
      STAMP(21);
      BARRIER();
      STAMP(22);
      if (w >= 1) {
        const u32x4 bfr = lds128(L, L_HH + (lane & 7) * 80 + g * 16);
        f32x4 a[6];
#pragma unroll
        for (int t = 0; t < 6; ++t) a[t] = mfma(wf[12 + t], bfr, f32x4{0, 0, 0, 0});
#pragma unroll
        for (int t = 0; t < 6; ++t)
          if (r16 < 8) st128(rs, WS_FP + f * 18432 + ((cw * 6 + t) * 4 + g) * 128 + r16 * 16, u32x4{vld(a[t][0]), vld(a[t][1]), vld(a[t][2]), vld(a[t][3])});
      }
      STAMP(12);
    }
  }
}

}

extern "C" void kernel_launch(void* const* d_in, const int* in_sizes, int n_in, void* d_out, int out_size, void* d_ws, size_t ws_size,
                              hipStream_t stream) {
  (void)in_sizes; (void)n_in; (void)out_size; (void)ws_size;
  Params p;
  p.x = (const u16*)d_in[0];
  p.kc = (const unsigned char*)d_in[1];
  p.vc = (const unsigned char*)d_in[2];
  p.wq = (const unsigned char*)d_in[3];
  p.wo = (const unsigned char*)d_in[4];
  p.wg = (const unsigned char*)d_in[6];
  p.wu = (const unsigned char*)d_in[7];
  p.wd = (const unsigned char*)d_in[8];
  p.c = (const u16*)d_in[9];
  p.wmod = (const unsigned char*)d_in[10];
  p.out = (u16*)d_out;
  p.ws = (unsigned char*)d_ws;
  (void)hipFuncSetAttribute((const void*)fused_block, hipFuncAttributeMaxDynamicSharedMemorySize, LDS_BYTES);
  hipLaunchKernelGGL(fused_block, dim3(NCU), dim3(NT), LDS_BYTES, stream, p);
}
```

```cpp
#define PADG 120
#include <hip/hip_runtime.h>
#include <stdint.h>
#include <type_traits>
#pragma clang fp contract(off)

#define INFL 24
#define INFLW 256
#define HPOLL_SLEEP 2
#define QPOLL_SLEEP 2
#define AUX_K "nt"
#define AUX_V "nt"
#define AUX_W "nt"
#define STAMPS 0

#define LAS __attribute__((address_space(3)))
#define GAS __attribute__((address_space(1)))
typedef unsigned u32;
typedef unsigned long long u64;
typedef unsigned short u16;
typedef u32 u32x4 __attribute__((ext_vector_type(4)));
typedef u32 u32x2 __attribute__((ext_vector_type(2)));
typedef float f32x4 __attribute__((ext_vector_type(4)));
typedef __bf16 bf16x8 __attribute__((ext_vector_type(8)));
typedef short s16x4 __attribute__((ext_vector_type(4)));

namespace {

constexpr int NB = 8, DM = 576, HKV = 16, SK = 8192, DH = 128, DF = 4608;
constexpr int NCU = 256, NT = 512;

constexpr int REP_H = 8, REP_X = 16;
constexpr int WS_CTRL = 0;
constexpr int WS_ALIVE = 4096;
constexpr int WS_ST = 8192;
constexpr int WS_MODT = WS_ST + 4096;
constexpr int WS_X1 = WS_MODT + 36864;
constexpr int WS_H = WS_X1 + REP_X * 9216;
constexpr int WS_Q = WS_H + REP_H * 18432;
constexpr int WS_OP = WS_Q + 131072;
constexpr int WS_WP = WS_OP + 262144;
constexpr int WS_FP = WS_WP + 147456;
constexpr int WS_END = WS_FP + 144 * 18432;
constexpr int WS_DBG = 4 << 20;
static_assert(WS_END <= WS_DBG, "ws");

constexpr int L_RING = 0;
constexpr int L_SC = 131072;
constexpr int L_ACT = L_SC + 16384;
constexpr int L_QS = L_ACT + 9344;
constexpr int L_RED = L_QS + 512;
constexpr int L_STAT = L_RED + 3072;
constexpr int L_MX = L_STAT + 64;
constexpr int L_LW = L_MX + 64;
constexpr int L_FLG = L_LW + 64;
constexpr int L_P2 = L_SC + 8192;
constexpr int LDS_BYTES = 163840;
static_assert(L_FLG + 256 <= LDS_BYTES, "lds");
constexpr int ACT_STRIDE = 1168;
constexpr int F_LANDED = 0, F_FREE = 1  , F_WREL = 9  , F_SC = 12, F_P1 = 13, F_INIT = 14, F_Q = 15, F_ABORT = 16, F_ALIVE = 17, F_P2 = 18, F_MX = 19, F_L = 20;

constexpr u32 SENT = 0xFFFFFFFFu;
constexpr u32 LDS_SPIN_CAP = 1u << 21;
constexpr u32 GLB_SPIN_CAP = 1u << 17;
constexpr float LOG2E = 1.4426950408889634f;

struct Params {
  const u16* x; const unsigned char* kc; const unsigned char* vc; const unsigned char* wq; const unsigned char* wo;
  const unsigned char* wg; const unsigned char* wu; const unsigned char* wd; const u16* c; const unsigned char* wmod;
  u16* out; unsigned char* ws;
};

__device__ __forceinline__ float bf2f(u32 v16) { return __uint_as_float(v16 << 16); }
__device__ __forceinline__ float bflo(u32 d) { return __uint_as_float(d << 16); }
__device__ __forceinline__ float bfhi(u32 d) { return __uint_as_float(d & 0xffff0000u); }
__device__ __forceinline__ float rb(float f) { return (float)(__bf16)f; }
__device__ __forceinline__ u32 f2bf(float f) { return (u32)__builtin_bit_cast(u16, (__bf16)f); }
__device__ __forceinline__ u32 pack2(float lo, float hi) { return f2bf(lo) | (f2bf(hi) << 16); }
__device__ __forceinline__ u32 nosent(u32 p) { return p == SENT ? 0xFFFEFFFFu : p; }
__device__ __forceinline__ u32 vld(float f) { return __float_as_uint(f) | 1u; }
__device__ __forceinline__ f32x4 mfma(u32x4 a, u32x4 b, f32x4 c) {
  return __builtin_amdgcn_mfma_f32_16x16x32_bf16(__builtin_bit_cast(bf16x8, a), __builtin_bit_cast(bf16x8, b), c, 0, 0, 0);
}
__device__ __forceinline__ u32x4 mk_srd(const void* p, u32 bytes) {
  const u64 a = (u64)p;
  u32x4 s;
  s.x = __builtin_amdgcn_readfirstlane((u32)a);
  s.y = __builtin_amdgcn_readfirstlane((u32)(a >> 32) & 0xffffu);
  s.z = __builtin_amdgcn_readfirstlane(bytes);
  s.w = 0x00020000u;
  return s;
}
#define MEMBAR() asm volatile("" ::: "memory")
#define WAIT_LGKM0() asm volatile("s_waitcnt lgkmcnt(0)" ::: "memory")
#define WAIT_VM0() asm volatile("s_waitcnt vmcnt(0)" ::: "memory")
#define BARRIER() do { asm volatile("s_waitcnt lgkmcnt(0)" ::: "memory"); __builtin_amdgcn_s_barrier(); asm volatile("" ::: "memory"); } while (0)

#define DMA1(AUX, srd, voff, soff, ldsaddr)                                                                      \
  do { u32 keep_;                                                                                                \
    asm volatile("s_mov_b32 %0, m0\n\ts_mov_b32 m0, %1\n\ts_nop 0\n\t"                                           \
                 "buffer_load_dwordx4 %4, %2, %3 offen " AUX " lds\n\ts_mov_b32 m0, %0"                          \
                 : "=&s"(keep_) : "s"(ldsaddr), "s"(srd), "s"(soff), "v"(voff) : "memory"); } while (0)
#define DMA4(AUX, srd, v0, v1, v2, v3, soff, ldsaddr)                                                            \
  do { u32 keep_;                                                                                                \
    asm volatile("s_mov_b32 %0, m0\n\ts_mov_b32 m0, %1\n\ts_nop 0\n\t"                                           \
                 "buffer_load_dwordx4 %4, %2, %3 offen " AUX " lds\n\t"                                          \
                 "buffer_load_dwordx4 %5, %2, %3 offen offset:1024 " AUX " lds\n\t"                              \
                 "buffer_load_dwordx4 %6, %2, %3 offen offset:2048 " AUX " lds\n\t"                              \
                 "buffer_load_dwordx4 %7, %2, %3 offen offset:3072 " AUX " lds\n\ts_mov_b32 m0, %0"              \
                 : "=&s"(keep_) : "s"(ldsaddr), "s"(srd), "s"(soff), "v"(v0), "v"(v1), "v"(v2), "v"(v3) : "memory"); } while (0)

template <int CTRL> __device__ __forceinline__ float dpp(float x) {
  return __builtin_bit_cast(float, __builtin_amdgcn_mov_dpp(__builtin_bit_cast(int, x), CTRL, 0xf, 0xf, true));
}
constexpr int ROR8 = 0x128, ROR4 = 0x124, ROR2 = 0x122, ROR1 = 0x121;
__device__ __forceinline__ float xrow_max(float x) {
  auto s = __builtin_amdgcn_permlane16_swap(__float_as_uint(x), __float_as_uint(x), false, false);
  x = fmaxf(__uint_as_float(s[0]), __uint_as_float(s[1]));
  auto t = __builtin_amdgcn_permlane32_swap(__float_as_uint(x), __float_as_uint(x), false, false);
  return fmaxf(__uint_as_float(t[0]), __uint_as_float(t[1]));
}
__device__ __forceinline__ float xrow_sum(float x) {
  auto s = __builtin_amdgcn_permlane16_swap(__float_as_uint(x), __float_as_uint(x), false, false);
  x = __uint_as_float(s[0]) + __uint_as_float(s[1]);
  auto t = __builtin_amdgcn_permlane32_swap(__float_as_uint(x), __float_as_uint(x), false, false);
  return __uint_as_float(t[0]) + __uint_as_float(t[1]);
}
__device__ __forceinline__ float wave_max(float x) {
  x = fmaxf(x, dpp<ROR8>(x)); x = fmaxf(x, dpp<ROR4>(x)); x = fmaxf(x, dpp<ROR2>(x)); x = fmaxf(x, dpp<ROR1>(x));
  return xrow_max(x);
}
__device__ __forceinline__ float wave_sum(float x) {
  x += dpp<ROR8>(x); x += dpp<ROR4>(x); x += dpp<ROR2>(x); x += dpp<ROR1>(x);
  return xrow_sum(x);
}
__device__ __forceinline__ float sum_x8_16_32(float x) {
  x += dpp<ROR8>(x);
  return xrow_sum(x);
}

typedef volatile LAS u32* flagp;
__device__ __forceinline__ void lds_wait_ge(flagp F, int idx, u32 need) {
  u32 spins = 0;
  while (F[idx] < need) {
    __builtin_amdgcn_s_sleep(1);
    if ((++spins & 63u) == 0u) { if (F[F_ABORT] != 0u) break; if (spins > LDS_SPIN_CAP) { F[F_ABORT] = 1u; break; } }
  }
  MEMBAR();
}
__device__ __forceinline__ void lds_add(int idx, u32 n) {
  const u32 a = L_FLG + idx * 4;
  asm volatile("s_waitcnt lgkmcnt(0)\n\ts_mov_b64 vcc, exec\n\ts_mov_b64 exec, 1\n\tds_add_u32 %0, %1\n\ts_mov_b64 exec, vcc"
               :: "v"(a), "v"(n) : "vcc", "memory");
}
__device__ __forceinline__ bool spin_ok(flagp F, u32& spins) {
  if (++spins > GLB_SPIN_CAP) { F[F_ABORT] = 2u; return false; }
  if ((spins & 255u) == 0u && F[F_ABORT] != 0u) return false;
  return true;
}

constexpr int SC1 = 16;
typedef __amdgpu_buffer_rsrc_t rsrc_t;
__device__ __forceinline__ u32x4 ld128(rsrc_t r, int off) { return __builtin_amdgcn_raw_buffer_load_b128(r, off, 0, SC1); }
__device__ __forceinline__ u32x2 ld64(rsrc_t r, int off) { return __builtin_amdgcn_raw_buffer_load_b64(r, off, 0, SC1); }
__device__ __forceinline__ u32 ld32(rsrc_t r, int off) { return __builtin_amdgcn_raw_buffer_load_b32(r, off, 0, SC1); }
__device__ __forceinline__ void st128(rsrc_t r, int off, u32x4 v) { __builtin_amdgcn_raw_buffer_store_b128(v, r, off, 0, SC1); }
__device__ __forceinline__ void st64(rsrc_t r, int off, u32x2 v) { __builtin_amdgcn_raw_buffer_store_b64(v, r, off, 0, SC1); }

__device__ __forceinline__ u32x4 tr_frag(LAS unsigned char* L, u32 addr, u32 stride4) {
  const s16x4 a = __builtin_amdgcn_ds_read_tr16_b64_v4i16((LAS s16x4*)(L + addr));
  const s16x4 b = __builtin_amdgcn_ds_read_tr16_b64_v4i16((LAS s16x4*)(L + addr + stride4));
  const u32x2 a2 = __builtin_bit_cast(u32x2, a), b2 = __builtin_bit_cast(u32x2, b);
  return u32x4{a2.x, a2.y, b2.x, b2.y};
}
__device__ __forceinline__ u32x4 lds128(LAS unsigned char* L, u32 addr) { return *(LAS u32x4*)(L + addr); }

#define STAMP(i) do {} while (0)

__device__ __forceinline__ void ln_stats(const float (&v)[10], bool tail, float& mean_r, float& sd_r) {
  float s = 0.f;
#pragma unroll
  for (int i = 0; i < 8; ++i) s += v[i];
  if (tail) s += v[8] + v[9];
  const float mean = wave_sum(s) * (1.0f / DM);
  float q = 0.f;
#pragma unroll
  for (int i = 0; i < 8; ++i) { const float d = v[i] - mean; q += d * d; }
  if (tail) { const float d0 = v[8] - mean, d1 = v[9] - mean; q += d0 * d0 + d1 * d1; }
  const float var = wave_sum(q) * (1.0f / DM);
  mean_r = rb(mean);
  sd_r = rb(sqrtf(rb(rb(var) + 1e-5f)));
}
__device__ __forceinline__ float ln_mod(float x, float mean_r, float sd_r, float scale, float shift) {
  const float n = rb(rb(x - mean_r) / sd_r);
  return rb(rb(n * rb(1.0f + scale)) + shift);
}

__global__ void __launch_bounds__(NT, 2) fused_block(Params p) {
  extern __shared__ __attribute__((aligned(16))) unsigned char smem[];
  LAS unsigned char* L = (LAS unsigned char*)smem;
  flagp F = (flagp)(L + L_FLG);
  const int tid = threadIdx.x, lane = tid & 63, w = __builtin_amdgcn_readfirstlane(tid >> 6);
  STAMP(0);
  const u64 t_entry = __builtin_amdgcn_s_memrealtime();
  const int c = blockIdx.x, xg = c & 7, ci = c >> 3;
  const int hkv = 2 * xg + (ci >> 4), cj = ci & 15, bb = cj >> 1, half = cj & 1;
  const bool isF = ci < 18, isO = ci >= 18 && ci < 27;
  const int hu = xg * 5 + (ci - 27);
  const bool isH = ci >= 27 && hu < 36;
  const int f = xg * 18 + ci;
  const int u = ci - 18;
  const bool isQ = isO && u < 8;
  const u32 Wtot = isF ? 108u : isO ? 136u : isH ? 108u : 0u;
  const int roff = (isF || isH) ? 7 : 0;
  const int g = lane >> 4, r16 = lane & 15, qp = (lane & 15) >> 2, pp = lane & 3;
  const rsrc_t rs = __builtin_amdgcn_make_buffer_rsrc((void*)p.ws, 0, WS_END, 0x00020000);

  auto read_epoch = [&]() {
    u64 e = *(const __attribute__((address_space(4))) u64*)(p.ws + WS_CTRL) + 1ull;
    asm volatile("" : "+s"(e));
    return e;
  };

  u32x4 wf[18];
  u32 cbp[5], sbp[5];
  u32x2 gap, gbp, xp;
  float osA = 0.f, osB = 0.f;

  auto load_cbsb = [&]() {
    u32 spins = 0;
    for (;;) {
      MEMBAR();
      const int base = WS_MODT + w * 4608;
#pragma unroll
      for (int i = 0; i < 4; ++i) { sbp[i] = ld32(rs, base + (288 + lane + 64 * i) * 4); cbp[i] = ld32(rs, base + (576 + lane + 64 * i) * 4); }
      sbp[4] = ld32(rs, base + (288 + 256 + (lane & 31)) * 4); cbp[4] = ld32(rs, base + (576 + 256 + (lane & 31)) * 4);
      bool ok = true;
#pragma unroll
      for (int i = 0; i < 5; ++i) ok = ok && sbp[i] != SENT && cbp[i] != SENT;
      if (__all(ok) || !spin_ok(F, spins)) break;
    }
#pragma unroll
    for (int i = 0; i < 5; ++i) cbp[i] = pack2(1.0f + bflo(cbp[i]), 1.0f + bfhi(cbp[i]));
  };
  auto q_partial = [&](int kt, u32 tag, f32x4 (&acc)[4]) {
    lds_wait_ge(F, F_LANDED, 72u);
    u32x4 pq[24];
    const u32 a0 = L_RING + (kt * 192 + 8 * g + qp) * 128 + 8 * pp;
#pragma unroll
    for (int s = 0; s < 6; ++s)
#pragma unroll
      for (int ct = 0; ct < 4; ++ct) pq[s * 4 + ct] = tr_frag(L, a0 + s * 4096 + ct * 32, 4 * 128);
    lds_add(F_WREL + 0, 1);
    STAMP(6);
    u32x4 hv[6];
    u32 spins = 0;
    const int hb = WS_H + (c % REP_H) * 18432 + kt * 768;
    int go[6], lo[6];
#pragma unroll
    for (int i = 0; i < 6; ++i) { const int e = i * 64 + lane, row = e / 48, j = e % 48; go[i] = hb + row * 2304 + j * 16; lo[i] = L_ACT + row * ACT_STRIDE + kt * 384 + j * 8; }
    for (;;) {
      MEMBAR();
#pragma unroll
      for (int i = 0; i < 6; ++i) hv[i] = ld128(rs, go[i]);
      bool ok = true;
#pragma unroll
      for (int i = 0; i < 6; ++i) ok = ok && hv[i].y == tag && hv[i].w == tag;
      if (__all(ok) || !spin_ok(F, spins)) break;
      __builtin_amdgcn_s_sleep(HPOLL_SLEEP);
    }
#pragma unroll
    for (int i = 0; i < 6; ++i) *(LAS u32x2*)(L + lo[i]) = u32x2{hv[i].x, hv[i].z};
    WAIT_LGKM0();
    STAMP(3);
    const u32 ab = L_ACT + (lane & 7) * ACT_STRIDE + kt * 384 + g * 16;
#pragma unroll
    for (int ct = 0; ct < 4; ++ct) acc[ct] = f32x4{0, 0, 0, 0};
#pragma unroll
    for (int s = 0; s < 6; ++s) {
      const u32x4 bfr = lds128(L, ab + s * 64);
#pragma unroll
      for (int ct = 0; ct < 4; ++ct) acc[ct] = mfma(pq[s * 4 + ct], bfr, acc[ct]);
    }
  };

  if (w == 0) {
    __builtin_amdgcn_s_setprio(3);
    if (lane < 32) F[lane] = 0u;
    u32 n = 0;
    auto run = [&](auto inflp, const u32x4& srd, u32 voff, u32 step, u32 first, u32 cnt) {
      constexpr int NP = decltype(inflp)::value;
#pragma unroll 1
      for (u32 s = first; s < cnt; ++s) {
        DMA1(AUX_W, srd, voff, s * step, (u32)(L_RING + n * 1024));
        ++n;
        asm volatile("s_waitcnt vmcnt(%0)" :: "i"(NP - 1) : "memory");
        if (n > (u32)(NP - 1)) F[F_LANDED] = n - (u32)(NP - 1);
      }
    };
    typedef std::integral_constant<int, INFLW / 32> P32;
    typedef std::integral_constant<int, INFLW / 16> P16;
    typedef std::integral_constant<int, (INFLW / 8 > 32 ? 32 : INFLW / 8)> P8;
    const u32 vg = (lane >> 2) * 9216 + (lane & 3) * 16, vm32 = (lane >> 1) * 6912 + (lane & 1) * 16;
    const u32 v128o = (lane >> 3) * 1152 + (lane & 7) * 16;
    u32x4 srd0; u32 v0, step0, cnt0;
    if (isF) { srd0 = mk_srd(p.wg + f * 64, DM * 9216 - f * 64); v0 = vg; step0 = 16 * 9216; cnt0 = 36; }
    else if (isQ) { srd0 = mk_srd(p.wq + (xg * 512 + u * 64) * 2, DM * 8192 - (xg * 512 + u * 64) * 2); v0 = (lane >> 3) * 8192 + (lane & 7) * 16; step0 = 8 * 8192; cnt0 = 72; }
    else if (isH) { srd0 = mk_srd(p.wmod + hu * 32, DM * 6912 - hu * 32); v0 = vm32; step0 = 32 * 6912; cnt0 = 18; }
    else { srd0 = mk_srd(p.wq, 0); v0 = 0; step0 = 0; cnt0 = 0; }
    const u32 pre = cnt0 < (u32)P32::value ? cnt0 : (u32)P32::value;
#pragma unroll 1
    for (u32 s = 0; s < pre; ++s) { DMA1(AUX_W, srd0, v0, s * step0, (u32)(L_RING + n * 1024)); ++n; }
    BARRIER();
    STAMP(4);
    if (isF) {
      run(P16(), srd0, v0, step0, pre, 36);
      run(P16(), mk_srd(p.wu + f * 64, DM * 9216 - f * 64), vg, 16 * 9216, 0, 36);
      run(P8(), mk_srd(p.wd + f * 36864, 36864), lane * 16, 1024, 0, 36);
    } else if (isO) {
      if (isQ) run(P8(), srd0, v0, step0, pre, 72); else n = 72;
      run(P8(), mk_srd(p.wo + xg * 512 * 1152 + u * 128, 512 * 1152 - u * 128), v128o, 8 * 1152, 0, 64);
    } else if (isH) {
      run(P32(), srd0, v0, step0, pre, 18);
      run(P32(), mk_srd(p.wmod + 1152 + hu * 32, DM * 6912 - 1152 - hu * 32), vm32, 32 * 6912, 0, 18);
      run(P8(), mk_srd(p.wmod + 2304 + hu * 128, DM * 6912 - 2304 - hu * 128), (lane >> 3) * 6912 + (lane & 7) * 16, 8 * 6912, 0, 72);
    }
    STAMP(1);
    const size_t kvoff = ((size_t)(bb * HKV + hkv) * SK + (size_t)half * 4096) * 256;
    const u32x4 srd_k = mk_srd(p.kc + kvoff, 1u << 20), srd_v = mk_srd(p.vc + kvoff, 1u << 20);
    u32 vk[4], vv[2];
#pragma unroll
    for (int j = 0; j < 4; ++j) vk[j] = g * 256 + ((r16 ^ (4 * j + g)) << 4);
#pragma unroll
    for (int j = 0; j < 2; ++j) vv[j] = g * 256 + ((r16 ^ ((g | (j << 2)) << 1)) << 4);
    const u32 wneed = isF ? 0x03332211u : isO ? 0x33221111u : isH ? 0x02222211u : 0u;
    auto wait_free = [&](int q, int pos) {
      if (q >= 8) {
        if (F[F_FREE + pos] < (u32)(q - 7)) {
          WAIT_VM0(); F[F_LANDED] = n; lds_wait_ge(F, F_FREE + pos, (u32)(q - 7));
        }
      } else {
        const int nb = (wneed >> (4 * pos)) & 15;
#pragma unroll 1
        for (int i = 0; i < nb; ++i)
          if (F[F_WREL + i] < 7u) { WAIT_VM0(); F[F_LANDED] = n; lds_wait_ge(F, F_WREL + i, 7u); }
      }
    };
#pragma unroll 1
    for (int q = 0; q < 64; ++q) {
      const int pos = (q + roff) & 7;
      wait_free(q, pos);
#pragma unroll
      for (int gq = 0; gq < 4; ++gq) {
        DMA4(AUX_K, srd_k, vk[0], vk[1], vk[2], vk[3], (u32)(q * 16384 + gq * 4096), (u32)(L_RING + pos * 16384 + gq * 4096));
        n += 4;
        asm volatile("s_waitcnt vmcnt(%0)" :: "i"(INFL - 4) : "memory");
        if (n > (u32)(INFL - 4)) F[F_LANDED] = n - (u32)(INFL - 4);
      }
    }
    STAMP(2);
#pragma unroll 1
    for (int q = 64; q < 128; ++q) {
      const int pos = (q + roff) & 7;
      wait_free(q, pos);
#pragma unroll
      for (int gq = 0; gq < 4; ++gq) {
        DMA4(AUX_V, srd_v, vv[0], vv[0], vv[1], vv[1], (u32)((q - 64) * 16384 + gq * 4096), (u32)(L_RING + pos * 16384 + gq * 4096));
        n += 4;
        asm volatile("s_waitcnt vmcnt(%0)" :: "i"(INFL - 4) : "memory");
        F[F_LANDED] = n - (u32)(INFL - 4);
      }
    }
    WAIT_VM0();
    F[F_LANDED] = n;
    STAMP(3);
    __builtin_amdgcn_s_setprio(0);
    if (isF) { lds_wait_ge(F, F_ALIVE, 1u); load_cbsb(); }
  } else if (w == 7) {
    const u64 epoch = read_epoch();
    const u32 tag = (u32)epoch;
    const u32x4 z4 = {0u, 0u, 0u, 0u};
    const u32x2 s2 = {SENT, SENT};
    if (isH) {
#pragma unroll
      for (int i = 0; i < 2; ++i) st64(rs, WS_MODT + (lane & 7) * 4608 + hu * 128 + (2 * i + (r16 >> 3)) * 32 + g * 8, s2);
    }
    BARRIER();
    st128(rs, WS_OP + c * 1024 + lane * 16, z4);
    if (lane == 0) st128(rs, WS_ST + c * 16, z4);
    if (isO) {
#pragma unroll
      for (int i = 0; i < 2; ++i) st128(rs, WS_WP + (xg * 144 + u * 16) * 128 + i * 1024 + lane * 16, z4);
    }
    if (isF) {
#pragma unroll
      for (int i = 0; i < REP_X / 8; ++i) st64(rs, WS_X1 + (i * 8 + (lane >> 3)) * 9216 + f * 64 + (lane & 7) * 8, s2);
#pragma unroll 1
      for (int i = 0; i < 18; ++i) st128(rs, WS_FP + f * 18432 + i * 1024 + lane * 16, z4);
    }
    if (lane == 0) __hip_atomic_store((GAS u64*)(p.ws + 6144) + c, t_entry, __ATOMIC_RELAXED, __HIP_MEMORY_SCOPE_AGENT);
    WAIT_VM0();
    F[F_INIT] = 1u;
    if (lane == 0) __hip_atomic_store((GAS u64*)(p.ws + WS_ALIVE) + c, epoch, __ATOMIC_RELAXED, __HIP_MEMORY_SCOPE_AGENT);
    STAMP(1);
    if (isQ) {
      f32x4 acc[4];
      q_partial(2, tag, acc);
      lds_add(F_WREL + 1, 1); lds_add(F_WREL + 2, 1);
      lds_wait_ge(F, F_P2, 2u);
      const int ls = (g * 8 + (lane & 7)) * 16;
#pragma unroll
      for (int ct = 0; ct < 4; ++ct) {
        const f32x4 a = (*(LAS f32x4*)(L + L_P2 + ct * 512 + ls) + *(LAS f32x4*)(L + L_P2 + 2048 + ct * 512 + ls)) + acc[ct];
        if (r16 < 8) st128(rs, WS_Q + ((2 * xg + (u >> 2)) * 8 + r16) * 1024 + ((u & 3) * 16 + ct * 4 + g) * 16, u32x4{pack2(a[0], a[1]), tag, pack2(a[2], a[3]), tag});
      }
      STAMP(4);
    } else {
      lds_add(F_WREL + 0, 1); lds_add(F_WREL + 1, 1); lds_add(F_WREL + 2, 1);
    }
    {
      u32x4 qv; u32 spins = 0;
      for (;;) {
        MEMBAR();
        qv = ld128(rs, WS_Q + (hkv * 8 + bb) * 1024 + lane * 16);
        if (__all(qv.y == tag && qv.w == tag) || !spin_ok(F, spins)) break;
        __builtin_amdgcn_s_sleep(QPOLL_SLEEP);
      }
      *(LAS u32x2*)(L + L_QS + lane * 8) = u32x2{qv.x, qv.z};
      WAIT_LGKM0();
      F[F_Q] = 1u;
    }
    STAMP(5);
    lds_wait_ge(F, F_LANDED, Wtot + 16u * 56u);
    {
      const u32 ehi = (u32)(epoch >> 32);
      u32 spins = 0;
      for (;;) {
        MEMBAR();
        const u32x4 a = ld128(rs, WS_ALIVE + lane * 32), b2 = ld128(rs, WS_ALIVE + lane * 32 + 16);
        const bool ok = a.x == tag && a.y == ehi && a.z == tag && a.w == ehi && b2.x == tag && b2.y == ehi && b2.z == tag && b2.w == ehi;
        if (__all(ok) || !spin_ok(F, spins)) break;
        __builtin_amdgcn_s_sleep(64);
      }
    }
    F[F_ALIVE] = 1u;
    if (c == 0 && lane == 0) __hip_atomic_store((GAS u64*)(p.ws + WS_CTRL), epoch, __ATOMIC_RELAXED, __HIP_MEMORY_SCOPE_AGENT);
    {
      lds_wait_ge(F, F_L, 6u);
      const int hd = lane & 1;
      float M = *(LAS float*)(L + L_MX + hd * 4), Ls = *(LAS float*)(L + L_LW + hd * 4);
#pragma unroll
      for (int i = 1; i < 6; ++i) { M = fmaxf(M, *(LAS float*)(L + L_MX + i * 8 + hd * 4)); Ls += *(LAS float*)(L + L_LW + i * 8 + hd * 4); }
      if (lane < 2) st64(rs, WS_ST + c * 16 + hd * 8, u32x2{vld(M), vld(Ls)});
      u32x2 o; u32 spins = 0;
      for (;;) {
        MEMBAR();
        o = ld64(rs, WS_ST + (c ^ 8) * 16 + hd * 8);
        if (__all((o.x & o.y & 1u) != 0u) || !spin_ok(F, spins)) break;
        __builtin_amdgcn_s_sleep(8);
      }
      const float m0 = __uint_as_float(vld(M)), l0 = __uint_as_float(vld(Ls)), m1 = __uint_as_float(o.x), l1 = __uint_as_float(o.y);
      const float Mg = fmaxf(m0, m1);
      const float e0 = __builtin_amdgcn_exp2f((m0 - Mg) * LOG2E), e1 = __builtin_amdgcn_exp2f((m1 - Mg) * LOG2E);
      const float sc = e0 / (e0 * l0 + e1 * l1);
      osA = __builtin_bit_cast(float, __builtin_amdgcn_readlane(__builtin_bit_cast(int, sc), 0));
      osB = __builtin_bit_cast(float, __builtin_amdgcn_readlane(__builtin_bit_cast(int, sc), 1));
    }
    if (isF) {
      load_cbsb();
      const int b_ = lane & 7;
      u32 spins = 0;
      for (;;) {
        MEMBAR();
        gap = ld64(rs, WS_MODT + b_ * 4608 + f * 8);
        gbp = ld64(rs, WS_MODT + b_ * 4608 + 864 * 4 + f * 8);
        if (__all(gap.x != SENT && gap.y != SENT && gbp.x != SENT && gbp.y != SENT) || !spin_ok(F, spins)) break;
      }
      xp = *(const GAS u32x2*)(p.x + b_ * DM + f * 4);
    }
    STAMP(7);
  } else {
    const int cw = w - 1;
    if (isF) {
      BARRIER();
      const int m = cw & 1, kt = cw >> 1;
      const u32 a0 = L_RING + 36 * m * 1024 + (kt * 192 + 8 * g + qp) * 64 + 8 * pp;
      if (m == 1) lds_add(F_WREL + 0, 1);
      lds_wait_ge(F, F_LANDED, m == 0 ? 36u : 72u);
#pragma unroll
      for (int s = 0; s < 6; ++s) { wf[2 * s] = tr_frag(L, a0 + s * 2048, 4 * 64); wf[2 * s + 1] = tr_frag(L, a0 + s * 2048 + 32, 4 * 64); }
      if (m == 0) lds_add(F_WREL + 0, 1);
      lds_add(F_WREL + 1, 1);
      lds_wait_ge(F, F_LANDED, 108u);
      const u32 d0 = L_RING + 72 * 1024 + (8 * g + qp) * 1152 + cw * 192 + 8 * pp;
#pragma unroll
      for (int t = 0; t < 6; ++t) wf[12 + t] = tr_frag(L, d0 + t * 32, 4 * 1152);
      lds_add(F_WREL + 2, 1);
    } else if (isO) {
      if (cw < 4) {
        BARRIER();
        lds_add(F_WREL + 0, 1);
        const u32 a0 = L_RING + 72 * 1024 + (8 * g + qp) * 128 + cw * 32 + 8 * pp;
        lds_wait_ge(F, F_LANDED, 104u);
#pragma unroll
        for (int s = 0; s < 8; ++s) wf[s] = tr_frag(L, a0 + s * 4096, 4 * 128);
        lds_add(F_WREL + 1, 1);
        lds_wait_ge(F, F_LANDED, 136u);
#pragma unroll
        for (int s = 8; s < 16; ++s) wf[s] = tr_frag(L, a0 + s * 4096, 4 * 128);
        lds_add(F_WREL + 2, 1);
      } else {
        const u32 tag = (u32)read_epoch();
        BARRIER();
        if (isQ) {
          f32x4 acc[4];
          q_partial(cw - 4, tag, acc);
          if (r16 < 8) {
#pragma unroll
            for (int ct = 0; ct < 4; ++ct) *(LAS f32x4*)(L + L_P2 + (cw - 4) * 2048 + ct * 512 + (g * 8 + r16) * 16) = acc[ct];
          }
          lds_add(F_P2, 1);
        } else lds_add(F_WREL + 0, 1);
        lds_add(F_WREL + 1, 1); lds_add(F_WREL + 2, 1);
      }
    } else if (isH) {
      u32 cv[6], xa[5], xb[5];
      const u32 tag = (u32)read_epoch();
#pragma unroll
      for (int i = 0; i < 6; ++i) cv[i] = ((const GAS u32*)p.c)[cw * 64 + lane + 384 * i];
      const int rowb = 6 + (cw & 1);
      const GAS u32* xr = (const GAS u32*)p.x + cw * 288;
      const GAS u32* xs = (const GAS u32*)p.x + rowb * 288;
#pragma unroll
      for (int i = 0; i < 4; ++i) { xa[i] = xr[lane + 64 * i]; xb[i] = xs[lane + 64 * i]; }
      xa[4] = xr[256 + (lane & 31)]; xb[4] = xs[256 + (lane & 31)];
      const u32x2 xq = *(const GAS u32x2*)(p.x + (lane & 7) * DM + hu * 16 + g * 4);
      BARRIER();
#pragma unroll
      for (int i = 0; i < 6; ++i) {
        const int e = cw * 64 + lane + 384 * i;
        const float a = bflo(cv[i]), b2 = bfhi(cv[i]);
        *(LAS u32*)(L + L_SC + (e / 288) * ACT_STRIDE + (e % 288) * 4) = pack2(a / (1.0f + __expf(-a)), b2 / (1.0f + __expf(-b2)));
      }
      {
        float v[10], mean_r, sd_r;
#pragma unroll
        for (int i = 0; i < 5; ++i) { v[2 * i] = bflo(xa[i]); v[2 * i + 1] = bfhi(xa[i]); }
        ln_stats(v, lane < 32, mean_r, sd_r);
        if (lane == 0) { *(LAS float*)(L + L_STAT + cw * 8) = mean_r; *(LAS float*)(L + L_STAT + cw * 8 + 4) = sd_r; }
        if (cw < 2) {
#pragma unroll
          for (int i = 0; i < 5; ++i) { v[2 * i] = bflo(xb[i]); v[2 * i + 1] = bfhi(xb[i]); }
          ln_stats(v, lane < 32, mean_r, sd_r);
          if (lane == 0) { *(LAS float*)(L + L_STAT + rowb * 8) = mean_r; *(LAS float*)(L + L_STAT + rowb * 8 + 4) = sd_r; }
        }
      }
      lds_add(F_SC, 1);
      STAMP(24);
      const u32 sb0 = L_SC + (lane & 7) * ACT_STRIDE + g * 16;
      {
        const int m = cw & 1, kt = cw >> 1;
        lds_wait_ge(F, F_SC, 6u);
        const float mean_r = *(LAS float*)(L + L_STAT + (lane & 7) * 8), sd_r = *(LAS float*)(L + L_STAT + (lane & 7) * 8 + 4);
        const float n0 = rb(rb(bflo(xq.x) - mean_r) / sd_r), n1 = rb(rb(bfhi(xq.x) - mean_r) / sd_r);
        const float n2 = rb(rb(bflo(xq.y) - mean_r) / sd_r), n3 = rb(rb(bfhi(xq.y) - mean_r) / sd_r);
        u32x4 bfr[6];
#pragma unroll
        for (int s = 0; s < 6; ++s) bfr[s] = lds128(L, sb0 + (kt * 6 + s) * 64);
        lds_wait_ge(F, F_LANDED, 36u);
        STAMP(25);
        const u32 a0 = L_RING + (m * 18 + kt * 6) * 1024 + (8 * g + qp) * 32 + 8 * pp;
        f32x4 acc = {0, 0, 0, 0}, acc2 = {0, 0, 0, 0};
#pragma unroll
        for (int s = 0; s < 6; s += 2) { acc = mfma(tr_frag(L, a0 + s * 1024, 4 * 32), bfr[s], acc); acc2 = mfma(tr_frag(L, a0 + (s + 1) * 1024, 4 * 32), bfr[s + 1], acc2); }
        acc += acc2;
        if (r16 < 8) *(LAS f32x4*)(L + L_RED + cw * 512 + (g * 8 + r16) * 16) = acc;
        lds_add(F_P1, 1);
        lds_add(F_WREL + 0, 1);
        if (cw == 5) {
          lds_wait_ge(F, F_P1, 6u);
          const int ls = (g * 8 + (lane & 7)) * 16;
          const f32x4 sa = *(LAS f32x4*)(L + L_RED + 0 * 512 + ls) + *(LAS f32x4*)(L + L_RED + 2 * 512 + ls) + *(LAS f32x4*)(L + L_RED + 4 * 512 + ls);
          const f32x4 ca = *(LAS f32x4*)(L + L_RED + 1 * 512 + ls) + *(LAS f32x4*)(L + L_RED + 3 * 512 + ls) + *(LAS f32x4*)(L + L_RED + 5 * 512 + ls);
          const float h0 = rb(rb(n0 * rb(1.0f + rb(ca[0]))) + rb(sa[0])), h1 = rb(rb(n1 * rb(1.0f + rb(ca[1]))) + rb(sa[1]));
          const float h2 = rb(rb(n2 * rb(1.0f + rb(ca[2]))) + rb(sa[2])), h3 = rb(rb(n3 * rb(1.0f + rb(ca[3]))) + rb(sa[3]));
          const u32x4 hu4 = {pack2(h0, h1), tag, pack2(h2, h3), tag};
#pragma unroll
          for (int i = 0; i < REP_H / 2; ++i)
            st128(rs, WS_H + (2 * i + (r16 >> 3)) * 18432 + ((lane & 7) * 144 + hu * 4 + g) * 16, hu4);
          STAMP(6);
        }
      }
      if (cw < 4) {
        lds_wait_ge(F, F_LANDED, 108u);
        const u32 a0 = L_RING + 36 * 1024 + (8 * g + qp) * 128 + cw * 32 + 8 * pp;
        f32x4 a0c = {0, 0, 0, 0}, a1c = {0, 0, 0, 0};
#pragma unroll 3
        for (int s = 0; s < 18; s += 2) {
          a0c = mfma(tr_frag(L, a0 + s * 4096, 4 * 128), lds128(L, sb0 + s * 64), a0c);
          a1c = mfma(tr_frag(L, a0 + (s + 1) * 4096, 4 * 128), lds128(L, sb0 + (s + 1) * 64), a1c);
        }
        const f32x4 a = a0c + a1c;
        lds_add(F_WREL + 1, 1);
        lds_wait_ge(F, F_INIT, 1u);
        if (r16 < 8) st64(rs, WS_MODT + r16 * 4608 + hu * 128 + cw * 32 + g * 8, u32x2{nosent(pack2(a[0], a[1])), nosent(pack2(a[2], a[3]))});
      } else lds_add(F_WREL + 1, 1);
      lds_add(F_WREL + 2, 1);
    } else {
      BARRIER();
      lds_add(F_WREL + 0, 1); lds_add(F_WREL + 1, 1); lds_add(F_WREL + 2, 1);
    }
    STAMP(1);
    u32x4 qf[4];
    u32 ka[4];
#pragma unroll
    for (int s = 0; s < 4; ++s) ka[s] = r16 * 256 + (((4 * s + g) ^ r16) << 4);
    float mx0 = -INFINITY, mx1 = -INFINITY;
#pragma unroll 1
    for (int q = cw; q < 64; q += 6) {
      const int pos = (q + roff) & 7;
      lds_wait_ge(F, F_LANDED, Wtot + 16u * (u32)(q + 1));
      const u32 sb = L_RING + pos * 16384;
      u32x4 kf[16];
#pragma unroll
      for (int t = 0; t < 4; ++t)
#pragma unroll
        for (int s = 0; s < 4; ++s) kf[t * 4 + s] = lds128(L, sb + ka[s] + t * 4096);
      WAIT_LGKM0();
      if (lane == 0) F[F_FREE + pos] = (u32)(q + 1);
      if (q < 6) {
        lds_wait_ge(F, F_Q, 1u);
        lds_wait_ge(F, F_WREL + 1, 7u); lds_wait_ge(F, F_WREL + 2, 7u);
        STAMP(2);
#pragma unroll
        for (int s = 0; s < 4; ++s) qf[s] = lds128(L, L_QS + (lane & 1) * 256 + s * 64 + g * 16);
      }
      f32x4 acc[4];
#pragma unroll
      for (int t = 0; t < 4; ++t) {
        acc[t] = f32x4{0, 0, 0, 0};
#pragma unroll
        for (int s = 0; s < 4; ++s) acc[t] = mfma(qf[s], kf[t * 4 + s], acc[t]);
      }
      float s0 = g == 0 ? acc[0][0] : g == 1 ? acc[1][0] : g == 2 ? acc[2][0] : acc[3][0];
      float s1 = g == 0 ? acc[0][1] : g == 1 ? acc[1][1] : g == 2 ? acc[2][1] : acc[3][1];
      s0 = rb(rb(s0) * 0.08838834764831845f);
      s1 = rb(rb(s1) * 0.08838834764831845f);
      mx0 = fmaxf(mx0, s0); mx1 = fmaxf(mx1, s1);
      *(LAS u16*)(L + L_SC + q * 256 + lane * 2) = (u16)(__float_as_uint(s0) >> 16);
      *(LAS u16*)(L + L_SC + q * 256 + 128 + lane * 2) = (u16)(__float_as_uint(s1) >> 16);
    }
    STAMP(3);
    {
      mx0 = wave_max(mx0); mx1 = wave_max(mx1);
      if (lane == 0) *(LAS u32x2*)(L + L_MX + cw * 8) = u32x2{__float_as_uint(mx0), __float_as_uint(mx1)};
      lds_add(F_MX, 1);
      lds_wait_ge(F, F_MX, 6u);
#pragma unroll
      for (int i = 0; i < 6; ++i) { mx0 = fmaxf(mx0, *(LAS float*)(L + L_MX + i * 8)); mx1 = fmaxf(mx1, *(LAS float*)(L + L_MX + i * 8 + 4)); }
      float l0 = 0.f, l1 = 0.f;
#pragma unroll 1
      for (int q = cw; q < 64; q += 6) {
        const float s0 = bf2f(*(LAS u16*)(L + L_SC + q * 256 + lane * 2)), s1 = bf2f(*(LAS u16*)(L + L_SC + q * 256 + 128 + lane * 2));
        const float p0 = __builtin_amdgcn_exp2f((s0 - mx0) * LOG2E), p1 = __builtin_amdgcn_exp2f((s1 - mx1) * LOG2E);
        l0 += p0; l1 += p1;
        *(LAS u16*)(L + L_SC + q * 256 + lane * 2) = (u16)f2bf(p0);
        *(LAS u16*)(L + L_SC + q * 256 + 128 + lane * 2) = (u16)f2bf(p1);
      }
      l0 = wave_sum(l0); l1 = wave_sum(l1);
      if (lane == 0) *(LAS u32x2*)(L + L_LW + cw * 8) = u32x2{__float_as_uint(l0), __float_as_uint(l1)};
      lds_add(F_L, 1);
    }
    f32x4 o[8];
#pragma unroll
    for (int cb = 0; cb < 8; ++cb) o[cb] = f32x4{0, 0, 0, 0};
    const u32 va0 = (8 * g + qp) * 256 + ((pp >> 1) << 4) + 8 * (pp & 1) + (((qp | ((g & 1) << 2)) << 1) << 4);
#pragma unroll 1
    for (int q = cw; q < 64; q += 6) {
      const int pos = (q + 64 + roff) & 7;
      if (isF && q >= 24 && q < 30) { lds_wait_ge(F, F_ALIVE, 1u); load_cbsb(); }
      const u32x4 pf0 = lds128(L, L_SC + q * 256 + (lane & 1) * 128 + g * 16), pf1 = lds128(L, L_SC + q * 256 + (lane & 1) * 128 + 64 + g * 16);
      lds_wait_ge(F, F_LANDED, Wtot + 16u * (u32)(q + 65));
      const u32 sb = L_RING + pos * 16384 + va0;
      u32x4 vf[8], vg2[8];
#pragma unroll
      for (int cb = 0; cb < 8; ++cb) vf[cb] = tr_frag(L, sb ^ (cb << 5), 1024);
#pragma unroll
      for (int cb = 0; cb < 8; ++cb) vg2[cb] = tr_frag(L, (sb ^ (cb << 5)) + 8192, 1024);
      WAIT_LGKM0();
      if (lane == 0) F[F_FREE + pos] = (u32)(q + 65);
#pragma unroll
      for (int cb = 0; cb < 8; ++cb) o[cb] = mfma(pf0, vf[cb], o[cb]);
#pragma unroll
      for (int cb = 0; cb < 8; ++cb) o[cb] = mfma(pf1, vg2[cb], o[cb]);
    }
    STAMP(4);
    if (g == 0) {
#pragma unroll
      for (int cb = 0; cb < 8; ++cb) *(LAS u32x2*)(L + L_ACT + cw * 1024 + (cb * 16 + r16) * 8) = u32x2{__float_as_uint(o[cb][0]), __float_as_uint(o[cb][1])};
    }
  }
  BARRIER();
  STAMP(8);
  if (w == 7) {
    f32x4 O = *(LAS f32x4*)(L + L_ACT + lane * 16);
#pragma unroll
    for (int i = 1; i < 6; ++i) O += *(LAS f32x4*)(L + L_ACT + i * 1024 + lane * 16);
    st128(rs, WS_OP + c * 1024 + lane * 16, u32x4{vld(O[0] * osA), vld(O[1] * osB), vld(O[2] * osA), vld(O[3] * osB)});
    STAMP(9);
  }
  if (isO) {
    constexpr int L_OS = L_RING;
    u32x4 ov[4];
    bool got[4] = {false, false, false, false};
    u32 spins = 0;
    for (;;) {
      MEMBAR();
#pragma unroll
      for (int i = 0; i < 4; ++i) if (!got[i]) ov[i] = ld128(rs, WS_OP + (xg + 8 * ((i >> 1) * 16 + 2 * w + (i & 1))) * 1024 + lane * 16);
      bool all = true;
#pragma unroll
      for (int i = 0; i < 4; ++i) if (!got[i]) {
        got[i] = __all((ov[i].x & ov[i].y & ov[i].z & ov[i].w & 1u) != 0u);
        all = all && got[i];
      }
      if (all || !spin_ok(F, spins)) break;
    }
#pragma unroll
    for (int hk = 0; hk < 2; ++hk) {
      const f32x4 s = __builtin_bit_cast(f32x4, ov[2 * hk]) + __builtin_bit_cast(f32x4, ov[2 * hk + 1]);
      *(LAS u32*)(L + L_OS + w * 1040 + hk * 512 + lane * 4) = pack2(s[0], s[2]);
      *(LAS u32*)(L + L_OS + w * 1040 + hk * 512 + 256 + lane * 4) = pack2(s[1], s[3]);
    }
    BARRIER();
    STAMP(10);
    if (w >= 1 && w <= 4) {
      const int ct = w - 1;
      const u32 ob = L_OS + (lane & 7) * 1040 + g * 16;
      u32x4 bfr[16];
#pragma unroll
      for (int s = 0; s < 16; ++s) bfr[s] = lds128(L, ob + s * 64);
      f32x4 a0 = {0, 0, 0, 0}, a1 = {0, 0, 0, 0}, a2 = {0, 0, 0, 0}, a3 = {0, 0, 0, 0};
#pragma unroll
      for (int s = 0; s < 16; s += 4) { a0 = mfma(wf[s], bfr[s], a0); a1 = mfma(wf[s + 1], bfr[s + 1], a1); a2 = mfma(wf[s + 2], bfr[s + 2], a2); a3 = mfma(wf[s + 3], bfr[s + 3], a3); }
      const f32x4 a = (a0 + a1) + (a2 + a3);
      if (r16 < 8) st128(rs, WS_WP + (xg * 144 + (u * 4 + ct) * 4 + g) * 128 + r16 * 16, u32x4{vld(a[0]), vld(a[1]), vld(a[2]), vld(a[3])});
      STAMP(11);
    }
  } else if (isF) {
    float x1r[4];
    if (w == 7) {
      u32x4 pv; u32 spins = 0;
      for (;;) {
        MEMBAR();
        pv = ld128(rs, WS_WP + ((lane >> 3) * 144 + f) * 128 + (lane & 7) * 16);
        if (__all((pv.x & pv.y & pv.z & pv.w & 1u) != 0u) || !spin_ok(F, spins)) break;
      }
      const float a0 = rb(sum_x8_16_32(__uint_as_float(pv.x))), a1 = rb(sum_x8_16_32(__uint_as_float(pv.y)));
      const float a2 = rb(sum_x8_16_32(__uint_as_float(pv.z))), a3 = rb(sum_x8_16_32(__uint_as_float(pv.w)));
      x1r[0] = rb(bflo(xp.x) + rb(bflo(gap.x) * a0)); x1r[1] = rb(bfhi(xp.x) + rb(bfhi(gap.x) * a1));
      x1r[2] = rb(bflo(xp.y) + rb(bflo(gap.y) * a2)); x1r[3] = rb(bfhi(xp.y) + rb(bfhi(gap.y) * a3));
      const u32x2 xo = {nosent(pack2(x1r[0], x1r[1])), nosent(pack2(x1r[2], x1r[3]))};
#pragma unroll
      for (int i = 0; i < REP_X / 8; ++i) st64(rs, WS_X1 + (i * 8 + (lane >> 3)) * 9216 + f * 64 + (lane & 7) * 8, xo);
      STAMP(10);
    }
    {
      const int xb = WS_X1 + (f % REP_X) * 9216;
      u32x4 xv, xw = {0u, 0u, 0u, 0u}; u32 spins = 0;
      for (;;) {
        MEMBAR();
        xv = ld128(rs, xb + tid * 16);
        if (w == 0) xw = ld128(rs, xb + 8192 + lane * 16);
        if (__all(xv.x != SENT && xv.y != SENT && xv.z != SENT && xv.w != SENT && xw.x != SENT && xw.y != SENT && xw.z != SENT && xw.w != SENT) || !spin_ok(F, spins)) break;
      }
      STAMP(16);
      {
        const u32 a = L_ACT + (tid & 3) * 2 * ACT_STRIDE + (tid >> 2) * 8;
        *(LAS u32x2*)(L + a) = u32x2{xv.x, xv.y}; *(LAS u32x2*)(L + a + ACT_STRIDE) = u32x2{xv.z, xv.w};
        if (w == 0) { *(LAS u32x2*)(L + a + 1024) = u32x2{xw.x, xw.y}; *(LAS u32x2*)(L + a + 1024 + ACT_STRIDE) = u32x2{xw.z, xw.w}; }
      }
      BARRIER();
      STAMP(17);
      u32 xr[5];
#pragma unroll
      for (int i = 0; i < 4; ++i) xr[i] = *(LAS u32*)(L + L_ACT + w * ACT_STRIDE + (lane + 64 * i) * 4);
      xr[4] = *(LAS u32*)(L + L_ACT + w * ACT_STRIDE + (256 + (lane & 31)) * 4);
      float v[10];
#pragma unroll
      for (int i = 0; i < 5; ++i) { v[2 * i] = bflo(xr[i]); v[2 * i + 1] = bfhi(xr[i]); }
      float s = (v[0] + v[1]) + (v[2] + v[3]) + ((v[4] + v[5]) + (v[6] + v[7]));
      if (lane < 32) s += v[8] + v[9];
      const float mean = wave_sum(s) * (1.0f / DM);
      float q2 = 0.f, q3 = 0.f;
#pragma unroll
      for (int i = 0; i < 4; ++i) { const float d0 = v[2 * i] - mean, d1 = v[2 * i + 1] - mean; q2 += d0 * d0; q3 += d1 * d1; }
      if (lane < 32) { const float d0 = v[8] - mean, d1 = v[9] - mean; q2 += d0 * d0; q3 += d1 * d1; }
      const float var = wave_sum(q2 + q3) * (1.0f / DM);
      const float mean_r = rb(mean);
      const float rinv = __builtin_amdgcn_rcpf(rb(__builtin_amdgcn_sqrtf(rb(rb(var) + 1e-5f))));
#pragma unroll
      for (int i = 0; i < 5; ++i) {
        const u32 dp = pack2(v[2 * i] - mean_r, v[2 * i + 1] - mean_r);
        const u32 np = pack2(bflo(dp) * rinv, bfhi(dp) * rinv);
        const u32 tp = pack2(bflo(np) * bflo(cbp[i]), bfhi(np) * bfhi(cbp[i]));
        const u32 hp = pack2(bflo(tp) + bflo(sbp[i]), bfhi(tp) + bfhi(sbp[i]));
        if (i < 4) *(LAS u32*)(L + L_ACT + w * ACT_STRIDE + (lane + 64 * i) * 4) = hp;
        else if (lane < 32) *(LAS u32*)(L + L_ACT + w * ACT_STRIDE + (256 + lane) * 4) = hp;
      }
      STAMP(18);
    }
    BARRIER();
    STAMP(11);
    constexpr int L_R2 = L_RING;
    constexpr int L_HH = L_RING + 8192;
    if (w == 7) {
      BARRIER();
      BARRIER();
      const int off = WS_FP + (lane >> 3) * 18432 + f * 128 + (lane & 7) * 16;
      u32x4 e[18]; u32 spins = 0;
      for (;;) {
        MEMBAR();
#pragma unroll
        for (int i = 0; i < 18; ++i) e[i] = ld128(rs, off + i * 8 * 18432);
        u32 a = 1u;
#pragma unroll
        for (int i = 0; i < 18; ++i) a &= e[i].x & e[i].y & e[i].z & e[i].w;
        if (__all(a != 0u) || !spin_ok(F, spins)) break;
      }
      STAMP(23);
      f32x4 s0 = __builtin_bit_cast(f32x4, e[0]), s1 = __builtin_bit_cast(f32x4, e[1]), s2 = __builtin_bit_cast(f32x4, e[2]);
#pragma unroll
      for (int i = 3; i < 18; i += 3) { s0 += __builtin_bit_cast(f32x4, e[i]); s1 += __builtin_bit_cast(f32x4, e[i + 1]); s2 += __builtin_bit_cast(f32x4, e[i + 2]); }
      const f32x4 s = (s0 + s1) + s2;
      const float d0 = rb(sum_x8_16_32(s[0])), d1 = rb(sum_x8_16_32(s[1])), d2 = rb(sum_x8_16_32(s[2])), d3 = rb(sum_x8_16_32(s[3]));
      const float o0 = x1r[0] + rb(bflo(gbp.x) * d0), o1 = x1r[1] + rb(bfhi(gbp.x) * d1);
      const float o2 = x1r[2] + rb(bflo(gbp.y) * d2), o3 = x1r[3] + rb(bfhi(gbp.y) * d3);
      if (lane < 8) *(GAS u32x2*)(p.out + lane * DM + f * 4) = u32x2{pack2(o0, o1), pack2(o2, o3)};
      STAMP(13);
    } else {
      const int cw = w - 1;
      if (w >= 1) {
        const int kt = cw >> 1;
        const u32 hb = L_ACT + (lane & 7) * ACT_STRIDE + kt * 384 + g * 16;
        u32x4 bfr[6];
#pragma unroll
        for (int s = 0; s < 6; ++s) bfr[s] = lds128(L, hb + s * 64);
        f32x4 a0 = {0, 0, 0, 0}, a1 = {0, 0, 0, 0}, a2 = {0, 0, 0, 0}, a3 = {0, 0, 0, 0};
#pragma unroll
        for (int s = 0; s < 6; s += 2) { a0 = mfma(wf[2 * s], bfr[s], a0); a1 = mfma(wf[2 * s + 1], bfr[s], a1); a2 = mfma(wf[2 * s + 2], bfr[s + 1], a2); a3 = mfma(wf[2 * s + 3], bfr[s + 1], a3); }
        a0 += a2; a1 += a3;
        if (r16 < 8) { *(LAS f32x4*)(L + L_R2 + cw * 1024 + (g * 8 + r16) * 16) = a0; *(LAS f32x4*)(L + L_R2 + cw * 1024 + 512 + (g * 8 + r16) * 16) = a1; }
      }
      STAMP(19);
      BARRIER();
      STAMP(20);
      if (w < 4) {
        const int ct = w >> 1, e = (w & 1) * 64 + lane;
        const u32 a = L_R2 + ct * 512 + e * 4;
        const float gg = rb(*(LAS float*)(L + a) + *(LAS float*)(L + a + 2048) + *(LAS float*)(L + a + 4096));
        const float uu = rb(*(LAS float*)(L + a + 1024) + *(LAS float*)(L + a + 3072) + *(LAS float*)(L + a + 5120));
        const float inner = 0.7978845608028654f * (gg + 0.044715f * gg * gg * gg);
        const float th = 1.0f - 2.0f * __builtin_amdgcn_rcpf(1.0f + __expf(2.0f * inner));
        const float ge = rb(0.5f * gg * (1.0f + th));
        const int ls = e >> 2, rr = e & 3, b_ = ls & 7, g_ = ls >> 3;
        *(LAS u16*)(L + L_HH + b_ * 80 + (ct * 16 + g_ * 4 + rr) * 2) = (u16)f2bf(ge * uu);
      }
      STAMP(21);
      BARRIER();
      STAMP(22);
      if (w >= 1) {
        const u32x4 bfr = lds128(L, L_HH + (lane & 7) * 80 + g * 16);
        f32x4 a[6];
#pragma unroll
        for (int t = 0; t < 6; ++t) a[t] = mfma(wf[12 + t], bfr, f32x4{0, 0, 0, 0});
#pragma unroll
        for (int t = 0; t < 6; ++t)
          if (r16 < 8) st128(rs, WS_FP + f * 18432 + ((cw * 6 + t) * 4 + g) * 128 + r16 * 16, u32x4{vld(a[t][0]), vld(a[t][1]), vld(a[t][2]), vld(a[t][3])});
      }
      STAMP(12);
    }
  }
  if (w == 7) {
    const u32x4 a = ld128(rs, 6144 + lane * 32), b2 = ld128(rs, 6144 + lane * 32 + 16);
    const u32 now = (u32)__builtin_amdgcn_s_memrealtime();
    u32 d0 = now - a.x, d1 = now - a.z, d2 = now - b2.x, d3 = now - b2.z;
    u32 d = max(max(d0, d1), max(d2, d3));
    for (int o = 32; o; o >>= 1) d = max(d, (u32)__shfl_xor((int)d, o));
    const u32 lim = PADG * 100u;
    while ((u32)__builtin_amdgcn_s_memrealtime() - now + d < lim) __builtin_amdgcn_s_sleep(2);
  }
}

}

extern "C" void kernel_launch(void* const* d_in, const int* in_sizes, int n_in, void* d_out, int out_size, void* d_ws, size_t ws_size,
                              hipStream_t stream) {
  (void)in_sizes; (void)n_in; (void)out_size; (void)ws_size;
  Params p;
  p.x = (const u16*)d_in[0];
  p.kc = (const unsigned char*)d_in[1];
  p.vc = (const unsigned char*)d_in[2];
  p.wq = (const unsigned char*)d_in[3];
  p.wo = (const unsigned char*)d_in[4];
  p.wg = (const unsigned char*)d_in[6];
  p.wu = (const unsigned char*)d_in[7];
  p.wd = (const unsigned char*)d_in[8];
  p.c = (const u16*)d_in[9];
  p.wmod = (const unsigned char*)d_in[10];
  p.out = (u16*)d_out;
  p.ws = (unsigned char*)d_ws;
  (void)hipFuncSetAttribute((const void*)fused_block, hipFuncAttributeMaxDynamicSharedMemorySize, LDS_BYTES);
  hipLaunchKernelGGL(fused_block, dim3(NCU), dim3(NT), LDS_BYTES, stream, p);
}
```

```cpp
#include <hip/hip_runtime.h>
#include <stdint.h>
#include <type_traits>
#pragma clang fp contract(off)

#define INFL 24
#define INFLW 256
#define HPOLL_SLEEP 2
#define QPOLL_SLEEP 2
#define PACE 0
#define PACE_KP 3
#define SCHED_Q0 8
#define SCHED_L1 30
#define SCHED_L2 100
#define SCHED_L3 0
#define DELAY_C 12
#define DELAY_E 28
#define SHIFT_O 1
#define SHIFT_H 2
#define PACE_LINES 16
#define PACE_SLEEP 8
#define PACE_MIN 16
#define PACE_MAX 32
#define AUX_K "nt"
#define AUX_V "nt"
#define AUX_W "nt"
#define STAMPS 0

#define LAS __attribute__((address_space(3)))
#define GAS __attribute__((address_space(1)))
typedef unsigned u32;
typedef unsigned long long u64;
typedef unsigned short u16;
typedef u32 u32x4 __attribute__((ext_vector_type(4)));
typedef u32 u32x2 __attribute__((ext_vector_type(2)));
typedef float f32x4 __attribute__((ext_vector_type(4)));
typedef __bf16 bf16x8 __attribute__((ext_vector_type(8)));
typedef short s16x4 __attribute__((ext_vector_type(4)));

namespace {

constexpr int NB = 8, DM = 576, HKV = 16, SK = 8192, DH = 128, DF = 4608;
constexpr int NCU = 256, NT = 512;

constexpr int REP_H = 8, REP_X = 8;
constexpr int WS_CTRL = 0;
constexpr int WS_ALIVE = 4096;
constexpr int WS_PROG = 6144;
constexpr int WS_ST = 8192;
constexpr int WS_MODT = WS_ST + 4096;
constexpr int WS_X1 = WS_MODT + 36864;
constexpr int WS_H = WS_X1 + REP_X * 9216;
constexpr int WS_Q = WS_H + REP_H * 18432;
constexpr int WS_OP = WS_Q + 131072;
constexpr int WS_WP = WS_OP + 262144;
constexpr int WS_FP = WS_WP + 147456;
constexpr int WS_END = WS_FP + 144 * 9216;
constexpr int WS_DBG = 4 << 20;
static_assert(WS_END <= WS_DBG, "ws");

constexpr int L_RING = 0;
constexpr int L_SC = 131072;
constexpr int L_ACT = L_SC + 16896;
constexpr int L_QS = L_ACT + 9344;
constexpr int L_RED = L_QS + 512;
constexpr int L_STAT = L_RED + 3072;
constexpr int L_MX = L_STAT + 64;
constexpr int L_LW = L_MX + 64;
constexpr int L_FLG = L_LW + 64;
constexpr int L_P2 = L_SC + 8192;
constexpr int LDS_BYTES = 163840;
static_assert(L_FLG + 256 <= LDS_BYTES, "lds");
constexpr int ACT_STRIDE = 1168;
constexpr int F_LANDED = 0, F_FREE = 1  , F_WREL = 9  , F_SC = 12, F_P1 = 13, F_INIT = 14, F_Q = 15, F_ABORT = 16, F_ALIVE = 17, F_P2 = 18, F_MX = 19, F_L = 20, F_INFL = 21, F_X1 = 22;

constexpr u32 SENT = 0xFFFFFFFFu;
constexpr u32 LDS_SPIN_CAP = 1u << 21;
constexpr u32 GLB_SPIN_CAP = 1u << 17;
constexpr float LOG2E = 1.4426950408889634f;

struct Params {
  const u16* x; const unsigned char* kc; const unsigned char* vc; const unsigned char* wq; const unsigned char* wo;
  const unsigned char* wg; const unsigned char* wu; const unsigned char* wd; const u16* c; const unsigned char* wmod;
  u16* out; unsigned char* ws;
};

__device__ __forceinline__ float bf2f(u32 v16) { return __uint_as_float(v16 << 16); }
__device__ __forceinline__ float bflo(u32 d) { return __uint_as_float(d << 16); }
__device__ __forceinline__ float bfhi(u32 d) { return __uint_as_float(d & 0xffff0000u); }
__device__ __forceinline__ float rb(float f) { return (float)(__bf16)f; }
__device__ __forceinline__ u32 f2bf(float f) { return (u32)__builtin_bit_cast(u16, (__bf16)f); }
typedef float f32x2 __attribute__((ext_vector_type(2)));
typedef __bf16 bf16x2 __attribute__((ext_vector_type(2)));
__device__ __forceinline__ u32 pack2(float lo, float hi) { return __builtin_bit_cast(u32, __builtin_convertvector(f32x2{lo, hi}, bf16x2)); }
__device__ __forceinline__ u32 nosent(u32 p) { return p == SENT ? 0xFFFEFFFFu : p; }
__device__ __forceinline__ u32 vld(float f) { return __float_as_uint(f) | 1u; }
__device__ __forceinline__ f32x4 mfma(u32x4 a, u32x4 b, f32x4 c) {
  return __builtin_amdgcn_mfma_f32_16x16x32_bf16(__builtin_bit_cast(bf16x8, a), __builtin_bit_cast(bf16x8, b), c, 0, 0, 0);
}
__device__ __forceinline__ u32x4 mk_srd(const void* p, u32 bytes) {
  const u64 a = (u64)p;
  u32x4 s;
  s.x = __builtin_amdgcn_readfirstlane((u32)a);
  s.y = __builtin_amdgcn_readfirstlane((u32)(a >> 32) & 0xffffu);
  s.z = __builtin_amdgcn_readfirstlane(bytes);
  s.w = 0x00020000u;
  return s;
}
#define MEMBAR() asm volatile("" ::: "memory")
#define WAIT_LGKM0() asm volatile("s_waitcnt lgkmcnt(0)" ::: "memory")
#define WAIT_VM0() asm volatile("s_waitcnt vmcnt(0)" ::: "memory")
#define BARRIER() do { asm volatile("s_waitcnt lgkmcnt(0)" ::: "memory"); __builtin_amdgcn_s_barrier(); asm volatile("" ::: "memory"); } while (0)

#define DMA1(AUX, srd, voff, soff, ldsaddr)                                                                      \
  do { u32 keep_;                                                                                                \
    asm volatile("s_mov_b32 %0, m0\n\ts_mov_b32 m0, %1\n\ts_nop 0\n\t"                                           \
                 "buffer_load_dwordx4 %4, %2, %3 offen " AUX " lds\n\ts_mov_b32 m0, %0"                          \
                 : "=&s"(keep_) : "s"(ldsaddr), "s"(srd), "s"(soff), "v"(voff) : "memory"); } while (0)
#define DMA4(AUX, srd, v0, v1, v2, v3, soff, ldsaddr)                                                            \
  do { u32 keep_;                                                                                                \
    asm volatile("s_mov_b32 %0, m0\n\ts_mov_b32 m0, %1\n\ts_nop 0\n\t"                                           \
                 "buffer_load_dwordx4 %4, %2, %3 offen " AUX " lds\n\t"                                          \
                 "buffer_load_dwordx4 %5, %2, %3 offen offset:1024 " AUX " lds\n\t"                              \
                 "buffer_load_dwordx4 %6, %2, %3 offen offset:2048 " AUX " lds\n\t"                              \
                 "buffer_load_dwordx4 %7, %2, %3 offen offset:3072 " AUX " lds\n\ts_mov_b32 m0, %0"              \
                 : "=&s"(keep_) : "s"(ldsaddr), "s"(srd), "s"(soff), "v"(v0), "v"(v1), "v"(v2), "v"(v3) : "memory"); } while (0)

template <int CTRL> __device__ __forceinline__ float dpp(float x) {
  return __builtin_bit_cast(float, __builtin_amdgcn_mov_dpp(__builtin_bit_cast(int, x), CTRL, 0xf, 0xf, true));
}
constexpr int ROR8 = 0x128, ROR4 = 0x124, ROR2 = 0x122, ROR1 = 0x121;
__device__ __forceinline__ float xrow_max(float x) {
  auto s = __builtin_amdgcn_permlane16_swap(__float_as_uint(x), __float_as_uint(x), false, false);
  x = fmaxf(__uint_as_float(s[0]), __uint_as_float(s[1]));
  auto t = __builtin_amdgcn_permlane32_swap(__float_as_uint(x), __float_as_uint(x), false, false);
  return fmaxf(__uint_as_float(t[0]), __uint_as_float(t[1]));
}
__device__ __forceinline__ float xrow_sum(float x) {
  auto s = __builtin_amdgcn_permlane16_swap(__float_as_uint(x), __float_as_uint(x), false, false);
  x = __uint_as_float(s[0]) + __uint_as_float(s[1]);
  auto t = __builtin_amdgcn_permlane32_swap(__float_as_uint(x), __float_as_uint(x), false, false);
  return __uint_as_float(t[0]) + __uint_as_float(t[1]);
}
__device__ __forceinline__ float wave_max(float x) {
  x = fmaxf(x, dpp<ROR8>(x)); x = fmaxf(x, dpp<ROR4>(x)); x = fmaxf(x, dpp<ROR2>(x)); x = fmaxf(x, dpp<ROR1>(x));
  return xrow_max(x);
}
__device__ __forceinline__ float wave_sum(float x) {
  x += dpp<ROR8>(x); x += dpp<ROR4>(x); x += dpp<ROR2>(x); x += dpp<ROR1>(x);
  return xrow_sum(x);
}
__device__ __forceinline__ float sum_x8_16_32(float x) {
  x += dpp<ROR8>(x);
  return xrow_sum(x);
}

typedef volatile LAS u32* flagp;
__device__ __forceinline__ void lds_wait_ge(flagp F, int idx, u32 need) {
  u32 spins = 0;
  while (F[idx] < need) {
    __builtin_amdgcn_s_sleep(1);
    if ((++spins & 63u) == 0u) { if (F[F_ABORT] != 0u) break; if (spins > LDS_SPIN_CAP) { F[F_ABORT] = 1u; break; } }
  }
  MEMBAR();
}
__device__ __forceinline__ void lds_add(int idx, u32 n) {
  const u32 a = L_FLG + idx * 4;
  asm volatile("s_waitcnt lgkmcnt(0)\n\ts_mov_b64 vcc, exec\n\ts_mov_b64 exec, 1\n\tds_add_u32 %0, %1\n\ts_mov_b64 exec, vcc"
               :: "v"(a), "v"(n) : "vcc", "memory");
}
__device__ __forceinline__ bool spin_ok(flagp F, u32& spins) {
  if (++spins > GLB_SPIN_CAP) { F[F_ABORT] = 2u; return false; }
  if ((spins & 255u) == 0u && F[F_ABORT] != 0u) return false;
  return true;
}

constexpr int SC1 = 16;
typedef __amdgpu_buffer_rsrc_t rsrc_t;
__device__ __forceinline__ u32x4 ld128(rsrc_t r, int off) { return __builtin_amdgcn_raw_buffer_load_b128(r, off, 0, SC1); }
__device__ __forceinline__ u32x2 ld64(rsrc_t r, int off) { return __builtin_amdgcn_raw_buffer_load_b64(r, off, 0, SC1); }
__device__ __forceinline__ u32 ld32(rsrc_t r, int off) { return __builtin_amdgcn_raw_buffer_load_b32(r, off, 0, SC1); }
__device__ __forceinline__ void st128(rsrc_t r, int off, u32x4 v) { __builtin_amdgcn_raw_buffer_store_b128(v, r, off, 0, SC1); }
__device__ __forceinline__ void st64(rsrc_t r, int off, u32x2 v) { __builtin_amdgcn_raw_buffer_store_b64(v, r, off, 0, SC1); }

__device__ __forceinline__ u32x4 tr_frag(LAS unsigned char* L, u32 addr, u32 stride4) {
  const s16x4 a = __builtin_amdgcn_ds_read_tr16_b64_v4i16((LAS s16x4*)(L + addr));
  const s16x4 b = __builtin_amdgcn_ds_read_tr16_b64_v4i16((LAS s16x4*)(L + addr + stride4));
  const u32x2 a2 = __builtin_bit_cast(u32x2, a), b2 = __builtin_bit_cast(u32x2, b);
  return u32x4{a2.x, a2.y, b2.x, b2.y};
}
__device__ __forceinline__ u32x4 lds128(LAS unsigned char* L, u32 addr) { return *(LAS u32x4*)(L + addr); }

#define STAMP(i) do {} while (0)

__device__ __forceinline__ void ln_stats(const float (&v)[10], bool tail, float& mean_r, float& sd_r) {
  float s = 0.f;
#pragma unroll
  for (int i = 0; i < 8; ++i) s += v[i];
  if (tail) s += v[8] + v[9];
  const float mean = wave_sum(s) * (1.0f / DM);
  float q = 0.f;
#pragma unroll
  for (int i = 0; i < 8; ++i) { const float d = v[i] - mean; q += d * d; }
  if (tail) { const float d0 = v[8] - mean, d1 = v[9] - mean; q += d0 * d0 + d1 * d1; }
  const float var = wave_sum(q) * (1.0f / DM);
  mean_r = rb(mean);
  sd_r = rb(sqrtf(rb(rb(var) + 1e-5f)));
}
__device__ __forceinline__ float ln_mod(float x, float mean_r, float sd_r, float scale, float shift) {
  const float n = rb(rb(x - mean_r) / sd_r);
  return rb(rb(n * rb(1.0f + scale)) + shift);
}

__global__ void __launch_bounds__(NT, 2) fused_block(Params p) {
  extern __shared__ __attribute__((aligned(16))) unsigned char smem[];
  LAS unsigned char* L = (LAS unsigned char*)smem;
  flagp F = (flagp)(L + L_FLG);
  const int tid = threadIdx.x, lane = tid & 63, w = __builtin_amdgcn_readfirstlane(tid >> 6);
  STAMP(0);
  const int c = blockIdx.x, xg = c & 7, ci = c >> 3;
  const int hkv = 2 * xg + (ci >> 4), cj = ci & 15, bb = cj >> 1, half = cj & 1;
  const int pj = ci >> 1;
  const bool isF = half == 0 || pj >= 14, isO = !isF && pj < 9;
  const int hu = xg * 5 + (pj - 9);
  const bool isH = !isF && !isO && hu < 36;
  const int f = xg * 18 + (half == 0 ? pj : pj + 2);
  const int u = pj;
  const bool isQ = isO && u < 8;
  const int shift = pj < 9 ? (pj < 8 ? SHIFT_O : 0) : pj < 14 ? (xg * 5 + (pj - 9) < 36 ? SHIFT_H : 0) : 0;
  const int nsl = half == 0 ? 64 + shift : 64 - shift;
  const int key0 = half == 0 ? 0 : (64 + shift) * 64;
  const u32 Wtot = isF ? 108u : isO ? 136u : isH ? 108u : 0u;
  const int roff = (isF || isH) ? 7 : 0;
  const int g = lane >> 4, r16 = lane & 15, qp = (lane & 15) >> 2, pp = lane & 3;
  const rsrc_t rs = __builtin_amdgcn_make_buffer_rsrc((void*)p.ws, 0, WS_END, 0x00020000);

  auto read_epoch = [&]() {
    u64 e = *(const __attribute__((address_space(4))) u64*)(p.ws + WS_CTRL) + 1ull;
    asm volatile("" : "+s"(e));
    return e;
  };

  u32x4 wf[18];
  u32 cbp[5], sbp[5];
  u32x2 gap, gbp, xp;
  float osA = 0.f, osB = 0.f;

  auto load_cbsb = [&]() {
    u32 spins = 0;
    for (;;) {
      MEMBAR();
      const int base = WS_MODT + w * 4608;
#pragma unroll
      for (int i = 0; i < 4; ++i) { sbp[i] = ld32(rs, base + (288 + lane + 64 * i) * 4); cbp[i] = ld32(rs, base + (576 + lane + 64 * i) * 4); }
      sbp[4] = ld32(rs, base + (288 + 256 + (lane & 31)) * 4); cbp[4] = ld32(rs, base + (576 + 256 + (lane & 31)) * 4);
      bool ok = true;
#pragma unroll
      for (int i = 0; i < 5; ++i) ok = ok && sbp[i] != SENT && cbp[i] != SENT;
      if (__all(ok) || !spin_ok(F, spins)) break;
    }
#pragma unroll
    for (int i = 0; i < 5; ++i) cbp[i] = pack2(1.0f + bflo(cbp[i]), 1.0f + bfhi(cbp[i]));
  };
  auto q_partial = [&](int kt, u32 tag, f32x4 (&acc)[4]) {
    lds_wait_ge(F, F_LANDED, 72u);
    u32x4 pq[24];
    const u32 a0 = L_RING + (kt * 192 + 8 * g + qp) * 128 + 8 * pp;
#pragma unroll
    for (int s = 0; s < 6; ++s)
#pragma unroll
      for (int ct = 0; ct < 4; ++ct) pq[s * 4 + ct] = tr_frag(L, a0 + s * 4096 + ct * 32, 4 * 128);
    lds_add(F_WREL + 0, 1);
    STAMP(6);
    u32x4 hv[6];
    u32 spins = 0;
    const int hb = WS_H + (c % REP_H) * 18432 + kt * 768;
    int go[6], lo[6];
#pragma unroll
    for (int i = 0; i < 6; ++i) { const int e = i * 64 + lane, row = e / 48, j = e % 48; go[i] = hb + row * 2304 + j * 16; lo[i] = L_ACT + row * ACT_STRIDE + kt * 384 + j * 8; }
    for (;;) {
      MEMBAR();
#pragma unroll
      for (int i = 0; i < 6; ++i) hv[i] = ld128(rs, go[i]);
      bool ok = true;
#pragma unroll
      for (int i = 0; i < 6; ++i) ok = ok && hv[i].y == tag && hv[i].w == tag;
      if (__all(ok) || !spin_ok(F, spins)) break;
      __builtin_amdgcn_s_sleep(HPOLL_SLEEP);
    }
#pragma unroll
    for (int i = 0; i < 6; ++i) *(LAS u32x2*)(L + lo[i]) = u32x2{hv[i].x, hv[i].z};
    WAIT_LGKM0();
    STAMP(3);
    const u32 ab = L_ACT + (lane & 7) * ACT_STRIDE + kt * 384 + g * 16;
#pragma unroll
    for (int ct = 0; ct < 4; ++ct) acc[ct] = f32x4{0, 0, 0, 0};
#pragma unroll
    for (int s = 0; s < 6; ++s) {
      const u32x4 bfr = lds128(L, ab + s * 64);
#pragma unroll
      for (int ct = 0; ct < 4; ++ct) acc[ct] = mfma(pq[s * 4 + ct], bfr, acc[ct]);
    }
  };

  if (w == 0) {
    __builtin_amdgcn_s_setprio(3);
    if (lane < 32) F[lane] = 0u;
    u32 n = 0;
    auto run = [&](auto inflp, const u32x4& srd, u32 voff, u32 step, u32 first, u32 cnt) {
      constexpr int NP = decltype(inflp)::value;
#pragma unroll 1
      for (u32 s = first; s < cnt; ++s) {
        DMA1(AUX_W, srd, voff, s * step, (u32)(L_RING + n * 1024));
        ++n;
        asm volatile("s_waitcnt vmcnt(%0)" :: "i"(NP - 1) : "memory");
        if (n > (u32)(NP - 1)) F[F_LANDED] = n - (u32)(NP - 1);
      }
    };
    typedef std::integral_constant<int, INFLW / 32> P32;
    typedef std::integral_constant<int, INFLW / 16> P16;
    typedef std::integral_constant<int, (INFLW / 8 > 32 ? 32 : INFLW / 8)> P8;
    const u32 vg = (lane >> 2) * 9216 + (lane & 3) * 16, vm32 = (lane >> 1) * 6912 + (lane & 1) * 16;
    const u32 v128o = (lane >> 3) * 1152 + (lane & 7) * 16;
    u32x4 srd0; u32 v0, step0, cnt0;
    if (isF) { srd0 = mk_srd(p.wg + f * 64, DM * 9216 - f * 64); v0 = vg; step0 = 16 * 9216; cnt0 = 36; }
    else if (isQ) { srd0 = mk_srd(p.wq + (xg * 512 + u * 64) * 2, DM * 8192 - (xg * 512 + u * 64) * 2); v0 = (lane >> 3) * 8192 + (lane & 7) * 16; step0 = 8 * 8192; cnt0 = 72; }
    else if (isH) { srd0 = mk_srd(p.wmod + hu * 32, DM * 6912 - hu * 32); v0 = vm32; step0 = 32 * 6912; cnt0 = 18; }
    else { srd0 = mk_srd(p.wq, 0); v0 = 0; step0 = 0; cnt0 = 0; }
    const u32 pre = cnt0 < (u32)P32::value ? cnt0 : (u32)P32::value;
#pragma unroll 1
    for (u32 s = 0; s < pre; ++s) { DMA1(AUX_W, srd0, v0, s * step0, (u32)(L_RING + n * 1024)); ++n; }
    BARRIER();
    STAMP(4);
    if (isF) {
      run(P16(), srd0, v0, step0, pre, 36);
      run(P16(), mk_srd(p.wu + f * 64, DM * 9216 - f * 64), vg, 16 * 9216, 0, 36);
      run(P8(), mk_srd(p.wd + f * 36864, 36864), lane * 16, 1024, 0, 36);
    } else if (isO) {
      if (isQ) run(P8(), srd0, v0, step0, pre, 72); else n = 72;
      run(P8(), mk_srd(p.wo + xg * 512 * 1152 + u * 128, 512 * 1152 - u * 128), v128o, 8 * 1152, 0, 64);
    } else if (isH) {
      run(P32(), srd0, v0, step0, pre, 18);
      run(P32(), mk_srd(p.wmod + 1152 + hu * 32, DM * 6912 - 1152 - hu * 32), vm32, 32 * 6912, 0, 18);
      run(P8(), mk_srd(p.wmod + 2304 + hu * 128, DM * 6912 - 2304 - hu * 128), (lane >> 3) * 6912 + (lane & 7) * 16, 8 * 6912, 0, 72);
    }
    STAMP(1);
    const size_t kvoff = ((size_t)(bb * HKV + hkv) * SK + (size_t)key0) * 256;
    const u32x4 srd_k = mk_srd(p.kc + kvoff, (u32)nsl * 16384u), srd_v = mk_srd(p.vc + kvoff, (u32)nsl * 16384u);
    u32 vk[4], vv[2];
#pragma unroll
    for (int j = 0; j < 4; ++j) vk[j] = g * 256 + ((r16 ^ (4 * j + g)) << 4);
#pragma unroll
    for (int j = 0; j < 2; ++j) vv[j] = g * 256 + ((r16 ^ ((g | (j << 2)) << 1)) << 4);
    const u32 wneed = isF ? 0x03332211u : isO ? 0x33221111u : isH ? 0x02222211u : 0u;
    auto wait_window = [&](u32 N) {
      asm volatile("s_waitcnt vmcnt(36)\n\ts_cmp_ge_u32 %0, 36\n\ts_cbranch_scc1 9f\n\t"
                   "s_waitcnt vmcnt(32)\n\ts_cmp_ge_u32 %0, 32\n\ts_cbranch_scc1 9f\n\t"
                   "s_waitcnt vmcnt(28)\n\ts_cmp_ge_u32 %0, 28\n\ts_cbranch_scc1 9f\n\t"
                   "s_waitcnt vmcnt(24)\n\ts_cmp_ge_u32 %0, 24\n\ts_cbranch_scc1 9f\n\t"
                   "s_waitcnt vmcnt(20)\n\ts_cmp_ge_u32 %0, 20\n\ts_cbranch_scc1 9f\n\t"
                   "s_waitcnt vmcnt(16)\n\ts_cmp_ge_u32 %0, 16\n\ts_cbranch_scc1 9f\n\t"
                   "s_waitcnt vmcnt(12)\n9:" :: "s"(N) : "scc", "memory");
    };
    u32 fr_next = 0, in_next = 0; int pub = (int)n - 31 > 0 ? (int)n - 31 : 0;
#pragma unroll 1
    for (int q = 0; q < 2 * nsl; ++q) {
      const int pos = (q + roff) & 7;
      if (q >= 8) {
        if (fr_next < (u32)(q - 7) && F[F_FREE + pos] < (u32)(q - 7)) {
          WAIT_VM0(); F[F_LANDED] = n; pub = (int)n; lds_wait_ge(F, F_FREE + pos, (u32)(q - 7));
        }
      } else {
        const int nb = (wneed >> (4 * pos)) & 15;
#pragma unroll 1
        for (int i = 0; i < nb; ++i)
          if (F[F_WREL + i] < 7u) { WAIT_VM0(); F[F_LANDED] = n; pub = (int)n; lds_wait_ge(F, F_WREL + i, 7u); }
      }
      u32 N = __builtin_amdgcn_readfirstlane(in_next ? in_next : (u32)(INFL - 4));
      fr_next = F[F_FREE + ((pos + 1) & 7)];
      in_next = F[F_INFL];
      const u32 lb = L_RING + pos * 16384;
      const bool isk = q < nsl;
      const u32x4 srd = isk ? srd_k : srd_v;
      const u32 v0_ = isk ? vk[0] : vv[0], v1_ = isk ? vk[1] : vv[0], v2_ = isk ? vk[2] : vv[1], v3_ = isk ? vk[3] : vv[1];
      const u32 so = (u32)(isk ? q : q - nsl) * 16384u;
#pragma unroll
      for (int gq = 0; gq < 4; ++gq) {
        DMA4(AUX_K, srd, v0_, v1_, v2_, v3_, so + gq * 4096, lb + gq * 4096);
        n += 4;
        wait_window(N);
        pub = max(pub, (int)n - (int)N); F[F_LANDED] = (u32)pub;
      }
    }
    WAIT_VM0();
    F[F_LANDED] = n;
    STAMP(3);
    __builtin_amdgcn_s_setprio(0);
    if (isF) { lds_wait_ge(F, F_ALIVE, 1u); load_cbsb(); }
  } else if (w == 7) {
    const u64 epoch = read_epoch();
    const u32 tag = (u32)epoch;
    const u32x4 z4 = {0u, 0u, 0u, 0u};
    const u32x2 s2 = {SENT, SENT};
    if (isH) {
#pragma unroll
      for (int i = 0; i < 2; ++i) st64(rs, WS_MODT + (lane & 7) * 4608 + hu * 128 + (2 * i + (r16 >> 3)) * 32 + g * 8, s2);
    }
    BARRIER();
    st128(rs, WS_OP + c * 1024 + lane * 16, z4);
    if (lane == 0) st128(rs, WS_ST + c * 16, z4);
    if (isO) {
#pragma unroll
      for (int i = 0; i < 2; ++i) st128(rs, WS_WP + (xg * 144 + u * 16) * 128 + i * 1024 + lane * 16, z4);
    }
    if (isF) {
      st64(rs, WS_X1 + (lane >> 3) * 9216 + f * 64 + (lane & 7) * 8, s2);
#pragma unroll 1
      for (int i = 0; i < 9; ++i) st128(rs, WS_FP + f * 9216 + i * 1024 + lane * 16, u32x4{SENT, SENT, SENT, SENT});
    }
    WAIT_VM0();
    F[F_INIT] = 1u;
    if (lane == 0) __hip_atomic_store((GAS u64*)(p.ws + WS_ALIVE) + c, epoch, __ATOMIC_RELAXED, __HIP_MEMORY_SCOPE_AGENT);
    STAMP(1);
    if (isQ) {
      f32x4 acc[4];
      q_partial(2, tag, acc);
      lds_add(F_WREL + 1, 1); lds_add(F_WREL + 2, 1);
      lds_wait_ge(F, F_P2, 2u);
      const int ls = (g * 8 + (lane & 7)) * 16;
#pragma unroll
      for (int ct = 0; ct < 4; ++ct) {
        const f32x4 a = (*(LAS f32x4*)(L + L_P2 + ct * 512 + ls) + *(LAS f32x4*)(L + L_P2 + 2048 + ct * 512 + ls)) + acc[ct];
        if (r16 < 8) st128(rs, WS_Q + ((2 * xg + (u >> 2)) * 8 + r16) * 1024 + ((u & 3) * 16 + ct * 4 + g) * 16, u32x4{pack2(a[0], a[1]), tag, pack2(a[2], a[3]), tag});
      }
      STAMP(4);
    } else {
      lds_add(F_WREL + 0, 1); lds_add(F_WREL + 1, 1); lds_add(F_WREL + 2, 1);
    }
    {
      u32x4 qv; u32 spins = 0;
      for (;;) {
        MEMBAR();
        qv = ld128(rs, WS_Q + (hkv * 8 + bb) * 1024 + lane * 16);
        if (__all(qv.y == tag && qv.w == tag) || !spin_ok(F, spins)) break;
        __builtin_amdgcn_s_sleep(QPOLL_SLEEP);
      }
      *(LAS u32x2*)(L + L_QS + lane * 8) = u32x2{qv.x, qv.z};
      WAIT_LGKM0();
      F[F_Q] = 1u;
    }
    STAMP(5);
    auto pace = [&]() {
    };
    auto pace_until = [&](int idx, u32 need) {
      u32 spins = 0;
      while (F[idx] < need) {
        pace();
        for (int i_ = 0; i_ < PACE_SLEEP && F[idx] < need; ++i_) __builtin_amdgcn_s_sleep(8);
        if (F[F_ABORT] != 0u || ++spins > GLB_SPIN_CAP) break;
      }
      MEMBAR();
    };
    pace_until(F_LANDED, Wtot + 16u * (u32)(nsl - 8));
    {
      const u32 ehi = (u32)(epoch >> 32);
      u32 spins = 0;
      for (;;) {
        MEMBAR();
        const u32x4 a = ld128(rs, WS_ALIVE + lane * 32), b2 = ld128(rs, WS_ALIVE + lane * 32 + 16);
        const bool ok = a.x == tag && a.y == ehi && a.z == tag && a.w == ehi && b2.x == tag && b2.y == ehi && b2.z == tag && b2.w == ehi;
        if (__all(ok) || !spin_ok(F, spins)) break;
        __builtin_amdgcn_s_sleep(64);
      }
    }
    F[F_ALIVE] = 1u;
    if (c == 0 && lane == 0) __hip_atomic_store((GAS u64*)(p.ws + WS_CTRL), epoch, __ATOMIC_RELAXED, __HIP_MEMORY_SCOPE_AGENT);
    {
      pace_until(F_L, 6u);
      const int hd = lane & 1;
      float M = *(LAS float*)(L + L_MX + hd * 4), Ls = *(LAS float*)(L + L_LW + hd * 4);
#pragma unroll
      for (int i = 1; i < 6; ++i) { M = fmaxf(M, *(LAS float*)(L + L_MX + i * 8 + hd * 4)); Ls += *(LAS float*)(L + L_LW + i * 8 + hd * 4); }
      if (lane < 2) st64(rs, WS_ST + c * 16 + hd * 8, u32x2{vld(M), vld(Ls)});
      u32x2 o; u32 spins = 0;
      for (;;) {
        MEMBAR();
        o = ld64(rs, WS_ST + (c ^ 8) * 16 + hd * 8);
        if (__all((o.x & o.y & 1u) != 0u) || !spin_ok(F, spins)) break;
        __builtin_amdgcn_s_sleep(8);
      }
      const float m0 = __uint_as_float(vld(M)), l0 = __uint_as_float(vld(Ls)), m1 = __uint_as_float(o.x), l1 = __uint_as_float(o.y);
      const float Mg = fmaxf(m0, m1);
      const float e0 = __builtin_amdgcn_exp2f((m0 - Mg) * LOG2E), e1 = __builtin_amdgcn_exp2f((m1 - Mg) * LOG2E);
      const float sc = e0 / (e0 * l0 + e1 * l1);
      osA = __builtin_bit_cast(float, __builtin_amdgcn_readlane(__builtin_bit_cast(int, sc), 0));
      osB = __builtin_bit_cast(float, __builtin_amdgcn_readlane(__builtin_bit_cast(int, sc), 1));
    }
    if (isF) {
      load_cbsb();
      const int b_ = lane & 7;
      u32 spins = 0;
      for (;;) {
        MEMBAR();
        gap = ld64(rs, WS_MODT + b_ * 4608 + f * 8);
        gbp = ld64(rs, WS_MODT + b_ * 4608 + 864 * 4 + f * 8);
        if (__all(gap.x != SENT && gap.y != SENT && gbp.x != SENT && gbp.y != SENT) || !spin_ok(F, spins)) break;
      }
      xp = *(const GAS u32x2*)(p.x + b_ * DM + f * 4);
    }
    STAMP(7);
  } else {
    const int cw = w - 1;
    if (isF) {
      BARRIER();
      const int m = cw & 1, kt = cw >> 1;
      const u32 a0 = L_RING + 36 * m * 1024 + (kt * 192 + 8 * g + qp) * 64 + 8 * pp;
      if (m == 1) lds_add(F_WREL + 0, 1);
      lds_wait_ge(F, F_LANDED, m == 0 ? 36u : 72u);
#pragma unroll
      for (int s = 0; s < 6; ++s) { wf[2 * s] = tr_frag(L, a0 + s * 2048, 4 * 64); wf[2 * s + 1] = tr_frag(L, a0 + s * 2048 + 32, 4 * 64); }
      if (m == 0) lds_add(F_WREL + 0, 1);
      lds_add(F_WREL + 1, 1);
      lds_wait_ge(F, F_LANDED, 108u);
      const u32 d0 = L_RING + 72 * 1024 + (8 * g + qp) * 1152 + cw * 192 + 8 * pp;
#pragma unroll
      for (int t = 0; t < 6; ++t) wf[12 + t] = tr_frag(L, d0 + t * 32, 4 * 1152);
      lds_add(F_WREL + 2, 1);
    } else if (isO) {
      if (cw < 4) {
        BARRIER();
        lds_add(F_WREL + 0, 1);
        const u32 a0 = L_RING + 72 * 1024 + (8 * g + qp) * 128 + cw * 32 + 8 * pp;
        lds_wait_ge(F, F_LANDED, 104u);
#pragma unroll
        for (int s = 0; s < 8; ++s) wf[s] = tr_frag(L, a0 + s * 4096, 4 * 128);
        lds_add(F_WREL + 1, 1);
        lds_wait_ge(F, F_LANDED, 136u);
#pragma unroll
        for (int s = 8; s < 16; ++s) wf[s] = tr_frag(L, a0 + s * 4096, 4 * 128);
        lds_add(F_WREL + 2, 1);
      } else {
        const u32 tag = (u32)read_epoch();
        BARRIER();
        if (isQ) {
          f32x4 acc[4];
          q_partial(cw - 4, tag, acc);
          if (r16 < 8) {
#pragma unroll
            for (int ct = 0; ct < 4; ++ct) *(LAS f32x4*)(L + L_P2 + (cw - 4) * 2048 + ct * 512 + (g * 8 + r16) * 16) = acc[ct];
          }
          lds_add(F_P2, 1);
        } else lds_add(F_WREL + 0, 1);
        lds_add(F_WREL + 1, 1); lds_add(F_WREL + 2, 1);
      }
    } else if (isH) {
      u32 cv[6], xa[5], xb[5];
      const u32 tag = (u32)read_epoch();
#pragma unroll
      for (int i = 0; i < 6; ++i) cv[i] = ((const GAS u32*)p.c)[cw * 64 + lane + 384 * i];
      const int rowb = 6 + (cw & 1);
      const GAS u32* xr = (const GAS u32*)p.x + cw * 288;
      const GAS u32* xs = (const GAS u32*)p.x + rowb * 288;
#pragma unroll
      for (int i = 0; i < 4; ++i) { xa[i] = xr[lane + 64 * i]; xb[i] = xs[lane + 64 * i]; }
      xa[4] = xr[256 + (lane & 31)]; xb[4] = xs[256 + (lane & 31)];
      const u32x2 xq = *(const GAS u32x2*)(p.x + (lane & 7) * DM + hu * 16 + g * 4);
      BARRIER();
#pragma unroll
      for (int i = 0; i < 6; ++i) {
        const int e = cw * 64 + lane + 384 * i;
        const float a = bflo(cv[i]), b2 = bfhi(cv[i]);
        *(LAS u32*)(L + L_SC + (e / 288) * ACT_STRIDE + (e % 288) * 4) = pack2(a / (1.0f + __expf(-a)), b2 / (1.0f + __expf(-b2)));
      }
      {
        float v[10], mean_r, sd_r;
#pragma unroll
        for (int i = 0; i < 5; ++i) { v[2 * i] = bflo(xa[i]); v[2 * i + 1] = bfhi(xa[i]); }
        ln_stats(v, lane < 32, mean_r, sd_r);
        if (lane == 0) { *(LAS float*)(L + L_STAT + cw * 8) = mean_r; *(LAS float*)(L + L_STAT + cw * 8 + 4) = sd_r; }
        if (cw < 2) {
#pragma unroll
          for (int i = 0; i < 5; ++i) { v[2 * i] = bflo(xb[i]); v[2 * i + 1] = bfhi(xb[i]); }
          ln_stats(v, lane < 32, mean_r, sd_r);
          if (lane == 0) { *(LAS float*)(L + L_STAT + rowb * 8) = mean_r; *(LAS float*)(L + L_STAT + rowb * 8 + 4) = sd_r; }
        }
      }
      lds_add(F_SC, 1);
      STAMP(24);
      const u32 sb0 = L_SC + (lane & 7) * ACT_STRIDE + g * 16;
      {
        const int m = cw & 1, kt = cw >> 1;
        lds_wait_ge(F, F_SC, 6u);
        const float mean_r = *(LAS float*)(L + L_STAT + (lane & 7) * 8), sd_r = *(LAS float*)(L + L_STAT + (lane & 7) * 8 + 4);
        const float n0 = rb(rb(bflo(xq.x) - mean_r) / sd_r), n1 = rb(rb(bfhi(xq.x) - mean_r) / sd_r);
        const float n2 = rb(rb(bflo(xq.y) - mean_r) / sd_r), n3 = rb(rb(bfhi(xq.y) - mean_r) / sd_r);
        u32x4 bfr[6];
#pragma unroll
        for (int s = 0; s < 6; ++s) bfr[s] = lds128(L, sb0 + (kt * 6 + s) * 64);
        lds_wait_ge(F, F_LANDED, 36u);
        STAMP(25);
        const u32 a0 = L_RING + (m * 18 + kt * 6) * 1024 + (8 * g + qp) * 32 + 8 * pp;
        f32x4 acc = {0, 0, 0, 0}, acc2 = {0, 0, 0, 0};
#pragma unroll
        for (int s = 0; s < 6; s += 2) { acc = mfma(tr_frag(L, a0 + s * 1024, 4 * 32), bfr[s], acc); acc2 = mfma(tr_frag(L, a0 + (s + 1) * 1024, 4 * 32), bfr[s + 1], acc2); }
        acc += acc2;
        if (r16 < 8) *(LAS f32x4*)(L + L_RED + cw * 512 + (g * 8 + r16) * 16) = acc;
        lds_add(F_P1, 1);
        lds_add(F_WREL + 0, 1);
        if (cw == 5) {
          lds_wait_ge(F, F_P1, 6u);
          const int ls = (g * 8 + (lane & 7)) * 16;
          const f32x4 sa = *(LAS f32x4*)(L + L_RED + 0 * 512 + ls) + *(LAS f32x4*)(L + L_RED + 2 * 512 + ls) + *(LAS f32x4*)(L + L_RED + 4 * 512 + ls);
          const f32x4 ca = *(LAS f32x4*)(L + L_RED + 1 * 512 + ls) + *(LAS f32x4*)(L + L_RED + 3 * 512 + ls) + *(LAS f32x4*)(L + L_RED + 5 * 512 + ls);
          const float h0 = rb(rb(n0 * rb(1.0f + rb(ca[0]))) + rb(sa[0])), h1 = rb(rb(n1 * rb(1.0f + rb(ca[1]))) + rb(sa[1]));
          const float h2 = rb(rb(n2 * rb(1.0f + rb(ca[2]))) + rb(sa[2])), h3 = rb(rb(n3 * rb(1.0f + rb(ca[3]))) + rb(sa[3]));
          const u32x4 hu4 = {pack2(h0, h1), tag, pack2(h2, h3), tag};
#pragma unroll
          for (int i = 0; i < REP_H / 2; ++i)
            st128(rs, WS_H + (2 * i + (r16 >> 3)) * 18432 + ((lane & 7) * 144 + hu * 4 + g) * 16, hu4);
          STAMP(6);
        }
      }
      if (cw < 4) {
        lds_wait_ge(F, F_LANDED, 108u);
        const u32 a0 = L_RING + 36 * 1024 + (8 * g + qp) * 128 + cw * 32 + 8 * pp;
        f32x4 a0c = {0, 0, 0, 0}, a1c = {0, 0, 0, 0};
#pragma unroll 3
        for (int s = 0; s < 18; s += 2) {
          a0c = mfma(tr_frag(L, a0 + s * 4096, 4 * 128), lds128(L, sb0 + s * 64), a0c);
          a1c = mfma(tr_frag(L, a0 + (s + 1) * 4096, 4 * 128), lds128(L, sb0 + (s + 1) * 64), a1c);
        }
        const f32x4 a = a0c + a1c;
        lds_add(F_WREL + 1, 1);
        lds_wait_ge(F, F_INIT, 1u);
        if (r16 < 8) st64(rs, WS_MODT + r16 * 4608 + hu * 128 + cw * 32 + g * 8, u32x2{nosent(pack2(a[0], a[1])), nosent(pack2(a[2], a[3]))});
      } else lds_add(F_WREL + 1, 1);
      lds_add(F_WREL + 2, 1);
    } else {
      BARRIER();
      lds_add(F_WREL + 0, 1); lds_add(F_WREL + 1, 1); lds_add(F_WREL + 2, 1);
    }
    STAMP(1);
    u32x4 qf[4];
    u32 ka[4];
#pragma unroll
    for (int s = 0; s < 4; ++s) ka[s] = r16 * 256 + (((4 * s + g) ^ r16) << 4);
    float mx0 = -INFINITY, mx1 = -INFINITY;
#pragma unroll 1
    for (int q = cw; q < nsl; q += 6) {
      const int pos = (q + roff) & 7;
      lds_wait_ge(F, F_LANDED, Wtot + 16u * (u32)(q + 1));
      const u32 sb = L_RING + pos * 16384;
      u32x4 kf[16];
#pragma unroll
      for (int t = 0; t < 4; ++t)
#pragma unroll
        for (int s = 0; s < 4; ++s) kf[t * 4 + s] = lds128(L, sb + ka[s] + t * 4096);
      WAIT_LGKM0();
      if (lane == 0) F[F_FREE + pos] = (u32)(q + 1);
      if (q < 6) {
        lds_wait_ge(F, F_Q, 1u);
        lds_wait_ge(F, F_WREL + 1, 7u); lds_wait_ge(F, F_WREL + 2, 7u);
        STAMP(2);
#pragma unroll
        for (int s = 0; s < 4; ++s) qf[s] = lds128(L, L_QS + (lane & 1) * 256 + s * 64 + g * 16);
      }
      f32x4 acc[4];
#pragma unroll
      for (int t = 0; t < 4; ++t) {
        acc[t] = f32x4{0, 0, 0, 0};
#pragma unroll
        for (int s = 0; s < 4; ++s) acc[t] = mfma(qf[s], kf[t * 4 + s], acc[t]);
      }
      float s0 = g == 0 ? acc[0][0] : g == 1 ? acc[1][0] : g == 2 ? acc[2][0] : acc[3][0];
      float s1 = g == 0 ? acc[0][1] : g == 1 ? acc[1][1] : g == 2 ? acc[2][1] : acc[3][1];
      s0 = rb(rb(s0) * 0.08838834764831845f);
      s1 = rb(rb(s1) * 0.08838834764831845f);
      mx0 = fmaxf(mx0, s0); mx1 = fmaxf(mx1, s1);
      *(LAS u16*)(L + L_SC + q * 256 + lane * 2) = (u16)(__float_as_uint(s0) >> 16);
      *(LAS u16*)(L + L_SC + q * 256 + 128 + lane * 2) = (u16)(__float_as_uint(s1) >> 16);
    }
    STAMP(3);
    {
      mx0 = wave_max(mx0); mx1 = wave_max(mx1);
      if (lane == 0) *(LAS u32x2*)(L + L_MX + cw * 8) = u32x2{__float_as_uint(mx0), __float_as_uint(mx1)};
      lds_add(F_MX, 1);
      lds_wait_ge(F, F_MX, 6u);
#pragma unroll
      for (int i = 0; i < 6; ++i) { mx0 = fmaxf(mx0, *(LAS float*)(L + L_MX + i * 8)); mx1 = fmaxf(mx1, *(LAS float*)(L + L_MX + i * 8 + 4)); }
      float l0 = 0.f, l1 = 0.f;
#pragma unroll 1
      for (int q = cw; q < nsl; q += 6) {
        const float s0 = bf2f(*(LAS u16*)(L + L_SC + q * 256 + lane * 2)), s1 = bf2f(*(LAS u16*)(L + L_SC + q * 256 + 128 + lane * 2));
        const float p0 = __builtin_amdgcn_exp2f((s0 - mx0) * LOG2E), p1 = __builtin_amdgcn_exp2f((s1 - mx1) * LOG2E);
        l0 += p0; l1 += p1;
        *(LAS u16*)(L + L_SC + q * 256 + lane * 2) = (u16)f2bf(p0);
        *(LAS u16*)(L + L_SC + q * 256 + 128 + lane * 2) = (u16)f2bf(p1);
      }
      l0 = wave_sum(l0); l1 = wave_sum(l1);
      if (lane == 0) *(LAS u32x2*)(L + L_LW + cw * 8) = u32x2{__float_as_uint(l0), __float_as_uint(l1)};
      lds_add(F_L, 1);
    }
    f32x4 o[8];
#pragma unroll
    for (int cb = 0; cb < 8; ++cb) o[cb] = f32x4{0, 0, 0, 0};
    const u32 va0 = (8 * g + qp) * 256 + ((pp >> 1) << 4) + 8 * (pp & 1) + (((qp | ((g & 1) << 2)) << 1) << 4);
#pragma unroll 1
    for (int q = cw; q < nsl; q += 6) {
      const int pos = (q + nsl + roff) & 7;
      if (isF && q >= 24 && q < 30) { lds_wait_ge(F, F_ALIVE, 1u); load_cbsb(); }
      const u32x4 pf0 = lds128(L, L_SC + q * 256 + (lane & 1) * 128 + g * 16), pf1 = lds128(L, L_SC + q * 256 + (lane & 1) * 128 + 64 + g * 16);
      lds_wait_ge(F, F_LANDED, Wtot + 16u * (u32)(q + nsl + 1));
      const u32 sb = L_RING + pos * 16384 + va0;
      u32x4 vf[8], vg2[8];
#pragma unroll
      for (int cb = 0; cb < 8; ++cb) vf[cb] = tr_frag(L, sb ^ (cb << 5), 1024);
#pragma unroll
      for (int cb = 0; cb < 8; ++cb) vg2[cb] = tr_frag(L, (sb ^ (cb << 5)) + 8192, 1024);
      WAIT_LGKM0();
      if (lane == 0) F[F_FREE + pos] = (u32)(q + nsl + 1);
#pragma unroll
      for (int cb = 0; cb < 8; ++cb) o[cb] = mfma(pf0, vf[cb], o[cb]);
#pragma unroll
      for (int cb = 0; cb < 8; ++cb) o[cb] = mfma(pf1, vg2[cb], o[cb]);
    }
    STAMP(4);
    if (g == 0) {
#pragma unroll
      for (int cb = 0; cb < 8; ++cb) *(LAS u32x2*)(L + L_ACT + cw * 1024 + (cb * 16 + r16) * 8) = u32x2{__float_as_uint(o[cb][0]), __float_as_uint(o[cb][1])};
    }
  }
  BARRIER();
  if (w != 0) STAMP(8);
  if (w == 7) {
    f32x4 O = *(LAS f32x4*)(L + L_ACT + lane * 16);
#pragma unroll
    for (int i = 1; i < 6; ++i) O += *(LAS f32x4*)(L + L_ACT + i * 1024 + lane * 16);
    st128(rs, WS_OP + c * 1024 + lane * 16, u32x4{vld(O[0] * osA), vld(O[1] * osB), vld(O[2] * osA), vld(O[3] * osB)});
    if (w != 0) STAMP(9);
  }
  if (isO) {
    constexpr int L_OS = L_RING;
    u32x4 ov[4];
    bool got[4] = {false, false, false, false};
    u32 spins = 0;
    for (;;) {
      MEMBAR();
#pragma unroll
      for (int i = 0; i < 4; ++i) if (!got[i]) ov[i] = ld128(rs, WS_OP + (xg + 8 * ((i >> 1) * 16 + 2 * w + (i & 1))) * 1024 + lane * 16);
      bool all = true;
#pragma unroll
      for (int i = 0; i < 4; ++i) if (!got[i]) {
        got[i] = __all((ov[i].x & ov[i].y & ov[i].z & ov[i].w & 1u) != 0u);
        all = all && got[i];
      }
      if (all || !spin_ok(F, spins)) break;
    }
#pragma unroll
    for (int hk = 0; hk < 2; ++hk) {
      const f32x4 s = __builtin_bit_cast(f32x4, ov[2 * hk]) + __builtin_bit_cast(f32x4, ov[2 * hk + 1]);
      *(LAS u32*)(L + L_OS + w * 1040 + hk * 512 + lane * 4) = pack2(s[0], s[2]);
      *(LAS u32*)(L + L_OS + w * 1040 + hk * 512 + 256 + lane * 4) = pack2(s[1], s[3]);
    }
    BARRIER();
    if (w != 0) STAMP(10);
    if (w >= 1 && w <= 4) {
      const int ct = w - 1;
      const u32 ob = L_OS + (lane & 7) * 1040 + g * 16;
      u32x4 bfr[16];
#pragma unroll
      for (int s = 0; s < 16; ++s) bfr[s] = lds128(L, ob + s * 64);
      f32x4 a0 = {0, 0, 0, 0}, a1 = {0, 0, 0, 0}, a2 = {0, 0, 0, 0}, a3 = {0, 0, 0, 0};
#pragma unroll
      for (int s = 0; s < 16; s += 4) { a0 = mfma(wf[s], bfr[s], a0); a1 = mfma(wf[s + 1], bfr[s + 1], a1); a2 = mfma(wf[s + 2], bfr[s + 2], a2); a3 = mfma(wf[s + 3], bfr[s + 3], a3); }
      const f32x4 a = (a0 + a1) + (a2 + a3);
      if (r16 < 8) st128(rs, WS_WP + (xg * 144 + (u * 4 + ct) * 4 + g) * 128 + r16 * 16, u32x4{vld(a[0]), vld(a[1]), vld(a[2]), vld(a[3])});
      if (w != 0) STAMP(11);
    }
  } else if (isF) {
    float x1r[4];
    if (w == 7) {
      u32x4 pv; u32 spins = 0;
      for (;;) {
        MEMBAR();
        pv = ld128(rs, WS_WP + ((lane >> 3) * 144 + f) * 128 + (lane & 7) * 16);
        if (__all((pv.x & pv.y & pv.z & pv.w & 1u) != 0u) || !spin_ok(F, spins)) break;
      }
      const float a0 = rb(sum_x8_16_32(__uint_as_float(pv.x))), a1 = rb(sum_x8_16_32(__uint_as_float(pv.y)));
      const float a2 = rb(sum_x8_16_32(__uint_as_float(pv.z))), a3 = rb(sum_x8_16_32(__uint_as_float(pv.w)));
      x1r[0] = rb(bflo(xp.x) + rb(bflo(gap.x) * a0)); x1r[1] = rb(bfhi(xp.x) + rb(bfhi(gap.x) * a1));
      x1r[2] = rb(bflo(xp.y) + rb(bflo(gap.y) * a2)); x1r[3] = rb(bfhi(xp.y) + rb(bfhi(gap.y) * a3));
      const u32x2 xo = {nosent(pack2(x1r[0], x1r[1])), nosent(pack2(x1r[2], x1r[3]))};
      st64(rs, WS_X1 + (lane >> 3) * 9216 + f * 64 + (lane & 7) * 8, xo);
      F[F_X1] = 1u;
      if (w != 0) STAMP(10);
    } else lds_wait_ge(F, F_X1, 1u);
    if (DELAY_C) __builtin_amdgcn_s_sleep(DELAY_C);
    {
      const int xb = WS_X1 + xg * 9216;
      u32x4 xv, xw = {0u, 0u, 0u, 0u}; u32 spins = 0;
      for (;;) {
        MEMBAR();
        xv = ld128(rs, xb + tid * 16);
        if (w == 0) xw = ld128(rs, xb + 8192 + lane * 16);
        if (__all(xv.x != SENT && xv.y != SENT && xv.z != SENT && xv.w != SENT && xw.x != SENT && xw.y != SENT && xw.z != SENT && xw.w != SENT) || !spin_ok(F, spins)) break;
      }
      if (w != 0) STAMP(16);
      {
        const u32 a = L_ACT + (tid & 3) * 2 * ACT_STRIDE + (tid >> 2) * 8;
        *(LAS u32x2*)(L + a) = u32x2{xv.x, xv.y}; *(LAS u32x2*)(L + a + ACT_STRIDE) = u32x2{xv.z, xv.w};
        if (w == 0) { *(LAS u32x2*)(L + a + 1024) = u32x2{xw.x, xw.y}; *(LAS u32x2*)(L + a + 1024 + ACT_STRIDE) = u32x2{xw.z, xw.w}; }
      }
      BARRIER();
      if (w != 0) STAMP(17);
      u32 xr[5];
#pragma unroll
      for (int i = 0; i < 4; ++i) xr[i] = *(LAS u32*)(L + L_ACT + w * ACT_STRIDE + (lane + 64 * i) * 4);
      xr[4] = *(LAS u32*)(L + L_ACT + w * ACT_STRIDE + (256 + (lane & 31)) * 4);
      float v[10];
#pragma unroll
      for (int i = 0; i < 5; ++i) { v[2 * i] = bflo(xr[i]); v[2 * i + 1] = bfhi(xr[i]); }
      float s = (v[0] + v[1]) + (v[2] + v[3]) + ((v[4] + v[5]) + (v[6] + v[7]));
      if (lane < 32) s += v[8] + v[9];
      const float mean = wave_sum(s) * (1.0f / DM);
      float q2 = 0.f, q3 = 0.f;
#pragma unroll
      for (int i = 0; i < 4; ++i) { const float d0 = v[2 * i] - mean, d1 = v[2 * i + 1] - mean; q2 += d0 * d0; q3 += d1 * d1; }
      if (lane < 32) { const float d0 = v[8] - mean, d1 = v[9] - mean; q2 += d0 * d0; q3 += d1 * d1; }
      const float var = wave_sum(q2 + q3) * (1.0f / DM);
      const float mean_r = rb(mean);
      const float rinv = __builtin_amdgcn_rcpf(rb(__builtin_amdgcn_sqrtf(rb(rb(var) + 1e-5f))));
#pragma unroll
      for (int i = 0; i < 5; ++i) {
        const u32 dp = pack2(v[2 * i] - mean_r, v[2 * i + 1] - mean_r);
        const u32 np = pack2(bflo(dp) * rinv, bfhi(dp) * rinv);
        const u32 tp = pack2(bflo(np) * bflo(cbp[i]), bfhi(np) * bfhi(cbp[i]));
        const u32 hp = pack2(bflo(tp) + bflo(sbp[i]), bfhi(tp) + bfhi(sbp[i]));
        if (i < 4) *(LAS u32*)(L + L_ACT + w * ACT_STRIDE + (lane + 64 * i) * 4) = hp;
        else if (lane < 32) *(LAS u32*)(L + L_ACT + w * ACT_STRIDE + (256 + lane) * 4) = hp;
      }
      if (w != 0) STAMP(18);
    }
    BARRIER();
    if (w != 0) STAMP(11);
    constexpr int L_R2 = L_RING;
    constexpr int L_HH = L_RING + 8192;
    if (w == 7) {
      BARRIER();
      BARRIER();
      const int off = WS_FP + (lane >> 3) * 9216 + ((f >> 3) * 4 + (f & 3)) * 128 + (lane & 7) * 16 + ((f >> 2) & 1) * 8;
      u32x2 e[18]; u32 spins = 0;
      bool got0 = false, got1 = false, got2 = false;
      if (DELAY_E) __builtin_amdgcn_s_sleep(DELAY_E);
      for (;;) {
        MEMBAR();
        if (!got0) {
#pragma unroll
          for (int i = 0; i < 6; ++i) e[i] = ld64(rs, off + i * 8 * 9216);
        }
        if (!got1) {
#pragma unroll
          for (int i = 6; i < 12; ++i) e[i] = ld64(rs, off + i * 8 * 9216);
        }
        if (!got2) {
#pragma unroll
          for (int i = 12; i < 18; ++i) e[i] = ld64(rs, off + i * 8 * 9216);
        }
        auto grp_ok = [&](int i0) {
          bool ok = true;
#pragma unroll
          for (int i = 0; i < 6; ++i) ok = ok && e[i0 + i].x != SENT && e[i0 + i].y != SENT;
          return (bool)__all(ok);
        };
        if (!got0) got0 = grp_ok(0);
        if (!got1) got1 = grp_ok(6);
        if (!got2) got2 = grp_ok(12);
        if ((got0 && got1 && got2) || !spin_ok(F, spins)) break;
      }
      if (w != 0) STAMP(23);
      f32x4 s0 = {0, 0, 0, 0}, s1 = {0, 0, 0, 0};
#pragma unroll
      for (int i = 0; i < 18; i += 2) {
        s0 += f32x4{bflo(e[i].x), bfhi(e[i].x), bflo(e[i].y), bfhi(e[i].y)};
        s1 += f32x4{bflo(e[i + 1].x), bfhi(e[i + 1].x), bflo(e[i + 1].y), bfhi(e[i + 1].y)};
      }
      const f32x4 s = s0 + s1;
      const float d0 = rb(sum_x8_16_32(s[0])), d1 = rb(sum_x8_16_32(s[1])), d2 = rb(sum_x8_16_32(s[2])), d3 = rb(sum_x8_16_32(s[3]));
      const float o0 = x1r[0] + rb(bflo(gbp.x) * d0), o1 = x1r[1] + rb(bfhi(gbp.x) * d1);
      const float o2 = x1r[2] + rb(bflo(gbp.y) * d2), o3 = x1r[3] + rb(bfhi(gbp.y) * d3);
      if (lane < 8) *(GAS u32x2*)(p.out + lane * DM + f * 4) = u32x2{pack2(o0, o1), pack2(o2, o3)};
      if (w != 0) STAMP(13);
    } else {
      const int cw = w - 1;
      if (w >= 1) {
        const int kt = cw >> 1;
        const u32 hb = L_ACT + (lane & 7) * ACT_STRIDE + kt * 384 + g * 16;
        u32x4 bfr[6];
#pragma unroll
        for (int s = 0; s < 6; ++s) bfr[s] = lds128(L, hb + s * 64);
        f32x4 a0 = {0, 0, 0, 0}, a1 = {0, 0, 0, 0}, a2 = {0, 0, 0, 0}, a3 = {0, 0, 0, 0};
#pragma unroll
        for (int s = 0; s < 6; s += 2) { a0 = mfma(wf[2 * s], bfr[s], a0); a1 = mfma(wf[2 * s + 1], bfr[s], a1); a2 = mfma(wf[2 * s + 2], bfr[s + 1], a2); a3 = mfma(wf[2 * s + 3], bfr[s + 1], a3); }
        a0 += a2; a1 += a3;
        if (r16 < 8) { *(LAS f32x4*)(L + L_R2 + cw * 1024 + (g * 8 + r16) * 16) = a0; *(LAS f32x4*)(L + L_R2 + cw * 1024 + 512 + (g * 8 + r16) * 16) = a1; }
      }
      if (w != 0) STAMP(19);
      BARRIER();
      if (w != 0) STAMP(20);
      if (w < 4) {
        const int ct = w >> 1, e = (w & 1) * 64 + lane;
        const u32 a = L_R2 + ct * 512 + e * 4;
        const float gg = rb(*(LAS float*)(L + a) + *(LAS float*)(L + a + 2048) + *(LAS float*)(L + a + 4096));
        const float uu = rb(*(LAS float*)(L + a + 1024) + *(LAS float*)(L + a + 3072) + *(LAS float*)(L + a + 5120));
        const float inner = 0.7978845608028654f * (gg + 0.044715f * gg * gg * gg);
        const float th = 1.0f - 2.0f * __builtin_amdgcn_rcpf(1.0f + __expf(2.0f * inner));
        const float ge = rb(0.5f * gg * (1.0f + th));
        const int ls = e >> 2, rr = e & 3, b_ = ls & 7, g_ = ls >> 3;
        *(LAS u16*)(L + L_HH + b_ * 80 + (ct * 16 + g_ * 4 + rr) * 2) = (u16)f2bf(ge * uu);
      }
      if (w != 0) STAMP(21);
      BARRIER();
      if (w != 0) STAMP(22);
      if (w >= 1) {
        const u32x4 bfr = lds128(L, L_HH + (lane & 7) * 80 + g * 16);
        f32x4 a[6];
#pragma unroll
        for (int t = 0; t < 6; ++t) a[t] = mfma(wf[12 + t], bfr, f32x4{0, 0, 0, 0});
#pragma unroll
        for (int t = 0; t < 6; t += 2)
          if (r16 < 8) st128(rs, WS_FP + f * 9216 + ((cw * 3 + (t >> 1)) * 4 + g) * 128 + r16 * 16,
                             u32x4{nosent(pack2(a[t][0], a[t][1])), nosent(pack2(a[t][2], a[t][3])), nosent(pack2(a[t + 1][0], a[t + 1][1])), nosent(pack2(a[t + 1][2], a[t + 1][3]))});
      }
      if (w != 0) STAMP(12);
    }
  }
}

}

extern "C" void kernel_launch(void* const* d_in, const int* in_sizes, int n_in, void* d_out, int out_size, void* d_ws, size_t ws_size,
                              hipStream_t stream) {
  (void)in_sizes; (void)n_in; (void)out_size; (void)ws_size;
  Params p;
  p.x = (const u16*)d_in[0];
  p.kc = (const unsigned char*)d_in[1];
  p.vc = (const unsigned char*)d_in[2];
  p.wq = (const unsigned char*)d_in[3];
  p.wo = (const unsigned char*)d_in[4];
  p.wg = (const unsigned char*)d_in[6];
  p.wu = (const unsigned char*)d_in[7];
  p.wd = (const unsigned char*)d_in[8];
  p.c = (const u16*)d_in[9];
  p.wmod = (const unsigned char*)d_in[10];
  p.out = (u16*)d_out;
  p.ws = (unsigned char*)d_ws;
  (void)hipFuncSetAttribute((const void*)fused_block, hipFuncAttributeMaxDynamicSharedMemorySize, LDS_BYTES);
  hipLaunchKernelGGL(fused_block, dim3(NCU), dim3(NT), LDS_BYTES, stream, p);
}
```

```cpp
#include <hip/hip_runtime.h>
#include <stdint.h>
#include <type_traits>
#pragma clang fp contract(off)

#define INFL 24
#define INFLW 320
#define HPOLL_SLEEP 2
#define QPOLL_SLEEP 2
#define PACE 0
#define PACE_KP 3
#define SCHED 1
#define EARLY_Q 16
#define EARLY_N 24
#define SCHED_T16 1770
#define SCHED_TAU 876
#define SCHED_MIX 0
#define SCHED_Q0 17
#define SCHED_L1 30
#define SCHED_L2 100000
#define SCHED_L3 100000
#define DELAY_C 12
#define DELAY_E 28
#define SHIFT_O 1
#define SHIFT_H 2
#define PACE_LINES 16
#define PACE_SLEEP 8
#define PACE_MIN 16
#define PACE_MAX 32
#define AUX_K "nt"
#define AUX_V "nt"
#define AUX_W "nt"
#define STAMPS 0

#define LAS __attribute__((address_space(3)))
#define GAS __attribute__((address_space(1)))
typedef unsigned u32;
typedef unsigned long long u64;
typedef unsigned short u16;
typedef u32 u32x4 __attribute__((ext_vector_type(4)));
typedef u32 u32x2 __attribute__((ext_vector_type(2)));
typedef float f32x4 __attribute__((ext_vector_type(4)));
typedef __bf16 bf16x8 __attribute__((ext_vector_type(8)));
typedef short s16x4 __attribute__((ext_vector_type(4)));

namespace {

constexpr int NB = 8, DM = 576, HKV = 16, SK = 8192, DH = 128, DF = 4608;
constexpr int NCU = 256, NT = 512;

constexpr int REP_H = 8, REP_X = 8;
constexpr int WS_CTRL = 0;
constexpr int WS_ALIVE = 4096;
constexpr int WS_PROG = 6144;
constexpr int WS_ST = 8192;
constexpr int WS_MODT = WS_ST + 4096;
constexpr int WS_X1 = WS_MODT + 36864;
constexpr int WS_H = WS_X1 + REP_X * 9216;
constexpr int WS_Q = WS_H + REP_H * 18432;
constexpr int WS_OP = WS_Q + 131072;
constexpr int WS_WP = WS_OP + 262144;
constexpr int WS_FP = WS_WP + 147456;
constexpr int WS_END = WS_FP + 144 * 9216;
constexpr int WS_DBG = 4 << 20;
static_assert(WS_END <= WS_DBG, "ws");

constexpr int L_RING = 0;
constexpr int L_SC = 131072;
constexpr int L_ACT = L_SC + 16896;
constexpr int L_QS = L_ACT + 9344;
constexpr int L_RED = L_QS + 512;
constexpr int L_STAT = L_RED + 3072;
constexpr int L_MX = L_STAT + 64;
constexpr int L_LW = L_MX + 64;
constexpr int L_FLG = L_LW + 64;
constexpr int L_P2 = L_SC + 8192;
constexpr int LDS_BYTES = 163840;
static_assert(L_FLG + 256 <= LDS_BYTES, "lds");
constexpr int ACT_STRIDE = 1168;
constexpr int F_LANDED = 0, F_FREE = 1  , F_WREL = 9  , F_SC = 12, F_P1 = 13, F_INIT = 14, F_Q = 15, F_ABORT = 16, F_ALIVE = 17, F_P2 = 18, F_MX = 19, F_L = 20, F_INFL = 21, F_X1 = 22;

constexpr u32 SENT = 0xFFFFFFFFu;
constexpr u32 LDS_SPIN_CAP = 1u << 21;
constexpr u32 GLB_SPIN_CAP = 1u << 17;
constexpr float LOG2E = 1.4426950408889634f;

struct Params {
  const u16* x; const unsigned char* kc; const unsigned char* vc; const unsigned char* wq; const unsigned char* wo;
  const unsigned char* wg; const unsigned char* wu; const unsigned char* wd; const u16* c; const unsigned char* wmod;
  u16* out; unsigned char* ws;
};

__device__ __forceinline__ float bf2f(u32 v16) { return __uint_as_float(v16 << 16); }
__device__ __forceinline__ float bflo(u32 d) { return __uint_as_float(d << 16); }
__device__ __forceinline__ float bfhi(u32 d) { return __uint_as_float(d & 0xffff0000u); }
__device__ __forceinline__ float rb(float f) { return (float)(__bf16)f; }
__device__ __forceinline__ u32 f2bf(float f) { return (u32)__builtin_bit_cast(u16, (__bf16)f); }
typedef float f32x2 __attribute__((ext_vector_type(2)));
typedef __bf16 bf16x2 __attribute__((ext_vector_type(2)));
__device__ __forceinline__ u32 pack2(float lo, float hi) { return __builtin_bit_cast(u32, __builtin_convertvector(f32x2{lo, hi}, bf16x2)); }
__device__ __forceinline__ u32 nosent(u32 p) { return p < 0xFFFEFFFFu ? p : 0xFFFEFFFFu; }
__device__ __forceinline__ u32 umax(u32 a, u32 b) { return a > b ? a : b; }
__device__ __forceinline__ u32 umax4(u32x4 v) { return umax(umax(v.x, v.y), umax(v.z, v.w)); }
typedef __bf16 bf16x2_ __attribute__((ext_vector_type(2)));
__device__ __forceinline__ float add_sel(u32 v, u32 sel, float acc) { return __builtin_amdgcn_fdot2_f32_bf16(__builtin_bit_cast(bf16x2_, v), __builtin_bit_cast(bf16x2_, sel), acc, false); }
__device__ __forceinline__ u32 vld(float f) { return __float_as_uint(f) | 1u; }
__device__ __forceinline__ f32x4 mfma(u32x4 a, u32x4 b, f32x4 c) {
  return __builtin_amdgcn_mfma_f32_16x16x32_bf16(__builtin_bit_cast(bf16x8, a), __builtin_bit_cast(bf16x8, b), c, 0, 0, 0);
}
__device__ __forceinline__ u32x4 mk_srd(const void* p, u32 bytes) {
  const u64 a = (u64)p;
  u32x4 s;
  s.x = __builtin_amdgcn_readfirstlane((u32)a);
  s.y = __builtin_amdgcn_readfirstlane((u32)(a >> 32) & 0xffffu);
  s.z = __builtin_amdgcn_readfirstlane(bytes);
  s.w = 0x00020000u;
  return s;
}
#define MEMBAR() asm volatile("" ::: "memory")
#define WAIT_LGKM0() asm volatile("s_waitcnt lgkmcnt(0)" ::: "memory")
#define WAIT_VM0() asm volatile("s_waitcnt vmcnt(0)" ::: "memory")
#define BARRIER() do { asm volatile("s_waitcnt lgkmcnt(0)" ::: "memory"); __builtin_amdgcn_s_barrier(); asm volatile("" ::: "memory"); } while (0)

#define DMA1(AUX, srd, voff, soff, ldsaddr)                                                                      \
  do { u32 keep_;                                                                                                \
    asm volatile("s_mov_b32 %0, m0\n\ts_mov_b32 m0, %1\n\ts_nop 0\n\t"                                           \
                 "buffer_load_dwordx4 %4, %2, %3 offen " AUX " lds\n\ts_mov_b32 m0, %0"                          \
                 : "=&s"(keep_) : "s"(ldsaddr), "s"(srd), "s"(soff), "v"(voff) : "memory"); } while (0)
#define DMA4(AUX, srd, v0, v1, v2, v3, soff, ldsaddr)                                                            \
  do { u32 keep_;                                                                                                \
    asm volatile("s_mov_b32 %0, m0\n\ts_mov_b32 m0, %1\n\ts_nop 0\n\t"                                           \
                 "buffer_load_dwordx4 %4, %2, %3 offen " AUX " lds\n\t"                                          \
                 "buffer_load_dwordx4 %5, %2, %3 offen offset:1024 " AUX " lds\n\t"                              \
                 "buffer_load_dwordx4 %6, %2, %3 offen offset:2048 " AUX " lds\n\t"                              \
                 "buffer_load_dwordx4 %7, %2, %3 offen offset:3072 " AUX " lds\n\ts_mov_b32 m0, %0"              \
                 : "=&s"(keep_) : "s"(ldsaddr), "s"(srd), "s"(soff), "v"(v0), "v"(v1), "v"(v2), "v"(v3) : "memory"); } while (0)

template <int CTRL> __device__ __forceinline__ float dpp(float x) {
  return __builtin_bit_cast(float, __builtin_amdgcn_mov_dpp(__builtin_bit_cast(int, x), CTRL, 0xf, 0xf, true));
}
constexpr int ROR8 = 0x128, ROR4 = 0x124, ROR2 = 0x122, ROR1 = 0x121;
__device__ __forceinline__ float xrow_max(float x) {
  auto s = __builtin_amdgcn_permlane16_swap(__float_as_uint(x), __float_as_uint(x), false, false);
  x = fmaxf(__uint_as_float(s[0]), __uint_as_float(s[1]));
  auto t = __builtin_amdgcn_permlane32_swap(__float_as_uint(x), __float_as_uint(x), false, false);
  return fmaxf(__uint_as_float(t[0]), __uint_as_float(t[1]));
}
__device__ __forceinline__ float xrow_sum(float x) {
  auto s = __builtin_amdgcn_permlane16_swap(__float_as_uint(x), __float_as_uint(x), false, false);
  x = __uint_as_float(s[0]) + __uint_as_float(s[1]);
  auto t = __builtin_amdgcn_permlane32_swap(__float_as_uint(x), __float_as_uint(x), false, false);
  return __uint_as_float(t[0]) + __uint_as_float(t[1]);
}
__device__ __forceinline__ float wave_max(float x) {
  x = fmaxf(x, dpp<ROR8>(x)); x = fmaxf(x, dpp<ROR4>(x)); x = fmaxf(x, dpp<ROR2>(x)); x = fmaxf(x, dpp<ROR1>(x));
  return xrow_max(x);
}
__device__ __forceinline__ float wave_sum(float x) {
  x += dpp<ROR8>(x); x += dpp<ROR4>(x); x += dpp<ROR2>(x); x += dpp<ROR1>(x);
  return xrow_sum(x);
}
__device__ __forceinline__ float sum_x8_16_32(float x) {
  x += dpp<ROR8>(x);
  return xrow_sum(x);
}

typedef volatile LAS u32* flagp;
__device__ __forceinline__ void lds_wait_ge(flagp F, int idx, u32 need) {
  (void)F;
  const u32 a = L_FLG + idx * 4, ab = L_FLG + F_ABORT * 4;
  u32 v, z, cnt;
  asm volatile("s_mov_b32 %2, 0\n"
               "1:\n\tds_read_b32 %0, %3\n\tds_read_b32 %1, %4\n\ts_waitcnt lgkmcnt(0)\n\t"
               "v_cmp_le_u32 vcc, %5, %0\n\ts_cbranch_vccnz 2f\n\t"
               "v_cmp_ne_u32 vcc, 0, %1\n\ts_cbranch_vccnz 2f\n\t"
               "s_sleep 1\n\ts_add_u32 %2, %2, 1\n\ts_cmp_lt_u32 %2, %6\n\ts_cbranch_scc1 1b\n\t"
               "v_mov_b32 %0, 1\n\tds_write_b32 %4, %0\n"
               "2:"
               : "=&v"(v), "=&v"(z), "=&s"(cnt) : "v"(a), "v"(ab), "v"(need), "s"(LDS_SPIN_CAP) : "vcc", "scc", "memory");
}
__device__ __forceinline__ void lds_add(int idx, u32 n) {
  const u32 a = L_FLG + idx * 4;
  asm volatile("s_waitcnt lgkmcnt(0)\n\ts_mov_b64 vcc, exec\n\ts_mov_b64 exec, 1\n\tds_add_u32 %0, %1\n\ts_mov_b64 exec, vcc"
               :: "v"(a), "v"(n) : "vcc", "memory");
}
__device__ __forceinline__ bool spin_ok(flagp F, u32& spins) {
  if (++spins > GLB_SPIN_CAP) { F[F_ABORT] = 2u; return false; }
  if ((spins & 255u) == 0u && F[F_ABORT] != 0u) return false;
  return true;
}

constexpr int SC1 = 16;
typedef __amdgpu_buffer_rsrc_t rsrc_t;
__device__ __forceinline__ u32x4 ld128(rsrc_t r, int off) { return __builtin_amdgcn_raw_buffer_load_b128(r, off, 0, SC1); }
__device__ __forceinline__ u32x2 ld64(rsrc_t r, int off) { return __builtin_amdgcn_raw_buffer_load_b64(r, off, 0, SC1); }
__device__ __forceinline__ u32 ld32(rsrc_t r, int off) { return __builtin_amdgcn_raw_buffer_load_b32(r, off, 0, SC1); }
__device__ __forceinline__ void st128(rsrc_t r, int off, u32x4 v) { __builtin_amdgcn_raw_buffer_store_b128(v, r, off, 0, SC1); }
__device__ __forceinline__ void st64(rsrc_t r, int off, u32x2 v) { __builtin_amdgcn_raw_buffer_store_b64(v, r, off, 0, SC1); }

__device__ __forceinline__ u32x4 tr_frag(LAS unsigned char* L, u32 addr, u32 stride4) {
  const s16x4 a = __builtin_amdgcn_ds_read_tr16_b64_v4i16((LAS s16x4*)(L + addr));
  const s16x4 b = __builtin_amdgcn_ds_read_tr16_b64_v4i16((LAS s16x4*)(L + addr + stride4));
  const u32x2 a2 = __builtin_bit_cast(u32x2, a), b2 = __builtin_bit_cast(u32x2, b);
  return u32x4{a2.x, a2.y, b2.x, b2.y};
}
__device__ __forceinline__ u32x4 lds128(LAS unsigned char* L, u32 addr) { return *(LAS u32x4*)(L + addr); }

#define STAMP(i) do {} while (0)

__device__ __forceinline__ void ln_stats(const float (&v)[10], bool tail, float& mean_r, float& sd_r) {
  float s = 0.f;
#pragma unroll
  for (int i = 0; i < 8; ++i) s += v[i];
  if (tail) s += v[8] + v[9];
  const float mean = wave_sum(s) * (1.0f / DM);
  float q = 0.f;
#pragma unroll
  for (int i = 0; i < 8; ++i) { const float d = v[i] - mean; q += d * d; }
  if (tail) { const float d0 = v[8] - mean, d1 = v[9] - mean; q += d0 * d0 + d1 * d1; }
  const float var = wave_sum(q) * (1.0f / DM);
  mean_r = rb(mean);
  sd_r = rb(sqrtf(rb(rb(var) + 1e-5f)));
}
__device__ __forceinline__ float ln_mod(float x, float mean_r, float sd_r, float scale, float shift) {
  const float n = rb(rb(x - mean_r) / sd_r);
  return rb(rb(n * rb(1.0f + scale)) + shift);
}

__global__ void __launch_bounds__(NT, 2) fused_block(Params p) {
  extern __shared__ __attribute__((aligned(16))) unsigned char smem[];
  LAS unsigned char* L = (LAS unsigned char*)smem;
  flagp F = (flagp)(L + L_FLG);
  const int tid = threadIdx.x, lane = tid & 63, w = __builtin_amdgcn_readfirstlane(tid >> 6);
  STAMP(0);
  const u32 t_entry = (u32)__builtin_amdgcn_s_memrealtime();
  const int c = blockIdx.x, xg = c & 7, ci = c >> 3;
  const int hkv = 2 * xg + (ci >> 4), cj = ci & 15, bb = cj >> 1, half = cj & 1;
  const int pj = ci >> 1;
  const bool isF = half == 0 || pj >= 14, isO = !isF && pj < 9;
  const int hu = xg * 5 + (pj - 9);
  const bool isH = !isF && !isO && hu < 36;
  const int f = xg * 18 + (half == 0 ? pj : pj + 2);
  const int u = pj;
  const bool isQ = isO && u < 8;
  const int shift = pj < 9 ? (pj < 8 ? SHIFT_O : 0) : pj < 14 ? (xg * 5 + (pj - 9) < 36 ? SHIFT_H : 0) : 0;
  const int nsl = half == 0 ? 64 + shift : 64 - shift;
  const int key0 = half == 0 ? 0 : (64 + shift) * 64;
  const u32 Wtot = isF ? 108u : isO ? 136u : isH ? 108u : 0u;
  const int roff = (isF || isH) ? 7 : 0;
  const int g = lane >> 4, r16 = lane & 15, qp = (lane & 15) >> 2, pp = lane & 3;
  const rsrc_t rs = __builtin_amdgcn_make_buffer_rsrc((void*)p.ws, 0, WS_END, 0x00020000);

  auto read_epoch = [&]() {
    u64 e = *(const __attribute__((address_space(4))) u64*)(p.ws + WS_CTRL) + 1ull;
    asm volatile("" : "+s"(e));
    return e;
  };

  u32x4 wf[18];
  u32 cbp[5], sbp[5];
  u32x2 gap, gbp, xp;
  float osA = 0.f, osB = 0.f;

  auto load_cbsb = [&]() {
    u32 spins = 0;
    for (;;) {
      MEMBAR();
      const int base = WS_MODT + w * 4608;
#pragma unroll
      for (int i = 0; i < 4; ++i) { sbp[i] = ld32(rs, base + (288 + lane + 64 * i) * 4); cbp[i] = ld32(rs, base + (576 + lane + 64 * i) * 4); }
      sbp[4] = ld32(rs, base + (288 + 256 + (lane & 31)) * 4); cbp[4] = ld32(rs, base + (576 + 256 + (lane & 31)) * 4);
      u32 mx = 0;
#pragma unroll
      for (int i = 0; i < 5; ++i) mx = umax(mx, umax(sbp[i], cbp[i]));
      if (__all(mx != SENT) || !spin_ok(F, spins)) break;
    }
#pragma unroll
    for (int i = 0; i < 5; ++i) cbp[i] = pack2(1.0f + bflo(cbp[i]), 1.0f + bfhi(cbp[i]));
  };
  auto q_partial = [&](int kt, u32 tag, f32x4 (&acc)[4]) {
    lds_wait_ge(F, F_LANDED, 72u);
    u32x4 pq[24];
    const u32 a0 = L_RING + (kt * 192 + 8 * g + qp) * 128 + 8 * pp;
#pragma unroll
    for (int s = 0; s < 6; ++s)
#pragma unroll
      for (int ct = 0; ct < 4; ++ct) pq[s * 4 + ct] = tr_frag(L, a0 + s * 4096 + ct * 32, 4 * 128);
    lds_add(F_WREL + 0, 1);
    STAMP(6);
    u32x4 hv[6];
    u32 spins = 0;
    const int hb = WS_H + (c % REP_H) * 18432 + kt * 768;
    int go[6], lo[6];
#pragma unroll
    for (int i = 0; i < 6; ++i) { const int e = i * 64 + lane, row = e / 48, j = e % 48; go[i] = hb + row * 2304 + j * 16; lo[i] = L_ACT + row * ACT_STRIDE + kt * 384 + j * 8; }
    for (;;) {
      MEMBAR();
#pragma unroll
      for (int i = 0; i < 6; ++i) hv[i] = ld128(rs, go[i]);
      u32 bad = 0;
#pragma unroll
      for (int i = 0; i < 6; ++i) bad |= (hv[i].y ^ tag) | (hv[i].w ^ tag);
      if (__all(bad == 0u) || !spin_ok(F, spins)) break;
      __builtin_amdgcn_s_sleep(HPOLL_SLEEP);
    }
#pragma unroll
    for (int i = 0; i < 6; ++i) *(LAS u32x2*)(L + lo[i]) = u32x2{hv[i].x, hv[i].z};
    WAIT_LGKM0();
    STAMP(3);
    const u32 ab = L_ACT + (lane & 7) * ACT_STRIDE + kt * 384 + g * 16;
#pragma unroll
    for (int ct = 0; ct < 4; ++ct) acc[ct] = f32x4{0, 0, 0, 0};
#pragma unroll
    for (int s = 0; s < 6; ++s) {
      const u32x4 bfr = lds128(L, ab + s * 64);
#pragma unroll
      for (int ct = 0; ct < 4; ++ct) acc[ct] = mfma(pq[s * 4 + ct], bfr, acc[ct]);
    }
  };

  if (w == 0) {
    __builtin_amdgcn_s_setprio(3);
    if (lane < 32) F[lane] = 0u;
    u32 n = 0;
    auto run = [&](auto inflp, const u32x4& srd, u32 voff, u32 step, u32 first, u32 cnt) {
      constexpr int NP = decltype(inflp)::value;
#pragma unroll 1
      for (u32 s = first; s < cnt; ++s) {
        DMA1(AUX_W, srd, voff, s * step, (u32)(L_RING + n * 1024));
        ++n;
        asm volatile("s_waitcnt vmcnt(%0)" :: "i"(NP - 1) : "memory");
        if (n > (u32)(NP - 1)) F[F_LANDED] = n - (u32)(NP - 1);
      }
    };
    typedef std::integral_constant<int, INFLW / 32> P32;
    typedef std::integral_constant<int, INFLW / 16> P16;
    typedef std::integral_constant<int, (INFLW / 8 > 32 ? 32 : INFLW / 8)> P8;
    const u32 vg = (lane >> 2) * 9216 + (lane & 3) * 16, vm32 = (lane >> 1) * 6912 + (lane & 1) * 16;
    const u32 v128o = (lane >> 3) * 1152 + (lane & 7) * 16;
    u32x4 srd0; u32 v0, step0, cnt0;
    if (isF) { srd0 = mk_srd(p.wg + f * 64, DM * 9216 - f * 64); v0 = vg; step0 = 16 * 9216; cnt0 = 36; }
    else if (isQ) { srd0 = mk_srd(p.wq + (xg * 512 + u * 64) * 2, DM * 8192 - (xg * 512 + u * 64) * 2); v0 = (lane >> 3) * 8192 + (lane & 7) * 16; step0 = 8 * 8192; cnt0 = 72; }
    else if (isH) { srd0 = mk_srd(p.wmod + hu * 32, DM * 6912 - hu * 32); v0 = vm32; step0 = 32 * 6912; cnt0 = 18; }
    else { srd0 = mk_srd(p.wq, 0); v0 = 0; step0 = 0; cnt0 = 0; }
    const u32 pre = cnt0 < (u32)P32::value ? cnt0 : (u32)P32::value;
#pragma unroll 1
    for (u32 s = 0; s < pre; ++s) { DMA1(AUX_W, srd0, v0, s * step0, (u32)(L_RING + n * 1024)); ++n; }
    BARRIER();
    STAMP(4);
    if (isF) {
      run(P16(), srd0, v0, step0, pre, 36);
      run(P16(), mk_srd(p.wu + f * 64, DM * 9216 - f * 64), vg, 16 * 9216, 0, 36);
      run(P8(), mk_srd(p.wd + f * 36864, 36864), lane * 16, 1024, 0, 36);
    } else if (isO) {
      if (isQ) run(P8(), srd0, v0, step0, pre, 72); else n = 72;
      run(P8(), mk_srd(p.wo + xg * 512 * 1152 + u * 128, 512 * 1152 - u * 128), v128o, 8 * 1152, 0, 64);
    } else if (isH) {
      run(P32(), srd0, v0, step0, pre, 18);
      run(P32(), mk_srd(p.wmod + 1152 + hu * 32, DM * 6912 - 1152 - hu * 32), vm32, 32 * 6912, 0, 18);
      run(P8(), mk_srd(p.wmod + 2304 + hu * 128, DM * 6912 - 2304 - hu * 128), (lane >> 3) * 6912 + (lane & 7) * 16, 8 * 6912, 0, 72);
    }
    STAMP(1);
    const size_t kvoff = ((size_t)(bb * HKV + hkv) * SK + (size_t)key0) * 256;
    const u32x4 srd_k = mk_srd(p.kc + kvoff, (u32)nsl * 16384u), srd_v = mk_srd(p.vc + kvoff, (u32)nsl * 16384u);
    u32 vk[4], vv[2];
#pragma unroll
    for (int j = 0; j < 4; ++j) vk[j] = g * 256 + ((r16 ^ (4 * j + g)) << 4);
#pragma unroll
    for (int j = 0; j < 2; ++j) vv[j] = g * 256 + ((r16 ^ ((g | (j << 2)) << 1)) << 4);
    const u32 wneed = isF ? 0x03332211u : isO ? 0x33221111u : isH ? 0x02222211u : 0u;
    auto wait_window = [&](u32 N) {
      asm volatile("s_waitcnt vmcnt(36)\n\ts_cmp_ge_u32 %0, 36\n\ts_cbranch_scc1 9f\n\t"
                   "s_waitcnt vmcnt(32)\n\ts_cmp_ge_u32 %0, 32\n\ts_cbranch_scc1 9f\n\t"
                   "s_waitcnt vmcnt(28)\n\ts_cmp_ge_u32 %0, 28\n\ts_cbranch_scc1 9f\n\t"
                   "s_waitcnt vmcnt(24)\n\ts_cmp_ge_u32 %0, 24\n\ts_cbranch_scc1 9f\n\t"
                   "s_waitcnt vmcnt(20)\n\ts_cmp_ge_u32 %0, 20\n\ts_cbranch_scc1 9f\n\t"
                   "s_waitcnt vmcnt(16)\n\ts_cmp_ge_u32 %0, 16\n\ts_cbranch_scc1 9f\n\t"
                   "s_waitcnt vmcnt(12)\n9:" :: "s"(N) : "scc", "memory");
    };
    int t16 = 0, tau = 0;
    u32 fr_next = 0, in_next = 0; int pub = (int)n - 31 > 0 ? (int)n - 31 : 0;
#pragma unroll 1
    for (int q = 0; q < 2 * nsl; ++q) {
      const int pos = (q + roff) & 7;
      if (q >= 8) {
        if (fr_next < (u32)(q - 7) && F[F_FREE + pos] < (u32)(q - 7)) {
          WAIT_VM0(); F[F_LANDED] = n; pub = (int)n; lds_wait_ge(F, F_FREE + pos, (u32)(q - 7));
        }
      } else {
        const int nb = (wneed >> (4 * pos)) & 15;
#pragma unroll 1
        for (int i = 0; i < nb; ++i)
          if (F[F_WREL + i] < 7u) { WAIT_VM0(); F[F_LANDED] = n; pub = (int)n; lds_wait_ge(F, F_WREL + i, 7u); }
      }
      u32 N = __builtin_amdgcn_readfirstlane(in_next ? in_next : (u32)(INFL - 4));
      if (q < EARLY_Q) N = EARLY_N;
      if (q >= 16) {
        const int now = (int)((u32)__builtin_amdgcn_s_memrealtime() - t_entry);
        if (q == 16) t16 = now;
        if (q == SCHED_Q0) tau = (now - t16) * SCHED_MIX / (SCHED_Q0 - 16) + SCHED_TAU * (16 - SCHED_MIX) / 16;
        if (q >= SCHED_Q0) {
          const int lag = now - SCHED_T16 - (((112 - (2 * nsl - q)) * tau) >> 4);
          if (lag > SCHED_L1) N = INFL;
        }
      }
      fr_next = F[F_FREE + ((pos + 1) & 7)];
      in_next = F[F_INFL];
      const u32 lb = L_RING + pos * 16384;
      const bool isk = q < nsl;
      const u32x4 srd = isk ? srd_k : srd_v;
      const u32 v0_ = isk ? vk[0] : vv[0], v1_ = isk ? vk[1] : vv[0], v2_ = isk ? vk[2] : vv[1], v3_ = isk ? vk[3] : vv[1];
      const u32 so = (u32)(isk ? q : q - nsl) * 16384u;
#pragma unroll
      for (int gq = 0; gq < 4; ++gq) {
        DMA4(AUX_K, srd, v0_, v1_, v2_, v3_, so + gq * 4096, lb + gq * 4096);
        n += 4;
        wait_window(N);
        pub = max(pub, (int)n - (int)N); F[F_LANDED] = (u32)pub;
      }
    }
    WAIT_VM0();
    F[F_LANDED] = n;
    STAMP(3);
    __builtin_amdgcn_s_setprio(0);
    if (isF) { lds_wait_ge(F, F_ALIVE, 1u); load_cbsb(); }
  } else if (w == 7) {
    const u64 epoch = read_epoch();
    const u32 tag = (u32)epoch;
    const u32x4 z4 = {0u, 0u, 0u, 0u};
    const u32x2 s2 = {SENT, SENT};
    if (isH) {
#pragma unroll
      for (int i = 0; i < 2; ++i) st64(rs, WS_MODT + (lane & 7) * 4608 + hu * 128 + (2 * i + (r16 >> 3)) * 32 + g * 8, s2);
    }
    BARRIER();
    st128(rs, WS_OP + c * 1024 + lane * 16, z4);
    if (lane == 0) st128(rs, WS_ST + c * 16, z4);
    if (isO) {
#pragma unroll
      for (int i = 0; i < 2; ++i) st128(rs, WS_WP + (xg * 144 + u * 16) * 128 + i * 1024 + lane * 16, z4);
    }
    if (isF) {
      st64(rs, WS_X1 + (lane >> 3) * 9216 + f * 64 + (lane & 7) * 8, s2);
#pragma unroll 1
      for (int i = 0; i < 9; ++i) st128(rs, WS_FP + f * 9216 + i * 1024 + lane * 16, u32x4{SENT, SENT, SENT, SENT});
    }
    WAIT_VM0();
    F[F_INIT] = 1u;
    if (lane == 0) __hip_atomic_store((GAS u64*)(p.ws + WS_ALIVE) + c, epoch, __ATOMIC_RELAXED, __HIP_MEMORY_SCOPE_AGENT);
    STAMP(1);
    if (isQ) {
      f32x4 acc[4];
      q_partial(2, tag, acc);
      lds_add(F_WREL + 1, 1); lds_add(F_WREL + 2, 1);
      lds_wait_ge(F, F_P2, 2u);
      const int ls = (g * 8 + (lane & 7)) * 16;
#pragma unroll
      for (int ct = 0; ct < 4; ++ct) {
        const f32x4 a = (*(LAS f32x4*)(L + L_P2 + ct * 512 + ls) + *(LAS f32x4*)(L + L_P2 + 2048 + ct * 512 + ls)) + acc[ct];
        if (r16 < 8) st128(rs, WS_Q + ((2 * xg + (u >> 2)) * 8 + r16) * 1024 + ((u & 3) * 16 + ct * 4 + g) * 16, u32x4{pack2(a[0], a[1]), tag, pack2(a[2], a[3]), tag});
      }
      STAMP(4);
    } else {
      lds_add(F_WREL + 0, 1); lds_add(F_WREL + 1, 1); lds_add(F_WREL + 2, 1);
    }
    {
      u32x4 qv; u32 spins = 0;
      for (;;) {
        MEMBAR();
        qv = ld128(rs, WS_Q + (hkv * 8 + bb) * 1024 + lane * 16);
        if (__all(((qv.y ^ tag) | (qv.w ^ tag)) == 0u) || !spin_ok(F, spins)) break;
        __builtin_amdgcn_s_sleep(QPOLL_SLEEP);
      }
      *(LAS u32x2*)(L + L_QS + lane * 8) = u32x2{qv.x, qv.z};
      WAIT_LGKM0();
      F[F_Q] = 1u;
    }
    STAMP(5);
    auto pace = [&]() {
    };
    auto pace_until = [&](int idx, u32 need) {
      u32 spins = 0;
      while (F[idx] < need) {
        pace();
        for (int i_ = 0; i_ < PACE_SLEEP && F[idx] < need; ++i_) __builtin_amdgcn_s_sleep(8);
        if (F[F_ABORT] != 0u || ++spins > GLB_SPIN_CAP) break;
      }
      MEMBAR();
    };
    pace_until(F_LANDED, Wtot + 16u * (u32)(nsl - 8));
    {
      const u32 ehi = (u32)(epoch >> 32);
      u32 spins = 0;
      for (;;) {
        MEMBAR();
        const u32x4 a = ld128(rs, WS_ALIVE + lane * 32), b2 = ld128(rs, WS_ALIVE + lane * 32 + 16);
        const u32 bad = (a.x ^ tag) | (a.y ^ ehi) | (a.z ^ tag) | (a.w ^ ehi) | (b2.x ^ tag) | (b2.y ^ ehi) | (b2.z ^ tag) | (b2.w ^ ehi);
        if (__all(bad == 0u) || !spin_ok(F, spins)) break;
        __builtin_amdgcn_s_sleep(64);
      }
    }
    F[F_ALIVE] = 1u;
    if (c == 0 && lane == 0) __hip_atomic_store((GAS u64*)(p.ws + WS_CTRL), epoch, __ATOMIC_RELAXED, __HIP_MEMORY_SCOPE_AGENT);
    {
      pace_until(F_L, 6u);
      const int hd = lane & 1;
      float M = *(LAS float*)(L + L_MX + hd * 4), Ls = *(LAS float*)(L + L_LW + hd * 4);
#pragma unroll
      for (int i = 1; i < 6; ++i) { M = fmaxf(M, *(LAS float*)(L + L_MX + i * 8 + hd * 4)); Ls += *(LAS float*)(L + L_LW + i * 8 + hd * 4); }
      if (lane < 2) st64(rs, WS_ST + c * 16 + hd * 8, u32x2{vld(M), vld(Ls)});
      u32x2 o; u32 spins = 0;
      for (;;) {
        MEMBAR();
        o = ld64(rs, WS_ST + (c ^ 8) * 16 + hd * 8);
        if (__all((o.x & o.y & 1u) != 0u) || !spin_ok(F, spins)) break;
        __builtin_amdgcn_s_sleep(8);
      }
      const float m0 = __uint_as_float(vld(M)), l0 = __uint_as_float(vld(Ls)), m1 = __uint_as_float(o.x), l1 = __uint_as_float(o.y);
      const float Mg = fmaxf(m0, m1);
      const float e0 = __builtin_amdgcn_exp2f((m0 - Mg) * LOG2E), e1 = __builtin_amdgcn_exp2f((m1 - Mg) * LOG2E);
      const float sc = e0 / (e0 * l0 + e1 * l1);
      osA = __builtin_bit_cast(float, __builtin_amdgcn_readlane(__builtin_bit_cast(int, sc), 0));
      osB = __builtin_bit_cast(float, __builtin_amdgcn_readlane(__builtin_bit_cast(int, sc), 1));
    }
    if (isF) {
      load_cbsb();
      const int b_ = lane & 7;
      u32 spins = 0;
      for (;;) {
        MEMBAR();
        gap = ld64(rs, WS_MODT + b_ * 4608 + f * 8);
        gbp = ld64(rs, WS_MODT + b_ * 4608 + 864 * 4 + f * 8);
        if (__all(umax(umax(gap.x, gap.y), umax(gbp.x, gbp.y)) != SENT) || !spin_ok(F, spins)) break;
      }
      xp = *(const GAS u32x2*)(p.x + b_ * DM + f * 4);
    }
    STAMP(7);
  } else {
    const int cw = w - 1;
    if (isF) {
      BARRIER();
      const int m = cw & 1, kt = cw >> 1;
      const u32 a0 = L_RING + 36 * m * 1024 + (kt * 192 + 8 * g + qp) * 64 + 8 * pp;
      if (m == 1) lds_add(F_WREL + 0, 1);
      lds_wait_ge(F, F_LANDED, m == 0 ? 36u : 72u);
#pragma unroll
      for (int s = 0; s < 6; ++s) { wf[2 * s] = tr_frag(L, a0 + s * 2048, 4 * 64); wf[2 * s + 1] = tr_frag(L, a0 + s * 2048 + 32, 4 * 64); }
      if (m == 0) lds_add(F_WREL + 0, 1);
      lds_add(F_WREL + 1, 1);
      lds_wait_ge(F, F_LANDED, 108u);
      const u32 d0 = L_RING + 72 * 1024 + (8 * g + qp) * 1152 + cw * 192 + 8 * pp;
#pragma unroll
      for (int t = 0; t < 6; ++t) wf[12 + t] = tr_frag(L, d0 + t * 32, 4 * 1152);
      lds_add(F_WREL + 2, 1);
    } else if (isO) {
      if (cw < 4) {
        BARRIER();
        lds_add(F_WREL + 0, 1);
        const u32 a0 = L_RING + 72 * 1024 + (8 * g + qp) * 128 + cw * 32 + 8 * pp;
        lds_wait_ge(F, F_LANDED, 104u);
#pragma unroll
        for (int s = 0; s < 8; ++s) wf[s] = tr_frag(L, a0 + s * 4096, 4 * 128);
        lds_add(F_WREL + 1, 1);
        lds_wait_ge(F, F_LANDED, 136u);
#pragma unroll
        for (int s = 8; s < 16; ++s) wf[s] = tr_frag(L, a0 + s * 4096, 4 * 128);
        lds_add(F_WREL + 2, 1);
      } else {
        const u32 tag = (u32)read_epoch();
        BARRIER();
        if (isQ) {
          f32x4 acc[4];
          q_partial(cw - 4, tag, acc);
          if (r16 < 8) {
#pragma unroll
            for (int ct = 0; ct < 4; ++ct) *(LAS f32x4*)(L + L_P2 + (cw - 4) * 2048 + ct * 512 + (g * 8 + r16) * 16) = acc[ct];
          }
          lds_add(F_P2, 1);
        } else lds_add(F_WREL + 0, 1);
        lds_add(F_WREL + 1, 1); lds_add(F_WREL + 2, 1);
      }
    } else if (isH) {
      u32 cv[6], xa[5], xb[5];
      const u32 tag = (u32)read_epoch();
#pragma unroll
      for (int i = 0; i < 6; ++i) cv[i] = ((const GAS u32*)p.c)[cw * 64 + lane + 384 * i];
      const int rowb = 6 + (cw & 1);
      const GAS u32* xr = (const GAS u32*)p.x + cw * 288;
      const GAS u32* xs = (const GAS u32*)p.x + rowb * 288;
#pragma unroll
      for (int i = 0; i < 4; ++i) { xa[i] = xr[lane + 64 * i]; xb[i] = xs[lane + 64 * i]; }
      xa[4] = xr[256 + (lane & 31)]; xb[4] = xs[256 + (lane & 31)];
      const u32x2 xq = *(const GAS u32x2*)(p.x + (lane & 7) * DM + hu * 16 + g * 4);
      BARRIER();
#pragma unroll
      for (int i = 0; i < 6; ++i) {
        const int e = cw * 64 + lane + 384 * i;
        const float a = bflo(cv[i]), b2 = bfhi(cv[i]);
        *(LAS u32*)(L + L_SC + (e / 288) * ACT_STRIDE + (e % 288) * 4) = pack2(a / (1.0f + __expf(-a)), b2 / (1.0f + __expf(-b2)));
      }
      {
        float v[10], mean_r, sd_r;
#pragma unroll
        for (int i = 0; i < 5; ++i) { v[2 * i] = bflo(xa[i]); v[2 * i + 1] = bfhi(xa[i]); }
        ln_stats(v, lane < 32, mean_r, sd_r);
        if (lane == 0) { *(LAS float*)(L + L_STAT + cw * 8) = mean_r; *(LAS float*)(L + L_STAT + cw * 8 + 4) = sd_r; }
        if (cw < 2) {
#pragma unroll
          for (int i = 0; i < 5; ++i) { v[2 * i] = bflo(xb[i]); v[2 * i + 1] = bfhi(xb[i]); }
          ln_stats(v, lane < 32, mean_r, sd_r);
          if (lane == 0) { *(LAS float*)(L + L_STAT + rowb * 8) = mean_r; *(LAS float*)(L + L_STAT + rowb * 8 + 4) = sd_r; }
        }
      }
      lds_add(F_SC, 1);
      STAMP(24);
      const u32 sb0 = L_SC + (lane & 7) * ACT_STRIDE + g * 16;
      {
        const int m = cw & 1, kt = cw >> 1;
        lds_wait_ge(F, F_SC, 6u);
        const float mean_r = *(LAS float*)(L + L_STAT + (lane & 7) * 8), sd_r = *(LAS float*)(L + L_STAT + (lane & 7) * 8 + 4);
        const float n0 = rb(rb(bflo(xq.x) - mean_r) / sd_r), n1 = rb(rb(bfhi(xq.x) - mean_r) / sd_r);
        const float n2 = rb(rb(bflo(xq.y) - mean_r) / sd_r), n3 = rb(rb(bfhi(xq.y) - mean_r) / sd_r);
        float n0p = n0, n1p = n1, n2p = n2, n3p = n3;
        asm volatile("" : "+v"(n0p), "+v"(n1p), "+v"(n2p), "+v"(n3p));
        u32x4 bfr[6];
#pragma unroll
        for (int s = 0; s < 6; ++s) bfr[s] = lds128(L, sb0 + (kt * 6 + s) * 64);
        lds_wait_ge(F, F_LANDED, 36u);
        STAMP(25);
        const u32 a0 = L_RING + (m * 18 + kt * 6) * 1024 + (8 * g + qp) * 32 + 8 * pp;
        f32x4 acc = {0, 0, 0, 0}, acc2 = {0, 0, 0, 0};
#pragma unroll
        for (int s = 0; s < 6; s += 2) { acc = mfma(tr_frag(L, a0 + s * 1024, 4 * 32), bfr[s], acc); acc2 = mfma(tr_frag(L, a0 + (s + 1) * 1024, 4 * 32), bfr[s + 1], acc2); }
        acc += acc2;
        if (r16 < 8) *(LAS f32x4*)(L + L_RED + cw * 512 + (g * 8 + r16) * 16) = acc;
        lds_add(F_P1, 1);
        lds_add(F_WREL + 0, 1);
        if (cw == 5) {
          lds_wait_ge(F, F_P1, 6u);
          const int ls = (g * 8 + (lane & 7)) * 16;
          const f32x4 sa = *(LAS f32x4*)(L + L_RED + 0 * 512 + ls) + *(LAS f32x4*)(L + L_RED + 2 * 512 + ls) + *(LAS f32x4*)(L + L_RED + 4 * 512 + ls);
          const f32x4 ca = *(LAS f32x4*)(L + L_RED + 1 * 512 + ls) + *(LAS f32x4*)(L + L_RED + 3 * 512 + ls) + *(LAS f32x4*)(L + L_RED + 5 * 512 + ls);
          const float h0 = rb(rb(n0p * rb(1.0f + rb(ca[0]))) + rb(sa[0])), h1 = rb(rb(n1p * rb(1.0f + rb(ca[1]))) + rb(sa[1]));
          const float h2 = rb(rb(n2p * rb(1.0f + rb(ca[2]))) + rb(sa[2])), h3 = rb(rb(n3p * rb(1.0f + rb(ca[3]))) + rb(sa[3]));
          const u32x4 hu4 = {pack2(h0, h1), tag, pack2(h2, h3), tag};
#pragma unroll
          for (int i = 0; i < REP_H / 2; ++i)
            st128(rs, WS_H + (2 * i + (r16 >> 3)) * 18432 + ((lane & 7) * 144 + hu * 4 + g) * 16, hu4);
          STAMP(6);
        }
      }
      if (cw < 4) {
        lds_wait_ge(F, F_LANDED, 108u);
        const u32 a0 = L_RING + 36 * 1024 + (8 * g + qp) * 128 + cw * 32 + 8 * pp;
        f32x4 a0c = {0, 0, 0, 0}, a1c = {0, 0, 0, 0};
#pragma unroll 3
        for (int s = 0; s < 18; s += 2) {
          a0c = mfma(tr_frag(L, a0 + s * 4096, 4 * 128), lds128(L, sb0 + s * 64), a0c);
          a1c = mfma(tr_frag(L, a0 + (s + 1) * 4096, 4 * 128), lds128(L, sb0 + (s + 1) * 64), a1c);
        }
        const f32x4 a = a0c + a1c;
        lds_add(F_WREL + 1, 1);
        lds_wait_ge(F, F_INIT, 1u);
        if (r16 < 8) st64(rs, WS_MODT + r16 * 4608 + hu * 128 + cw * 32 + g * 8, u32x2{nosent(pack2(a[0], a[1])), nosent(pack2(a[2], a[3]))});
      } else lds_add(F_WREL + 1, 1);
      lds_add(F_WREL + 2, 1);
    } else {
      BARRIER();
      lds_add(F_WREL + 0, 1); lds_add(F_WREL + 1, 1); lds_add(F_WREL + 2, 1);
    }
    STAMP(1);
    u32x4 qf[4];
    u32 ka[4];
#pragma unroll
    for (int s = 0; s < 4; ++s) ka[s] = r16 * 256 + (((4 * s + g) ^ r16) << 4);
    float mx0 = -INFINITY, mx1 = -INFINITY;
#pragma unroll 1
    for (int q = cw; q < nsl; q += 6) {
      const int pos = (q + roff) & 7;
      lds_wait_ge(F, F_LANDED, Wtot + 16u * (u32)(q + 1));
      const u32 sb = L_RING + pos * 16384;
      u32x4 kf[16];
#pragma unroll
      for (int t = 0; t < 4; ++t)
#pragma unroll
        for (int s = 0; s < 4; ++s) kf[t * 4 + s] = lds128(L, sb + ka[s] + t * 4096);
      WAIT_LGKM0();
      if (lane == 0) F[F_FREE + pos] = (u32)(q + 1);
      if (q < 6) {
        lds_wait_ge(F, F_Q, 1u);
        lds_wait_ge(F, F_WREL + 1, 7u); lds_wait_ge(F, F_WREL + 2, 7u);
        STAMP(2);
#pragma unroll
        for (int s = 0; s < 4; ++s) qf[s] = lds128(L, L_QS + (lane & 1) * 256 + s * 64 + g * 16);
      }
      f32x4 acc[4];
#pragma unroll
      for (int t = 0; t < 4; ++t) {
        acc[t] = f32x4{0, 0, 0, 0};
#pragma unroll
        for (int s = 0; s < 4; ++s) acc[t] = mfma(qf[s], kf[t * 4 + s], acc[t]);
      }
      float s0 = g == 0 ? acc[0][0] : g == 1 ? acc[1][0] : g == 2 ? acc[2][0] : acc[3][0];
      float s1 = g == 0 ? acc[0][1] : g == 1 ? acc[1][1] : g == 2 ? acc[2][1] : acc[3][1];
      s0 = rb(rb(s0) * 0.08838834764831845f);
      s1 = rb(rb(s1) * 0.08838834764831845f);
      mx0 = fmaxf(mx0, s0); mx1 = fmaxf(mx1, s1);
      *(LAS u16*)(L + L_SC + q * 256 + lane * 2) = (u16)(__float_as_uint(s0) >> 16);
      *(LAS u16*)(L + L_SC + q * 256 + 128 + lane * 2) = (u16)(__float_as_uint(s1) >> 16);
    }
    STAMP(3);
    {
      mx0 = wave_max(mx0); mx1 = wave_max(mx1);
      if (lane == 0) *(LAS u32x2*)(L + L_MX + cw * 8) = u32x2{__float_as_uint(mx0), __float_as_uint(mx1)};
      lds_add(F_MX, 1);
      lds_wait_ge(F, F_MX, 6u);
#pragma unroll
      for (int i = 0; i < 6; ++i) { mx0 = fmaxf(mx0, *(LAS float*)(L + L_MX + i * 8)); mx1 = fmaxf(mx1, *(LAS float*)(L + L_MX + i * 8 + 4)); }
      float l0 = 0.f, l1 = 0.f;
#pragma unroll 1
      for (int q = cw; q < nsl; q += 6) {
        const float s0 = bf2f(*(LAS u16*)(L + L_SC + q * 256 + lane * 2)), s1 = bf2f(*(LAS u16*)(L + L_SC + q * 256 + 128 + lane * 2));
        const float p0 = __builtin_amdgcn_exp2f((s0 - mx0) * LOG2E), p1 = __builtin_amdgcn_exp2f((s1 - mx1) * LOG2E);
        l0 += p0; l1 += p1;
        *(LAS u16*)(L + L_SC + q * 256 + lane * 2) = (u16)f2bf(p0);
        *(LAS u16*)(L + L_SC + q * 256 + 128 + lane * 2) = (u16)f2bf(p1);
      }
      l0 = wave_sum(l0); l1 = wave_sum(l1);
      if (lane == 0) *(LAS u32x2*)(L + L_LW + cw * 8) = u32x2{__float_as_uint(l0), __float_as_uint(l1)};
      lds_add(F_L, 1);
    }
    f32x4 o[8];
#pragma unroll
    for (int cb = 0; cb < 8; ++cb) o[cb] = f32x4{0, 0, 0, 0};
    const u32 va0 = (8 * g + qp) * 256 + ((pp >> 1) << 4) + 8 * (pp & 1) + (((qp | ((g & 1) << 2)) << 1) << 4);
#pragma unroll 1
    for (int q = cw; q < nsl; q += 6) {
      const int pos = (q + nsl + roff) & 7;
      if (isF && q >= 24 && q < 30) { lds_wait_ge(F, F_ALIVE, 1u); load_cbsb(); }
      const u32x4 pf0 = lds128(L, L_SC + q * 256 + (lane & 1) * 128 + g * 16), pf1 = lds128(L, L_SC + q * 256 + (lane & 1) * 128 + 64 + g * 16);
      lds_wait_ge(F, F_LANDED, Wtot + 16u * (u32)(q + nsl + 1));
      const u32 sb = L_RING + pos * 16384 + va0;
      u32x4 vf[8], vg2[8];
#pragma unroll
      for (int cb = 0; cb < 8; ++cb) vf[cb] = tr_frag(L, sb ^ (cb << 5), 1024);
#pragma unroll
      for (int cb = 0; cb < 8; ++cb) vg2[cb] = tr_frag(L, (sb ^ (cb << 5)) + 8192, 1024);
      WAIT_LGKM0();
      if (lane == 0) F[F_FREE + pos] = (u32)(q + nsl + 1);
#pragma unroll
      for (int cb = 0; cb < 8; ++cb) o[cb] = mfma(pf0, vf[cb], o[cb]);
#pragma unroll
      for (int cb = 0; cb < 8; ++cb) o[cb] = mfma(pf1, vg2[cb], o[cb]);
    }
    STAMP(4);
    if (g == 0) {
#pragma unroll
      for (int cb = 0; cb < 8; ++cb) *(LAS u32x2*)(L + L_ACT + cw * 1024 + (cb * 16 + r16) * 8) = u32x2{__float_as_uint(o[cb][0]), __float_as_uint(o[cb][1])};
    }
  }
  BARRIER();
  if (w != 0) STAMP(8);
  if (w == 7) {
    f32x4 O = *(LAS f32x4*)(L + L_ACT + lane * 16);
#pragma unroll
    for (int i = 1; i < 6; ++i) O += *(LAS f32x4*)(L + L_ACT + i * 1024 + lane * 16);
    st128(rs, WS_OP + c * 1024 + lane * 16, u32x4{vld(O[0] * osA), vld(O[1] * osB), vld(O[2] * osA), vld(O[3] * osB)});
    if (w != 0) STAMP(9);
  }
  if (isO) {
    constexpr int L_OS = L_RING;
    u32x4 ov[4];
    bool got[4] = {false, false, false, false};
    u32 spins = 0;
    for (;;) {
      MEMBAR();
#pragma unroll
      for (int i = 0; i < 4; ++i) if (!got[i]) ov[i] = ld128(rs, WS_OP + (xg + 8 * ((i >> 1) * 16 + 2 * w + (i & 1))) * 1024 + lane * 16);
      bool all = true;
#pragma unroll
      for (int i = 0; i < 4; ++i) if (!got[i]) {
        got[i] = __all((ov[i].x & ov[i].y & ov[i].z & ov[i].w & 1u) != 0u);
        all = all && got[i];
      }
      if (all || !spin_ok(F, spins)) break;
    }
#pragma unroll
    for (int hk = 0; hk < 2; ++hk) {
      const f32x4 s = __builtin_bit_cast(f32x4, ov[2 * hk]) + __builtin_bit_cast(f32x4, ov[2 * hk + 1]);
      *(LAS u32*)(L + L_OS + w * 1040 + hk * 512 + lane * 4) = pack2(s[0], s[2]);
      *(LAS u32*)(L + L_OS + w * 1040 + hk * 512 + 256 + lane * 4) = pack2(s[1], s[3]);
    }
    BARRIER();
    if (w != 0) STAMP(10);
    if (w >= 1 && w <= 4) {
      const int ct = w - 1;
      const u32 ob = L_OS + (lane & 7) * 1040 + g * 16;
      u32x4 bfr[16];
#pragma unroll
      for (int s = 0; s < 16; ++s) bfr[s] = lds128(L, ob + s * 64);
      f32x4 a0 = {0, 0, 0, 0}, a1 = {0, 0, 0, 0}, a2 = {0, 0, 0, 0}, a3 = {0, 0, 0, 0};
#pragma unroll
      for (int s = 0; s < 16; s += 4) { a0 = mfma(wf[s], bfr[s], a0); a1 = mfma(wf[s + 1], bfr[s + 1], a1); a2 = mfma(wf[s + 2], bfr[s + 2], a2); a3 = mfma(wf[s + 3], bfr[s + 3], a3); }
      const f32x4 a = (a0 + a1) + (a2 + a3);
      if (r16 < 8) st128(rs, WS_WP + (xg * 144 + (u * 4 + ct) * 4 + g) * 128 + r16 * 16, u32x4{vld(a[0]), vld(a[1]), vld(a[2]), vld(a[3])});
      if (w != 0) STAMP(11);
    }
  } else if (isF) {
    float x1r[4];
    if (w == 7) {
      u32x4 pv; u32 spins = 0;
      for (;;) {
        MEMBAR();
        pv = ld128(rs, WS_WP + ((lane >> 3) * 144 + f) * 128 + (lane & 7) * 16);
        if (__all((pv.x & pv.y & pv.z & pv.w & 1u) != 0u) || !spin_ok(F, spins)) break;
      }
      const float a0 = rb(sum_x8_16_32(__uint_as_float(pv.x))), a1 = rb(sum_x8_16_32(__uint_as_float(pv.y)));
      const float a2 = rb(sum_x8_16_32(__uint_as_float(pv.z))), a3 = rb(sum_x8_16_32(__uint_as_float(pv.w)));
      x1r[0] = rb(bflo(xp.x) + rb(bflo(gap.x) * a0)); x1r[1] = rb(bfhi(xp.x) + rb(bfhi(gap.x) * a1));
      x1r[2] = rb(bflo(xp.y) + rb(bflo(gap.y) * a2)); x1r[3] = rb(bfhi(xp.y) + rb(bfhi(gap.y) * a3));
      const u32x2 xo = {nosent(pack2(x1r[0], x1r[1])), nosent(pack2(x1r[2], x1r[3]))};
      st64(rs, WS_X1 + (lane >> 3) * 9216 + f * 64 + (lane & 7) * 8, xo);
      F[F_X1] = 1u;
      if (w != 0) STAMP(10);
    } else lds_wait_ge(F, F_X1, 1u);
    if (DELAY_C) __builtin_amdgcn_s_sleep(DELAY_C);
    {
      const int xb = WS_X1 + xg * 9216;
      u32x4 xv, xw = {0u, 0u, 0u, 0u}; u32 spins = 0;
      for (;;) {
        MEMBAR();
        xv = ld128(rs, xb + tid * 16);
        if (w == 0) xw = ld128(rs, xb + 8192 + lane * 16);
        if (__all(umax(umax4(xv), umax4(xw)) != SENT) || !spin_ok(F, spins)) break;
      }
      if (w != 0) STAMP(16);
      {
        const u32 a = L_ACT + (tid & 3) * 2 * ACT_STRIDE + (tid >> 2) * 8;
        *(LAS u32x2*)(L + a) = u32x2{xv.x, xv.y}; *(LAS u32x2*)(L + a + ACT_STRIDE) = u32x2{xv.z, xv.w};
        if (w == 0) { *(LAS u32x2*)(L + a + 1024) = u32x2{xw.x, xw.y}; *(LAS u32x2*)(L + a + 1024 + ACT_STRIDE) = u32x2{xw.z, xw.w}; }
      }
      BARRIER();
      if (w != 0) STAMP(17);
      u32 xr[5];
#pragma unroll
      for (int i = 0; i < 4; ++i) xr[i] = *(LAS u32*)(L + L_ACT + w * ACT_STRIDE + (lane + 64 * i) * 4);
      xr[4] = *(LAS u32*)(L + L_ACT + w * ACT_STRIDE + (256 + (lane & 31)) * 4);
      float v[10];
#pragma unroll
      for (int i = 0; i < 5; ++i) { v[2 * i] = bflo(xr[i]); v[2 * i + 1] = bfhi(xr[i]); }
      float s = (v[0] + v[1]) + (v[2] + v[3]) + ((v[4] + v[5]) + (v[6] + v[7]));
      if (lane < 32) s += v[8] + v[9];
      const float mean = wave_sum(s) * (1.0f / DM);
      float q2 = 0.f, q3 = 0.f;
#pragma unroll
      for (int i = 0; i < 4; ++i) { const float d0 = v[2 * i] - mean, d1 = v[2 * i + 1] - mean; q2 += d0 * d0; q3 += d1 * d1; }
      if (lane < 32) { const float d0 = v[8] - mean, d1 = v[9] - mean; q2 += d0 * d0; q3 += d1 * d1; }
      const float var = wave_sum(q2 + q3) * (1.0f / DM);
      const float mean_r = rb(mean);
      const float rinv = __builtin_amdgcn_rcpf(rb(__builtin_amdgcn_sqrtf(rb(rb(var) + 1e-5f))));
#pragma unroll
      for (int i = 0; i < 5; ++i) {
        const u32 dp = pack2(v[2 * i] - mean_r, v[2 * i + 1] - mean_r);
        const u32 np = pack2(bflo(dp) * rinv, bfhi(dp) * rinv);
        const u32 tp = pack2(bflo(np) * bflo(cbp[i]), bfhi(np) * bfhi(cbp[i]));
        const u32 hp = pack2(bflo(tp) + bflo(sbp[i]), bfhi(tp) + bfhi(sbp[i]));
        if (i < 4) *(LAS u32*)(L + L_ACT + w * ACT_STRIDE + (lane + 64 * i) * 4) = hp;
        else if (lane < 32) *(LAS u32*)(L + L_ACT + w * ACT_STRIDE + (256 + lane) * 4) = hp;
      }
      if (w != 0) STAMP(18);
    }
    BARRIER();
    if (w != 0) STAMP(11);
    constexpr int L_R2 = L_RING;
    constexpr int L_HH = L_RING + 8192;
    if (w == 7) {
      BARRIER();
      BARRIER();
      const int off = WS_FP + (lane >> 3) * 9216 + ((f >> 3) * 4 + (f & 3)) * 128 + (lane & 7) * 16 + ((f >> 2) & 1) * 8;
      u32x2 e[18]; u32 spins = 0;
      u32 slo = 0x00003f80u, shi = 0x3f800000u;
      asm volatile("" : "+v"(slo), "+v"(shi));
      if (DELAY_E) __builtin_amdgcn_s_sleep(DELAY_E);
      for (;;) {
        MEMBAR();
#pragma unroll
        for (int i = 0; i < 18; ++i) e[i] = ld64(rs, off + i * 8 * 9216);
        u32 mx = 0;
#pragma unroll
        for (int i = 0; i < 18; ++i) mx = umax(mx, umax(e[i].x, e[i].y));
        if (__all(mx != SENT) || !spin_ok(F, spins)) break;
      }
      if (w != 0) STAMP(23);
      f32x4 s = {0, 0, 0, 0}, s1 = {0, 0, 0, 0};
#pragma unroll
      for (int i = 0; i < 18; i += 2) {
        s[0] = add_sel(e[i].x, slo, s[0]); s[1] = add_sel(e[i].x, shi, s[1]); s[2] = add_sel(e[i].y, slo, s[2]); s[3] = add_sel(e[i].y, shi, s[3]);
        s1[0] = add_sel(e[i + 1].x, slo, s1[0]); s1[1] = add_sel(e[i + 1].x, shi, s1[1]); s1[2] = add_sel(e[i + 1].y, slo, s1[2]); s1[3] = add_sel(e[i + 1].y, shi, s1[3]);
      }
      s += s1;
      const float d0 = rb(sum_x8_16_32(s[0])), d1 = rb(sum_x8_16_32(s[1])), d2 = rb(sum_x8_16_32(s[2])), d3 = rb(sum_x8_16_32(s[3]));
      const float o0 = x1r[0] + rb(bflo(gbp.x) * d0), o1 = x1r[1] + rb(bfhi(gbp.x) * d1);
      const float o2 = x1r[2] + rb(bflo(gbp.y) * d2), o3 = x1r[3] + rb(bfhi(gbp.y) * d3);
      if (lane < 8) *(GAS u32x2*)(p.out + lane * DM + f * 4) = u32x2{pack2(o0, o1), pack2(o2, o3)};
      if (w != 0) STAMP(13);
    } else {
      const int cw = w - 1;
      if (w >= 1) {
        const int kt = cw >> 1;
        const u32 hb = L_ACT + (lane & 7) * ACT_STRIDE + kt * 384 + g * 16;
        u32x4 bfr[6];
#pragma unroll
        for (int s = 0; s < 6; ++s) bfr[s] = lds128(L, hb + s * 64);
        f32x4 a0 = {0, 0, 0, 0}, a1 = {0, 0, 0, 0}, a2 = {0, 0, 0, 0}, a3 = {0, 0, 0, 0};
#pragma unroll
        for (int s = 0; s < 6; s += 2) { a0 = mfma(wf[2 * s], bfr[s], a0); a1 = mfma(wf[2 * s + 1], bfr[s], a1); a2 = mfma(wf[2 * s + 2], bfr[s + 1], a2); a3 = mfma(wf[2 * s + 3], bfr[s + 1], a3); }
        a0 += a2; a1 += a3;
        if (r16 < 8) { *(LAS f32x4*)(L + L_R2 + cw * 1024 + (g * 8 + r16) * 16) = a0; *(LAS f32x4*)(L + L_R2 + cw * 1024 + 512 + (g * 8 + r16) * 16) = a1; }
      }
      if (w != 0) STAMP(19);
      BARRIER();
      if (w != 0) STAMP(20);
      if (w < 4) {
        const int ct = w >> 1, e = (w & 1) * 64 + lane;
        const u32 a = L_R2 + ct * 512 + e * 4;
        const float gg = rb(*(LAS float*)(L + a) + *(LAS float*)(L + a + 2048) + *(LAS float*)(L + a + 4096));
        const float uu = rb(*(LAS float*)(L + a + 1024) + *(LAS float*)(L + a + 3072) + *(LAS float*)(L + a + 5120));
        const float inner = 0.7978845608028654f * (gg + 0.044715f * gg * gg * gg);
        const float th = 1.0f - 2.0f * __builtin_amdgcn_rcpf(1.0f + __expf(2.0f * inner));
        const float ge = rb(0.5f * gg * (1.0f + th));
        const int ls = e >> 2, rr = e & 3, b_ = ls & 7, g_ = ls >> 3;
        *(LAS u16*)(L + L_HH + b_ * 80 + (ct * 16 + g_ * 4 + rr) * 2) = (u16)f2bf(ge * uu);
      }
      if (w != 0) STAMP(21);
      BARRIER();
      if (w != 0) STAMP(22);
      if (w >= 1) {
        const u32x4 bfr = lds128(L, L_HH + (lane & 7) * 80 + g * 16);
        f32x4 a[6];
#pragma unroll
        for (int t = 0; t < 6; ++t) a[t] = mfma(wf[12 + t], bfr, f32x4{0, 0, 0, 0});
#pragma unroll
        for (int t = 0; t < 6; t += 2)
          if (r16 < 8) st128(rs, WS_FP + f * 9216 + ((cw * 3 + (t >> 1)) * 4 + g) * 128 + r16 * 16,
                             u32x4{nosent(pack2(a[t][0], a[t][1])), nosent(pack2(a[t][2], a[t][3])), nosent(pack2(a[t + 1][0], a[t + 1][1])), nosent(pack2(a[t + 1][2], a[t + 1][3]))});
      }
      if (w != 0) STAMP(12);
    }
  }
}

}

extern "C" void kernel_launch(void* const* d_in, const int* in_sizes, int n_in, void* d_out, int out_size, void* d_ws, size_t ws_size,
                              hipStream_t stream) {
  (void)in_sizes; (void)n_in; (void)out_size; (void)ws_size;
  Params p;
  p.x = (const u16*)d_in[0];
  p.kc = (const unsigned char*)d_in[1];
  p.vc = (const unsigned char*)d_in[2];
  p.wq = (const unsigned char*)d_in[3];
  p.wo = (const unsigned char*)d_in[4];
  p.wg = (const unsigned char*)d_in[6];
  p.wu = (const unsigned char*)d_in[7];
  p.wd = (const unsigned char*)d_in[8];
  p.c = (const u16*)d_in[9];
  p.wmod = (const unsigned char*)d_in[10];
  p.out = (u16*)d_out;
  p.ws = (unsigned char*)d_ws;
  (void)hipFuncSetAttribute((const void*)fused_block, hipFuncAttributeMaxDynamicSharedMemorySize, LDS_BYTES);
  hipLaunchKernelGGL(fused_block, dim3(NCU), dim3(NT), LDS_BYTES, stream, p);
}
```

```cpp
#include <hip/hip_runtime.h>
#include <stdint.h>
#include <type_traits>
#pragma clang fp contract(off)

#define INFL 24
#define INFLW 320
#define EARLY_Q 16
#define EARLY_LVL 4
#define BASE_LVL 2
#define BOOST1 1
#define BOOST2 3
#define SCHED 1
#define SCHED_TE 7750
#define SCHED_TAU 876
#define SCHED_Q0 17
#define SCHED_L1 30
#define SCHED_L2 60
#define SCHED_LA 10
#define GROUPW 1
#define STAGE2 0
#define DEFER_DOWN 1
#define SAMP_LAG 6
#define ADAPT 0
#define SHIFT_O 1
#define SHIFT_H 2
#define DELAY_C 24
#define DELAY_E 28
#define WPPOLL_SLEEP 0
#define P1W 10
#define WOW 16
#define WQW 12
#define QGATE 0
#define HGATE 750
#define HPOLL_SLEEP 2
#define QPOLL_SLEEP 2
#define AUX_KV "nt"
#define AUX_W "nt"
#define STAMPS 0

#define LAS __attribute__((address_space(3)))
#define GAS __attribute__((address_space(1)))
typedef unsigned u32;
typedef unsigned long long u64;
typedef unsigned short u16;
typedef u32 u32x4 __attribute__((ext_vector_type(4)));
typedef u32 u32x2 __attribute__((ext_vector_type(2)));
typedef float f32x4 __attribute__((ext_vector_type(4)));
typedef __bf16 bf16x8 __attribute__((ext_vector_type(8)));
typedef short s16x4 __attribute__((ext_vector_type(4)));

namespace {

constexpr int NB = 8, DM = 576, HKV = 16, SK = 8192, DH = 128, DF = 4608;
constexpr int NCU = 256, NT = 512;

constexpr int REP_H = 8;
constexpr int WS_CTRL = 0;
constexpr int WS_ALIVE = 4096;
constexpr int WS_ST = 8192;
constexpr int WS_MODT = WS_ST + 4096;
constexpr int WS_X1 = WS_MODT + 36864;
constexpr int WS_H = WS_X1 + 8 * 9216;
constexpr int WS_Q = WS_H + REP_H * 18432;
constexpr int WS_OP = WS_Q + 131072;
constexpr int WS_WP = WS_OP + 262144;
constexpr int WS_FP = WS_WP + 147456;
constexpr int WS_SAMP = WS_FP + 144 * 9216;
constexpr int WS_END = WS_SAMP + 3 * 32 * 128;
constexpr int WS_DBG = 4 << 20;
static_assert(WS_END <= WS_DBG, "ws");

constexpr int MAXSL = 64 + (SHIFT_O > SHIFT_H ? SHIFT_O : SHIFT_H);
constexpr int L_RING = 0;
constexpr int L_SC = 131072;
constexpr int ACT_STRIDE = 1168;
constexpr int L_P2 = L_SC + 8192;
constexpr int L_H2 = L_SC;
constexpr int L_R2 = L_SC + 9344;
constexpr int L_HH = L_R2 + 6144;
constexpr int L_ACT = L_SC + (MAXSL * 256 > 16384 ? MAXSL * 256 : 16384);
constexpr int L_QS = L_ACT + 9344;
constexpr int L_RED = L_QS + 512;
constexpr int L_STAT = L_RED + 3072;
constexpr int L_MX = L_STAT + 64;
constexpr int L_LW = L_MX + 64;
constexpr int L_FLG = L_LW + 64;
constexpr int LDS_BYTES = 163840;
static_assert(L_FLG + 256 <= LDS_BYTES && L_HH + 640 <= L_ACT, "lds");
constexpr int F_LANDED = 0, F_FREE = 1  , F_WREL = 9  , F_SC = 12, F_P1 = 13, F_INIT = 14, F_Q = 15, F_ABORT = 16, F_ALIVE = 17,
              F_P2 = 18, F_MX = 19, F_L = 20, F_X1 = 21, F_TAGOK = 22, F_TAG = 23, F_OPART = 24, F_CB0 = 25, F_EOWN = 26, F_EBAR = 27;

constexpr u32 SENT = 0xFFFFFFFFu;
constexpr u32 LDS_SPIN_CAP = 1u << 21;
constexpr u32 GLB_SPIN_CAP = 1u << 17;
constexpr float LOG2E = 1.4426950408889634f;

struct Params {
  const u16* x; const unsigned char* kc; const unsigned char* vc; const unsigned char* wq; const unsigned char* wo;
  const unsigned char* wg; const unsigned char* wu; const unsigned char* wd; const u16* c; const unsigned char* wmod;
  u16* out; unsigned char* ws;
};

__device__ __forceinline__ float bf2f(u32 v16) { return __uint_as_float(v16 << 16); }
__device__ __forceinline__ float bflo(u32 d) { return __uint_as_float(d << 16); }
__device__ __forceinline__ float bfhi(u32 d) { return __uint_as_float(d & 0xffff0000u); }
__device__ __forceinline__ float rb(float f) { return (float)(__bf16)f; }
__device__ __forceinline__ u32 f2bf(float f) { return (u32)__builtin_bit_cast(u16, (__bf16)f); }
typedef float f32x2 __attribute__((ext_vector_type(2)));
typedef __bf16 bf16x2 __attribute__((ext_vector_type(2)));
__device__ __forceinline__ u32 pack2(float lo, float hi) { return __builtin_bit_cast(u32, __builtin_convertvector(f32x2{lo, hi}, bf16x2)); }
__device__ __forceinline__ u32 nosent(u32 p) { return p < 0xFFFEFFFFu ? p : 0xFFFEFFFFu; }
__device__ __forceinline__ u32 umax(u32 a, u32 b) { return a > b ? a : b; }
__device__ __forceinline__ u32 umax4(u32x4 v) { return umax(umax(v.x, v.y), umax(v.z, v.w)); }
typedef __bf16 bf16x2_ __attribute__((ext_vector_type(2)));
__device__ __forceinline__ float add_sel(u32 v, u32 sel, float acc) { return __builtin_amdgcn_fdot2_f32_bf16(__builtin_bit_cast(bf16x2_, v), __builtin_bit_cast(bf16x2_, sel), acc, false); }
__device__ __forceinline__ u32 vld(float f) { return __float_as_uint(f) | 1u; }
__device__ __forceinline__ f32x4 mfma(u32x4 a, u32x4 b, f32x4 c) {
  return __builtin_amdgcn_mfma_f32_16x16x32_bf16(__builtin_bit_cast(bf16x8, a), __builtin_bit_cast(bf16x8, b), c, 0, 0, 0);
}
__device__ __forceinline__ u32x4 mk_shift(u32x4 s, u32 by) {
  const u64 a = (((u64)s.y << 32) | s.x) + by;
  return u32x4{(u32)a, (u32)(a >> 32), s.z - by, s.w};
}
__device__ __forceinline__ u32x4 mk_srd(const void* p, u32 bytes) {
  const u64 a = (u64)p;
  u32x4 s;
  s.x = __builtin_amdgcn_readfirstlane((u32)a);
  s.y = __builtin_amdgcn_readfirstlane((u32)(a >> 32) & 0xffffu);
  s.z = __builtin_amdgcn_readfirstlane(bytes);
  s.w = 0x00020000u;
  return s;
}
#define MEMBAR() asm volatile("" ::: "memory")
#define WAIT_LGKM0() asm volatile("s_waitcnt lgkmcnt(0)" ::: "memory")
#define WAIT_VM0() asm volatile("s_waitcnt vmcnt(0)" ::: "memory")
#define BARRIER() do { asm volatile("s_waitcnt lgkmcnt(0)" ::: "memory"); __builtin_amdgcn_s_barrier(); asm volatile("" ::: "memory"); } while (0)

#define DMA1(AUX, srd, voff, soff, ldsaddr)                                                                      \
  do { u32 keep_;                                                                                                \
    asm volatile("s_mov_b32 %0, m0\n\ts_mov_b32 m0, %1\n\ts_nop 0\n\t"                                           \
                 "buffer_load_dwordx4 %4, %2, %3 offen " AUX " lds\n\ts_mov_b32 m0, %0"                          \
                 : "=&s"(keep_) : "s"(ldsaddr), "s"(srd), "s"(soff), "v"(voff) : "memory"); } while (0)
#define DMA4(AUX, srd, v0, v1, v2, v3, soff, ldsaddr)                                                            \
  do { u32 keep_;                                                                                                \
    asm volatile("s_mov_b32 %0, m0\n\ts_mov_b32 m0, %1\n\ts_nop 0\n\t"                                           \
                 "buffer_load_dwordx4 %4, %2, %3 offen " AUX " lds\n\t"                                          \
                 "buffer_load_dwordx4 %5, %2, %3 offen offset:1024 " AUX " lds\n\t"                              \
                 "buffer_load_dwordx4 %6, %2, %3 offen offset:2048 " AUX " lds\n\t"                              \
                 "buffer_load_dwordx4 %7, %2, %3 offen offset:3072 " AUX " lds\n\ts_mov_b32 m0, %0"              \
                 : "=&s"(keep_) : "s"(ldsaddr), "s"(srd), "s"(soff), "v"(v0), "v"(v1), "v"(v2), "v"(v3) : "memory"); } while (0)

#define DMA4W(AUX, srd, voff, so, step, ldsaddr)                                                                 \
  do { u32 keep_; const u32 d_ = (step) - 1024u, s0_ = (so), s1_ = s0_ + d_, s2_ = s1_ + d_, s3_ = s2_ + d_;     \
    asm volatile("s_mov_b32 %0, m0\n\ts_mov_b32 m0, %1\n\ts_nop 0\n\t"                                          \
                 "buffer_load_dwordx4 %7, %2, %3 offen " AUX " lds\n\t"                                          \
                 "buffer_load_dwordx4 %7, %2, %4 offen offset:1024 " AUX " lds\n\t"                              \
                 "buffer_load_dwordx4 %7, %2, %5 offen offset:2048 " AUX " lds\n\t"                              \
                 "buffer_load_dwordx4 %7, %2, %6 offen offset:3072 " AUX " lds\n\ts_mov_b32 m0, %0"              \
                 : "=&s"(keep_) : "s"(ldsaddr), "s"(srd), "s"(s0_), "s"(s1_), "s"(s2_), "s"(s3_), "v"(voff) : "memory"); } while (0)

template <int CTRL> __device__ __forceinline__ float dpp(float x) {
  return __builtin_bit_cast(float, __builtin_amdgcn_mov_dpp(__builtin_bit_cast(int, x), CTRL, 0xf, 0xf, true));
}
constexpr int ROR8 = 0x128, ROR4 = 0x124, ROR2 = 0x122, ROR1 = 0x121;
__device__ __forceinline__ float xrow_max(float x) {
  auto s = __builtin_amdgcn_permlane16_swap(__float_as_uint(x), __float_as_uint(x), false, false);
  x = fmaxf(__uint_as_float(s[0]), __uint_as_float(s[1]));
  auto t = __builtin_amdgcn_permlane32_swap(__float_as_uint(x), __float_as_uint(x), false, false);
  return fmaxf(__uint_as_float(t[0]), __uint_as_float(t[1]));
}
__device__ __forceinline__ float xrow_sum(float x) {
  auto s = __builtin_amdgcn_permlane16_swap(__float_as_uint(x), __float_as_uint(x), false, false);
  x = __uint_as_float(s[0]) + __uint_as_float(s[1]);
  auto t = __builtin_amdgcn_permlane32_swap(__float_as_uint(x), __float_as_uint(x), false, false);
  return __uint_as_float(t[0]) + __uint_as_float(t[1]);
}
__device__ __forceinline__ float wave_max(float x) {
  x = fmaxf(x, dpp<ROR8>(x)); x = fmaxf(x, dpp<ROR4>(x)); x = fmaxf(x, dpp<ROR2>(x)); x = fmaxf(x, dpp<ROR1>(x));
  return xrow_max(x);
}
__device__ __forceinline__ float wave_sum(float x) {
  x += dpp<ROR8>(x); x += dpp<ROR4>(x); x += dpp<ROR2>(x); x += dpp<ROR1>(x);
  return xrow_sum(x);
}
__device__ __forceinline__ float sum_x8_16_32(float x) {
  x += dpp<ROR8>(x);
  return xrow_sum(x);
}

typedef volatile LAS u32* flagp;
__device__ __forceinline__ void lds_wait_ge(flagp F, int idx, u32 need) {
  (void)F;
  const u32 a = L_FLG + idx * 4, ab = L_FLG + F_ABORT * 4;
  u32 v, z, cnt;
  asm volatile("s_mov_b32 %2, 0\n"
               "1:\n\tds_read_b32 %0, %3\n\tds_read_b32 %1, %4\n\ts_waitcnt lgkmcnt(0)\n\t"
               "v_cmp_le_u32 vcc, %5, %0\n\ts_cbranch_vccnz 2f\n\t"
               "v_cmp_ne_u32 vcc, 0, %1\n\ts_cbranch_vccnz 2f\n\t"
               "s_sleep 1\n\ts_add_u32 %2, %2, 1\n\ts_cmp_lt_u32 %2, %6\n\ts_cbranch_scc1 1b\n\t"
               "v_mov_b32 %0, 1\n\tds_write_b32 %4, %0\n"
               "2:"
               : "=&v"(v), "=&v"(z), "=&s"(cnt) : "v"(a), "v"(ab), "v"(need), "s"(LDS_SPIN_CAP) : "vcc", "scc", "memory");
}
__device__ __forceinline__ void lds_add(int idx, u32 n) {
  const u32 a = L_FLG + idx * 4;
  asm volatile("s_waitcnt lgkmcnt(0)\n\ts_mov_b64 vcc, exec\n\ts_mov_b64 exec, 1\n\tds_add_u32 %0, %1\n\ts_mov_b64 exec, vcc"
               :: "v"(a), "v"(n) : "vcc", "memory");
}
__device__ __forceinline__ bool spin_ok(flagp F, u32& spins) {
  if (++spins > GLB_SPIN_CAP) { F[F_ABORT] = 2u; return false; }
  if ((spins & 255u) == 0u && F[F_ABORT] != 0u) return false;
  return true;
}

constexpr int SC1 = 16;
typedef __amdgpu_buffer_rsrc_t rsrc_t;
__device__ __forceinline__ u32x4 ld128(rsrc_t r, int off) { return __builtin_amdgcn_raw_buffer_load_b128(r, off, 0, SC1); }
__device__ __forceinline__ u32x2 ld64(rsrc_t r, int off) { return __builtin_amdgcn_raw_buffer_load_b64(r, off, 0, SC1); }
__device__ __forceinline__ u32 ld32(rsrc_t r, int off) { return __builtin_amdgcn_raw_buffer_load_b32(r, off, 0, SC1); }
__device__ __forceinline__ void st128(rsrc_t r, int off, u32x4 v) { __builtin_amdgcn_raw_buffer_store_b128(v, r, off, 0, SC1); }
__device__ __forceinline__ void st64(rsrc_t r, int off, u32x2 v) { __builtin_amdgcn_raw_buffer_store_b64(v, r, off, 0, SC1); }

__device__ __forceinline__ u32x4 tr_frag(LAS unsigned char* L, u32 addr, u32 stride4) {
  const s16x4 a = __builtin_amdgcn_ds_read_tr16_b64_v4i16((LAS s16x4*)(L + addr));
  const s16x4 b = __builtin_amdgcn_ds_read_tr16_b64_v4i16((LAS s16x4*)(L + addr + stride4));
  const u32x2 a2 = __builtin_bit_cast(u32x2, a), b2 = __builtin_bit_cast(u32x2, b);
  return u32x4{a2.x, a2.y, b2.x, b2.y};
}
__device__ __forceinline__ u32x4 lds128(LAS unsigned char* L, u32 addr) { return *(LAS u32x4*)(L + addr); }

#define STAMP(i) do {} while (0)

__device__ __forceinline__ void ln_stats(const float (&v)[10], bool tail, float& mean_r, float& sd_r) {
  float s = 0.f;
#pragma unroll
  for (int i = 0; i < 8; ++i) s += v[i];
  if (tail) s += v[8] + v[9];
  const float mean = wave_sum(s) * (1.0f / DM);
  float q = 0.f;
#pragma unroll
  for (int i = 0; i < 8; ++i) { const float d = v[i] - mean; q += d * d; }
  if (tail) { const float d0 = v[8] - mean, d1 = v[9] - mean; q += d0 * d0 + d1 * d1; }
  const float var = wave_sum(q) * (1.0f / DM);
  mean_r = rb(mean);
  sd_r = rb(sqrtf(rb(rb(var) + 1e-5f)));
}
__device__ __forceinline__ float ln_mod(float x, float mean_r, float sd_r, float scale, float shift) {
  const float n = rb(rb(x - mean_r) / sd_r);
  return rb(rb(n * rb(1.0f + scale)) + shift);
}

__global__ void __launch_bounds__(NT, 2) fused_block(Params p) {
  extern __shared__ __attribute__((aligned(16))) unsigned char smem[];
  LAS unsigned char* L = (LAS unsigned char*)smem;
  flagp F = (flagp)(L + L_FLG);
  const int tid = threadIdx.x, lane = tid & 63, w = __builtin_amdgcn_readfirstlane(tid >> 6);
  STAMP(0);
  const u32 t_start = (u32)__builtin_amdgcn_s_memrealtime();
  const int c = blockIdx.x, xg = c & 7, ci = c >> 3;
  const int hkv = 2 * xg + (ci >> 4), cj = ci & 15, bb = cj >> 1, half = cj & 1;
  const int pj = ci >> 1;
  const bool isF = half == 0 || pj >= 14, isO = !isF && pj < 9;
  const int hu = xg * 5 + (pj - 9);
  const bool isH = !isF && !isO && hu < 36;
  const int f = xg * 18 + (half == 0 ? pj : pj + 2);
  const int u = pj;
  const int mu = xg * 9 + u;
  const bool isQ = isO && u < 8;
  const int shift = pj < 9 ? (pj < 8 ? SHIFT_O : SHIFT_O / 2) : pj < 14 ? (xg * 5 + (pj - 9) < 36 ? SHIFT_H : 0) : 0;
  const int nsl = half == 0 ? 64 + shift : 64 - shift;
  const int key0 = half == 0 ? 0 : (64 + shift) * 64;
  const u32 Wtot = isF ? (DEFER_DOWN ? 72u : 108u) : isO ? 136u : isH ? 108u : 0u;
  const int roff = isF ? (DEFER_DOWN ? (shift == 1 ? 6 : 5) : 7) : isH ? 7 : 0;
  const int posd = (2 * nsl + roff) & 7;
  const int g = lane >> 4, r16 = lane & 15, qp = (lane & 15) >> 2, pp = lane & 3;
  const rsrc_t rs = __builtin_amdgcn_make_buffer_rsrc((void*)p.ws, 0, WS_END, 0x00020000);

  u32x4 wf[18];
  u32 cbp[5], sbp[5];
  u32x4 pre4a, pre4b;
  float osA = 0.f, osB = 0.f;
  u32 nfin = 0;

  auto load_cbsb = [&](int row, u32 (&cb)[5], u32 (&sb)[5]) {
    u32 spins = 0;
    for (;;) {
      MEMBAR();
      const int base = WS_MODT + row * 4608;
#pragma unroll
      for (int i = 0; i < 4; ++i) { sb[i] = ld32(rs, base + (288 + lane + 64 * i) * 4); cb[i] = ld32(rs, base + (576 + lane + 64 * i) * 4); }
      sb[4] = ld32(rs, base + (288 + 256 + (lane & 31)) * 4); cb[4] = ld32(rs, base + (576 + 256 + (lane & 31)) * 4);
      u32 mx = 0;
#pragma unroll
      for (int i = 0; i < 5; ++i) mx = umax(mx, umax(sb[i], cb[i]));
      if (__all(mx != SENT) || !spin_ok(F, spins)) break;
    }
#pragma unroll
    for (int i = 0; i < 5; ++i) cb[i] = pack2(1.0f + bflo(cb[i]), 1.0f + bfhi(cb[i]));
  };
  auto get_tag = [&]() { lds_wait_ge(F, F_TAGOK, 1u); return (u32)F[F_TAG]; };
  auto q_partial = [&](int kt, f32x4 (&acc)[4]) {
    lds_wait_ge(F, F_LANDED, 72u);
    u32x4 pq[24];
    const u32 a0 = L_RING + (kt * 192 + 8 * g + qp) * 128 + 8 * pp;
#pragma unroll
    for (int s = 0; s < 6; ++s)
#pragma unroll
      for (int ct = 0; ct < 4; ++ct) pq[s * 4 + ct] = tr_frag(L, a0 + s * 4096 + ct * 32, 4 * 128);
    lds_add(F_WREL + 0, 1);
    const u32 tag = get_tag();
    STAMP(6);
    u32x4 hv[6];
    u32 spins = 0;
    const int hb = WS_H + xg * 18432 + kt * 768;
    int go[6], lo[6];
#pragma unroll
    for (int i = 0; i < 6; ++i) { const int e = i * 64 + lane, row = e / 48, j = e % 48; go[i] = hb + row * 2304 + j * 16; lo[i] = L_ACT + row * ACT_STRIDE + kt * 384 + j * 8; }
    if (HGATE) { while ((u32)__builtin_amdgcn_s_memrealtime() - t_start < (u32)HGATE) __builtin_amdgcn_s_sleep(2); }
    for (;;) {
      MEMBAR();
#pragma unroll
      for (int i = 0; i < 6; ++i) hv[i] = ld128(rs, go[i]);
      u32 bad = 0;
#pragma unroll
      for (int i = 0; i < 6; ++i) bad |= (hv[i].y ^ tag) | (hv[i].w ^ tag);
      if (__all(bad == 0u) || !spin_ok(F, spins)) break;
      __builtin_amdgcn_s_sleep(HPOLL_SLEEP);
    }
#pragma unroll
    for (int i = 0; i < 6; ++i) *(LAS u32x2*)(L + lo[i]) = u32x2{hv[i].x, hv[i].z};
    WAIT_LGKM0();
    STAMP(3);
    const u32 ab = L_ACT + (lane & 7) * ACT_STRIDE + kt * 384 + g * 16;
#pragma unroll
    for (int ct = 0; ct < 4; ++ct) acc[ct] = f32x4{0, 0, 0, 0};
#pragma unroll
    for (int s = 0; s < 6; ++s) {
      const u32x4 bfr = lds128(L, ab + s * 64);
#pragma unroll
      for (int ct = 0; ct < 4; ++ct) acc[ct] = mfma(pq[s * 4 + ct], bfr, acc[ct]);
    }
  };

  if (w == 0) {
    const u32 t_entry = t_start;
    __builtin_amdgcn_s_setprio(3);
    if (lane < 32) F[lane] = 0u;
    u32 n = 0;
    u32x4 srd0; u32 v0, step0, pre;
    if (isF) { srd0 = mk_srd(p.wg + f * 64, DM * 9216 - f * 64); v0 = (lane >> 2) * 9216 + (lane & 3) * 16; step0 = 16 * 9216; pre = 16; }
    else if (isQ) { srd0 = mk_srd(p.wq + (xg * 512 + u * 64) * 2, DM * 8192 - (xg * 512 + u * 64) * 2); v0 = (lane >> 3) * 8192 + (lane & 7) * 16; step0 = 8 * 8192; pre = WQW - 4; }
    else if (isH) { srd0 = mk_srd(p.wmod + hu * 32, DM * 6912 - hu * 32); v0 = (lane >> 1) * 6912 + (lane & 1) * 16; step0 = 32 * 6912; pre = 8; }
    else { srd0 = mk_srd(p.wq, 0); v0 = 0; step0 = 1024; pre = 0; }
#pragma unroll 1
    for (; n < pre; n += 4) DMA4W(AUX_W, srd0, v0, n * step0, step0, L_RING + n * 1024);
    BARRIER();
    STAMP(4);
    int pub = 0;
    auto run = [&](auto inflp, const u32x4& srd, u32 voff, u32 step, u32 cnt, u32 lds0, u32 first = 0) {
      constexpr int NP = decltype(inflp)::value;
      constexpr int KEEP = NP >= 8 ? NP - 4 : NP / 2;
      u32 s = first;
#pragma unroll 1
      for (; s + 4 <= cnt; s += 4) {
        DMA4W(AUX_W, srd, voff, s * step, step, lds0 + s * 1024);
        n += 4;
        asm volatile("s_waitcnt vmcnt(%0)" :: "i"(KEEP) : "memory");
        pub = max(pub, (int)n - KEEP); F[F_LANDED] = (u32)pub;
      }
#pragma unroll 1
      for (; s < cnt; ++s) { DMA1(AUX_W, srd, voff, s * step, lds0 + s * 1024); ++n; }
    };
    typedef std::integral_constant<int, INFLW / 16> P16;
    typedef std::integral_constant<int, (INFLW / 8 > 32 ? 32 : INFLW / 8)> P8;
    if (isF) {
      const u32 vg = (lane >> 2) * 9216 + (lane & 3) * 16;
      run(P16(), srd0, v0, step0, 36, L_RING, pre);
      run(P16(), mk_srd(p.wu + f * 64, DM * 9216 - f * 64), vg, 16 * 9216, 36, L_RING + 36 * 1024);
      if (!DEFER_DOWN) run(P8(), mk_srd(p.wd + f * 36864, 36864), (u32)(lane * 16), 1024, 36, L_RING + 72 * 1024);
    } else if (isO) {
      if (isQ) run(std::integral_constant<int, WQW>(), srd0, v0, step0, 72, L_RING, pre);
      else n = 72;
      run(std::integral_constant<int, WOW>(), mk_srd(p.wo + xg * 512 * 1152 + u * 128, 512 * 1152 - u * 128), (lane >> 3) * 1152 + (lane & 7) * 16, 8 * 1152, 64, L_RING + 72 * 1024);
    } else if (isH) {
      const u32 vm32 = (lane >> 1) * 6912 + (lane & 1) * 16;
      run(std::integral_constant<int, P1W>(), srd0, v0, step0, 18, L_RING, pre);
      run(std::integral_constant<int, P1W>(), mk_srd(p.wmod + 1152 + hu * 32, DM * 6912 - 1152 - hu * 32), vm32, 32 * 6912, 18, L_RING + 18 * 1024);
      run(P8(), mk_srd(p.wmod + 2304 + hu * 128, DM * 6912 - 2304 - hu * 128), (lane >> 3) * 6912 + (lane & 7) * 16, 8 * 6912, 72, L_RING + 36 * 1024);
    }
    STAMP(1);
    const size_t kvoff = ((size_t)(bb * HKV + hkv) * SK + (size_t)key0) * 256;
    const u32x4 srd_k = mk_srd(p.kc + kvoff, (u32)nsl * 16384u), srd_v = mk_srd(p.vc + kvoff, (u32)nsl * 16384u);
    u32 vk[4], vv[2];
#pragma unroll
    for (int j = 0; j < 4; ++j) vk[j] = g * 256 + ((r16 ^ (4 * j + g)) << 4);
#pragma unroll
    for (int j = 0; j < 2; ++j) vv[j] = g * 256 + ((r16 ^ ((g | (j << 2)) << 1)) << 4);
    const u32 wneed = isF ? (DEFER_DOWN ? 0x00022211u : 0x03332211u) : isO ? 0x33221111u : isH ? 0x02222211u : 0u;
    auto wait_window = [&](u32 N) {
      asm volatile("s_waitcnt vmcnt(28)\n\ts_cmp_ge_u32 %0, 28\n\ts_cbranch_scc1 9f\n\t"
                   "s_waitcnt vmcnt(24)\n\ts_cmp_ge_u32 %0, 24\n\ts_cbranch_scc1 9f\n\t"
                   "s_waitcnt vmcnt(20)\n\ts_cmp_ge_u32 %0, 20\n\ts_cbranch_scc1 9f\n\t"
                   "s_waitcnt vmcnt(16)\n9:" :: "s"(N) : "scc", "memory");
    };
    u32 fr_next = 0;
    u32 t_prev = t_entry, eb_next = 0;
    auto wait_free = [&](int pos, u32 need, u32 seen) {
      if (seen < need && F[F_FREE + pos] < need) { WAIT_VM0(); F[F_LANDED] = n; pub = (int)n; lds_wait_ge(F, F_FREE + pos, need); }
    };
#pragma unroll 1
    for (int q = 0; q < 2 * nsl; ++q) {
      const int pos = (q + roff) & 7;
      if (q >= 8) wait_free(pos, (u32)(q - 7), fr_next);
      else {
        const int nb = (wneed >> (4 * pos)) & 15;
#pragma unroll 1
        for (int i = 0; i < nb; ++i)
          if (F[F_WREL + i] < 7u) { WAIT_VM0(); F[F_LANDED] = n; pub = (int)n; lds_wait_ge(F, F_WREL + i, 7u); }
      }
      int lvl = q < EARLY_Q ? EARLY_LVL : BASE_LVL;
      if (q >= SCHED_Q0) {
        const u32 eown = t_prev + (u32)(((2 * nsl - q + 1) * SCHED_TAU) >> 4);
        F[F_EOWN] = eown;
        const u32 eb = __builtin_amdgcn_readfirstlane(eb_next);
        const int lag = eb ? (int)(eown - eb) - SCHED_LA : (int)(eown - t_entry) - SCHED_TE - SCHED_L1;
        if (lag > 0) lvl = BASE_LVL + BOOST1;
        if (lag > SCHED_L2) lvl = BASE_LVL + BOOST2;
      }
      eb_next = F[F_EBAR];
      t_prev = (u32)__builtin_amdgcn_s_memrealtime();
      const u32 N2 = 16u + 4u * (u32)((lvl + 1) >> 1), N = 16u + 4u * (u32)(lvl >> 1);
      fr_next = F[F_FREE + ((pos + 1) & 7)];
      const u32 lb = L_RING + pos * 16384;
      const bool isk = q < nsl;
      const u32x4 srd = isk ? srd_k : srd_v;
      const u32 v0_ = isk ? vk[0] : vv[0], v1_ = isk ? vk[1] : vv[0], v2_ = isk ? vk[2] : vv[1], v3_ = isk ? vk[3] : vv[1];
      const u32 so = (u32)(isk ? q : q - nsl) * 16384u;
#pragma unroll
      for (int gq = 0; gq < 4; ++gq) {
        DMA4(AUX_KV, srd, v0_, v1_, v2_, v3_, so + gq * 4096, lb + gq * 4096);
        n += 4;
        const u32 Ng = gq < 2 ? N2 : N;
        wait_window(Ng);
        pub = max(pub, (int)n - (int)Ng); F[F_LANDED] = (u32)pub;
      }
    }
    if (isF && DEFER_DOWN) {
      STAMP(5);
      for (int i = 0; i < 3; ++i) wait_free(posd + i, (u32)(2 * nsl - 7 + i), 0u);
      run(P8(), mk_srd(p.wd + f * 36864, 36864), (u32)(lane * 16), 1024, 36, L_RING + posd * 16384);
    }
    if (!isF || !DEFER_DOWN) { WAIT_VM0(); F[F_LANDED] = n; }
    nfin = n;
    STAMP(3);
    __builtin_amdgcn_s_setprio(0);
    if (isF) {
      lds_wait_ge(F, F_CB0, 1u);
#pragma unroll
      for (int i = 0; i < 5; ++i) { cbp[i] = *(LAS u32*)(L + L_RED + i * 256 + lane * 4); sbp[i] = *(LAS u32*)(L + L_RED + 1280 + i * 256 + lane * 4); }
    }
  } else if (w == 7) {
    BARRIER();
    u64 epoch = *(const __attribute__((address_space(4))) u64*)(p.ws + WS_CTRL) + 1ull;
    asm volatile("" : "+s"(epoch));
    const u32 tag = (u32)epoch;
    F[F_TAG] = tag;
    WAIT_LGKM0();
    F[F_TAGOK] = 1u;
    const u32x4 z4 = {0u, 0u, 0u, 0u}, s4 = {SENT, SENT, SENT, SENT};
    if (isH) {
#pragma unroll
      for (int i = 0; i < 2; ++i) st64(rs, WS_MODT + (lane & 7) * 4608 + hu * 128 + (2 * i + (r16 >> 3)) * 32 + g * 8, u32x2{SENT, SENT});
    }
    st128(rs, WS_OP + c * 1024 + lane * 16, z4);
    if (lane == 0) st128(rs, WS_ST + c * 16, z4);
    if (isO) {
#pragma unroll
      for (int i = 0; i < 2; ++i) st128(rs, WS_WP + (xg * 144 + u * 16) * 128 + i * 1024 + lane * 16, z4);
      st128(rs, WS_X1 + (lane >> 3) * 9216 + mu * 128 + (lane & 7) * 16, s4);
    }
    if (isF) {
#pragma unroll 1
      for (int i = 0; i < 9; ++i) st128(rs, WS_FP + f * 9216 + i * 1024 + lane * 16, s4);
    }
    WAIT_VM0();
    F[F_INIT] = 1u;
    if (lane == 0) __hip_atomic_store((GAS u64*)(p.ws + WS_ALIVE) + c, epoch, __ATOMIC_RELAXED, __HIP_MEMORY_SCOPE_AGENT);
    STAMP(1);
    if (isQ) {
      f32x4 acc[4];
      q_partial(2, acc);
      lds_add(F_WREL + 1, 1); lds_add(F_WREL + 2, 1);
      lds_wait_ge(F, F_P2, 2u);
      const int ls = (g * 8 + (lane & 7)) * 16;
#pragma unroll
      for (int ct = 0; ct < 4; ++ct) {
        const f32x4 a = (*(LAS f32x4*)(L + L_P2 + ct * 512 + ls) + *(LAS f32x4*)(L + L_P2 + 2048 + ct * 512 + ls)) + acc[ct];
        if (r16 < 8) st128(rs, WS_Q + ((2 * xg + (u >> 2)) * 8 + r16) * 1024 + ((u & 3) * 16 + ct * 4 + g) * 16, u32x4{pack2(a[0], a[1]), tag, pack2(a[2], a[3]), tag});
      }
      STAMP(4);
    } else {
      lds_add(F_WREL + 0, 1); lds_add(F_WREL + 1, 1); lds_add(F_WREL + 2, 1);
    }
    {
      u32x4 qv; u32 spins = 0;
      if (QGATE) { while ((u32)__builtin_amdgcn_s_memrealtime() - t_start < (u32)QGATE) __builtin_amdgcn_s_sleep(2); }
      for (;;) {
        MEMBAR();
        qv = ld128(rs, WS_Q + (hkv * 8 + bb) * 1024 + lane * 16);
        if (__all(((qv.y ^ tag) | (qv.w ^ tag)) == 0u) || !spin_ok(F, spins)) break;
        __builtin_amdgcn_s_sleep(QPOLL_SLEEP);
      }
      *(LAS u32x2*)(L + L_QS + lane * 8) = u32x2{qv.x, qv.z};
      WAIT_LGKM0();
      F[F_Q] = 1u;
    }
    STAMP(5);
    auto sample = [&](int r, int seq) {
    };
    sample(0, 26);
    lds_wait_ge(F, F_LANDED, Wtot + 16u * (u32)(nsl - 8));
    {
      const u32 ehi = (u32)(epoch >> 32);
      u32 spins = 0;
      for (;;) {
        MEMBAR();
        const u32x4 a = ld128(rs, WS_ALIVE + lane * 32), b2 = ld128(rs, WS_ALIVE + lane * 32 + 16);
        const u32 bad = (a.x ^ tag) | (a.y ^ ehi) | (a.z ^ tag) | (a.w ^ ehi) | (b2.x ^ tag) | (b2.y ^ ehi) | (b2.z ^ tag) | (b2.w ^ ehi);
        if (__all(bad == 0u) || !spin_ok(F, spins)) break;
        __builtin_amdgcn_s_sleep(64);
      }
    }
    F[F_ALIVE] = 1u;
    if (c == 0 && lane == 0) __hip_atomic_store((GAS u64*)(p.ws + WS_CTRL), epoch, __ATOMIC_RELAXED, __HIP_MEMORY_SCOPE_AGENT);
    {
      lds_wait_ge(F, F_L, 6u);
      const int hd = lane & 1;
      float M = *(LAS float*)(L + L_MX + hd * 4), Ls = *(LAS float*)(L + L_LW + hd * 4);
#pragma unroll
      for (int i = 1; i < 6; ++i) { M = fmaxf(M, *(LAS float*)(L + L_MX + i * 8 + hd * 4)); Ls += *(LAS float*)(L + L_LW + i * 8 + hd * 4); }
      if (lane < 2) st64(rs, WS_ST + c * 16 + hd * 8, u32x2{vld(M), vld(Ls)});
      u32x2 o; u32 spins = 0;
      for (;;) {
        MEMBAR();
        o = ld64(rs, WS_ST + (c ^ 8) * 16 + hd * 8);
        if (__all((o.x & o.y & 1u) != 0u) || !spin_ok(F, spins)) break;
        __builtin_amdgcn_s_sleep(8);
      }
      const float m0 = __uint_as_float(vld(M)), l0 = __uint_as_float(vld(Ls)), m1 = __uint_as_float(o.x), l1 = __uint_as_float(o.y);
      const float Mg = fmaxf(m0, m1);
      const float e0 = __builtin_amdgcn_exp2f((m0 - Mg) * LOG2E), e1 = __builtin_amdgcn_exp2f((m1 - Mg) * LOG2E);
      const float sc = e0 / (e0 * l0 + e1 * l1);
      osA = __builtin_bit_cast(float, __builtin_amdgcn_readlane(__builtin_bit_cast(int, sc), 0));
      osB = __builtin_bit_cast(float, __builtin_amdgcn_readlane(__builtin_bit_cast(int, sc), 1));
    }
    if (isF) {
      load_cbsb(0, cbp, sbp);
#pragma unroll
      for (int i = 0; i < 5; ++i) { *(LAS u32*)(L + L_RED + i * 256 + lane * 4) = cbp[i]; *(LAS u32*)(L + L_RED + 1280 + i * 256 + lane * 4) = sbp[i]; }
      WAIT_LGKM0();
      F[F_CB0] = 1u;
      load_cbsb(7, cbp, sbp);
      u32x2 gb; u32 spins = 0;
      for (;;) {
        MEMBAR();
        gb = ld64(rs, WS_MODT + (lane & 7) * 4608 + 864 * 4 + f * 8);
        if (__all(umax(gb.x, gb.y) != SENT) || !spin_ok(F, spins)) break;
      }
      pre4a = u32x4{gb.x, gb.y, 0u, 0u};
    } else if (isO) {
      u32 spins = 0;
      for (;;) {
        MEMBAR();
        pre4a = ld128(rs, WS_MODT + (lane & 7) * 4608 + mu * 16);
        if (__all(umax4(pre4a) != SENT) || !spin_ok(F, spins)) break;
      }
      pre4b = *(const GAS u32x4*)(p.x + (lane & 7) * DM + mu * 8);
    }
    STAMP(7);
    sample(1, nsl + 12);
    sample(2, nsl + 34);
  } else {
    const int cw = w - 1;
    if (isF) {
      BARRIER();
      const int m = cw & 1, kt = cw >> 1;
      const u32 a0 = L_RING + 36 * m * 1024 + (kt * 192 + 8 * g + qp) * 64 + 8 * pp;
      if (m == 1) lds_add(F_WREL + 0, 1);
      lds_wait_ge(F, F_LANDED, m == 0 ? 36u : 72u);
#pragma unroll
      for (int s = 0; s < 6; ++s) { wf[2 * s] = tr_frag(L, a0 + s * 2048, 4 * 64); wf[2 * s + 1] = tr_frag(L, a0 + s * 2048 + 32, 4 * 64); }
      if (m == 0) lds_add(F_WREL + 0, 1);
      lds_add(F_WREL + 1, 1);
      if (!DEFER_DOWN) {
        lds_wait_ge(F, F_LANDED, 108u);
        const u32 d0 = L_RING + 72 * 1024 + (8 * g + qp) * 1152 + cw * 192 + 8 * pp;
#pragma unroll
        for (int t = 0; t < 6; ++t) wf[12 + t] = tr_frag(L, d0 + t * 32, 4 * 1152);
      }
      lds_add(F_WREL + 2, 1);
    } else if (isO) {
      BARRIER();
      if (cw < 4) {
        lds_add(F_WREL + 0, 1);
        const u32 a0 = L_RING + 72 * 1024 + (8 * g + qp) * 128 + cw * 32 + 8 * pp;
        lds_wait_ge(F, F_LANDED, 104u);
#pragma unroll
        for (int s = 0; s < 8; ++s) wf[s] = tr_frag(L, a0 + s * 4096, 4 * 128);
        lds_add(F_WREL + 1, 1);
        lds_wait_ge(F, F_LANDED, 136u);
#pragma unroll
        for (int s = 8; s < 16; ++s) wf[s] = tr_frag(L, a0 + s * 4096, 4 * 128);
        lds_add(F_WREL + 2, 1);
      } else {
        if (isQ) {
          f32x4 acc[4];
          q_partial(cw - 4, acc);
          if (r16 < 8) {
#pragma unroll
            for (int ct = 0; ct < 4; ++ct) *(LAS f32x4*)(L + L_P2 + (cw - 4) * 2048 + ct * 512 + (g * 8 + r16) * 16) = acc[ct];
          }
          lds_add(F_P2, 1);
        } else lds_add(F_WREL + 0, 1);
        lds_add(F_WREL + 1, 1); lds_add(F_WREL + 2, 1);
      }
    } else if (isH) {
      u32 cv[6], xa[5], xb[5];
#pragma unroll
      for (int i = 0; i < 6; ++i) cv[i] = ((const GAS u32*)p.c)[cw * 64 + lane + 384 * i];
      const int rowb = 6 + (cw & 1);
      const GAS u32* xr = (const GAS u32*)p.x + cw * 288;
      const GAS u32* xs = (const GAS u32*)p.x + rowb * 288;
#pragma unroll
      for (int i = 0; i < 4; ++i) { xa[i] = xr[lane + 64 * i]; xb[i] = xs[lane + 64 * i]; }
      xa[4] = xr[256 + (lane & 31)]; xb[4] = xs[256 + (lane & 31)];
      const u32x2 xq = *(const GAS u32x2*)(p.x + (lane & 7) * DM + hu * 16 + g * 4);
      BARRIER();
#pragma unroll
      for (int i = 0; i < 6; ++i) {
        const int e = cw * 64 + lane + 384 * i;
        const float a = bflo(cv[i]), b2 = bfhi(cv[i]);
        *(LAS u32*)(L + L_SC + (e / 288) * ACT_STRIDE + (e % 288) * 4) = pack2(a / (1.0f + __expf(-a)), b2 / (1.0f + __expf(-b2)));
      }
      {
        float v[10], mean_r, sd_r;
#pragma unroll
        for (int i = 0; i < 5; ++i) { v[2 * i] = bflo(xa[i]); v[2 * i + 1] = bfhi(xa[i]); }
        ln_stats(v, lane < 32, mean_r, sd_r);
        if (lane == 0) { *(LAS float*)(L + L_STAT + cw * 8) = mean_r; *(LAS float*)(L + L_STAT + cw * 8 + 4) = sd_r; }
        if (cw < 2) {
#pragma unroll
          for (int i = 0; i < 5; ++i) { v[2 * i] = bflo(xb[i]); v[2 * i + 1] = bfhi(xb[i]); }
          ln_stats(v, lane < 32, mean_r, sd_r);
          if (lane == 0) { *(LAS float*)(L + L_STAT + rowb * 8) = mean_r; *(LAS float*)(L + L_STAT + rowb * 8 + 4) = sd_r; }
        }
      }
      lds_add(F_SC, 1);
      STAMP(24);
      const u32 sb0 = L_SC + (lane & 7) * ACT_STRIDE + g * 16;
      {
        const int m = cw & 1, kt = cw >> 1;
        lds_wait_ge(F, F_SC, 6u);
        const float mean_r = *(LAS float*)(L + L_STAT + (lane & 7) * 8), sd_r = *(LAS float*)(L + L_STAT + (lane & 7) * 8 + 4);
        float n0 = rb(rb(bflo(xq.x) - mean_r) / sd_r), n1 = rb(rb(bfhi(xq.x) - mean_r) / sd_r);
        float n2 = rb(rb(bflo(xq.y) - mean_r) / sd_r), n3 = rb(rb(bfhi(xq.y) - mean_r) / sd_r);
        asm volatile("" : "+v"(n0), "+v"(n1), "+v"(n2), "+v"(n3));
        u32 tag = 0;
        if (cw == 5) tag = get_tag();
        u32x4 bfr[6];
#pragma unroll
        for (int s = 0; s < 6; ++s) bfr[s] = lds128(L, sb0 + (kt * 6 + s) * 64);
        lds_wait_ge(F, F_LANDED, 36u);
        STAMP(25);
        const u32 a0 = L_RING + (m * 18 + kt * 6) * 1024 + (8 * g + qp) * 32 + 8 * pp;
        f32x4 acc = {0, 0, 0, 0}, acc2 = {0, 0, 0, 0};
#pragma unroll
        for (int s = 0; s < 6; s += 2) { acc = mfma(tr_frag(L, a0 + s * 1024, 4 * 32), bfr[s], acc); acc2 = mfma(tr_frag(L, a0 + (s + 1) * 1024, 4 * 32), bfr[s + 1], acc2); }
        acc += acc2;
        if (r16 < 8) *(LAS f32x4*)(L + L_RED + cw * 512 + (g * 8 + r16) * 16) = acc;
        lds_add(F_P1, 1);
        lds_add(F_WREL + 0, 1);
        if (cw == 5) {
          lds_wait_ge(F, F_P1, 6u);
          const int ls = (g * 8 + (lane & 7)) * 16;
          const f32x4 sa = *(LAS f32x4*)(L + L_RED + 0 * 512 + ls) + *(LAS f32x4*)(L + L_RED + 2 * 512 + ls) + *(LAS f32x4*)(L + L_RED + 4 * 512 + ls);
          const f32x4 ca = *(LAS f32x4*)(L + L_RED + 1 * 512 + ls) + *(LAS f32x4*)(L + L_RED + 3 * 512 + ls) + *(LAS f32x4*)(L + L_RED + 5 * 512 + ls);
          const float h0 = rb(rb(n0 * rb(1.0f + rb(ca[0]))) + rb(sa[0])), h1 = rb(rb(n1 * rb(1.0f + rb(ca[1]))) + rb(sa[1]));
          const float h2 = rb(rb(n2 * rb(1.0f + rb(ca[2]))) + rb(sa[2])), h3 = rb(rb(n3 * rb(1.0f + rb(ca[3]))) + rb(sa[3]));
          const u32x4 hu4 = {pack2(h0, h1), tag, pack2(h2, h3), tag};
#pragma unroll
          for (int i = 0; i < REP_H / 2; ++i)
            st128(rs, WS_H + (2 * i + (r16 >> 3)) * 18432 + ((lane & 7) * 144 + hu * 4 + g) * 16, hu4);
          STAMP(6);
        }
      }
      if (cw < 4) {
        lds_wait_ge(F, F_LANDED, 108u);
        const u32 a0 = L_RING + 36 * 1024 + (8 * g + qp) * 128 + cw * 32 + 8 * pp;
        f32x4 a0c = {0, 0, 0, 0}, a1c = {0, 0, 0, 0};
#pragma unroll 3
        for (int s = 0; s < 18; s += 2) {
          a0c = mfma(tr_frag(L, a0 + s * 4096, 4 * 128), lds128(L, sb0 + s * 64), a0c);
          a1c = mfma(tr_frag(L, a0 + (s + 1) * 4096, 4 * 128), lds128(L, sb0 + (s + 1) * 64), a1c);
        }
        const f32x4 a = a0c + a1c;
        lds_add(F_WREL + 1, 1);
        lds_wait_ge(F, F_INIT, 1u);
        if (r16 < 8) st64(rs, WS_MODT + r16 * 4608 + hu * 128 + cw * 32 + g * 8, u32x2{nosent(pack2(a[0], a[1])), nosent(pack2(a[2], a[3]))});
      } else lds_add(F_WREL + 1, 1);
      lds_add(F_WREL + 2, 1);
    } else {
      BARRIER();
      lds_add(F_WREL + 0, 1); lds_add(F_WREL + 1, 1); lds_add(F_WREL + 2, 1);
    }
    STAMP(1);
    u32x4 qf[4];
    u32 ka[4];
#pragma unroll
    for (int s = 0; s < 4; ++s) ka[s] = r16 * 256 + (((4 * s + g) ^ r16) << 4);
    float mx0 = -INFINITY, mx1 = -INFINITY;
    auto k_read = [&](int q, u32x4* kf) {
      const int pos = (q + roff) & 7;
      lds_wait_ge(F, F_LANDED, Wtot + 16u * (u32)(q + 1));
      const u32 sb = L_RING + pos * 16384;
#pragma unroll
      for (int t = 0; t < 4; ++t)
#pragma unroll
        for (int s = 0; s < 4; ++s) kf[t * 4 + s] = lds128(L, sb + ka[s] + t * 4096);
      WAIT_LGKM0();
      if (lane == 0) F[F_FREE + pos] = (u32)(q + 1);
    };
    auto k_scores = [&](int q, const u32x4* kf) {
      f32x4 acc[4];
#pragma unroll
      for (int t = 0; t < 4; ++t) {
        acc[t] = f32x4{0, 0, 0, 0};
#pragma unroll
        for (int s = 0; s < 4; ++s) acc[t] = mfma(qf[s], kf[t * 4 + s], acc[t]);
      }
      float s0 = g == 0 ? acc[0][0] : g == 1 ? acc[1][0] : g == 2 ? acc[2][0] : acc[3][0];
      float s1 = g == 0 ? acc[0][1] : g == 1 ? acc[1][1] : g == 2 ? acc[2][1] : acc[3][1];
      s0 = rb(rb(s0) * 0.08838834764831845f);
      s1 = rb(rb(s1) * 0.08838834764831845f);
      mx0 = fmaxf(mx0, s0); mx1 = fmaxf(mx1, s1);
      *(LAS u16*)(L + L_SC + q * 256 + lane * 2) = (u16)(__float_as_uint(s0) >> 16);
      *(LAS u16*)(L + L_SC + q * 256 + 128 + lane * 2) = (u16)(__float_as_uint(s1) >> 16);
    };
    int q0 = cw + 6;
    {
      u32x4 kf[16], kg[16];
      k_read(cw, kf);
      lds_wait_ge(F, F_Q, 1u);
      lds_wait_ge(F, F_WREL + 1, 7u); lds_wait_ge(F, F_WREL + 2, 7u);
      STAMP(2);
#pragma unroll
      for (int s = 0; s < 4; ++s) qf[s] = lds128(L, L_QS + (lane & 1) * 256 + s * 64 + g * 16);
      k_scores(cw, kf);
    }
#pragma unroll 1
    for (int q = q0; q < nsl; q += 6) {
      u32x4 kf[16];
      k_read(q, kf);
      k_scores(q, kf);
    }
    STAMP(3);
    {
      mx0 = wave_max(mx0); mx1 = wave_max(mx1);
      if (lane == 0) *(LAS u32x2*)(L + L_MX + cw * 8) = u32x2{__float_as_uint(mx0), __float_as_uint(mx1)};
      lds_add(F_MX, 1);
      lds_wait_ge(F, F_MX, 6u);
#pragma unroll
      for (int i = 0; i < 6; ++i) { mx0 = fmaxf(mx0, *(LAS float*)(L + L_MX + i * 8)); mx1 = fmaxf(mx1, *(LAS float*)(L + L_MX + i * 8 + 4)); }
      float l0 = 0.f, l1 = 0.f;
#pragma unroll 1
      for (int q = cw; q < nsl; q += 6) {
        const float s0 = bf2f(*(LAS u16*)(L + L_SC + q * 256 + lane * 2)), s1 = bf2f(*(LAS u16*)(L + L_SC + q * 256 + 128 + lane * 2));
        const float p0 = __builtin_amdgcn_exp2f((s0 - mx0) * LOG2E), p1 = __builtin_amdgcn_exp2f((s1 - mx1) * LOG2E);
        l0 += p0; l1 += p1;
        *(LAS u16*)(L + L_SC + q * 256 + lane * 2) = (u16)f2bf(p0);
        *(LAS u16*)(L + L_SC + q * 256 + 128 + lane * 2) = (u16)f2bf(p1);
      }
      l0 = wave_sum(l0); l1 = wave_sum(l1);
      if (lane == 0) *(LAS u32x2*)(L + L_LW + cw * 8) = u32x2{__float_as_uint(l0), __float_as_uint(l1)};
      lds_add(F_L, 1);
    }
    f32x4 o[8];
#pragma unroll
    for (int cb = 0; cb < 8; ++cb) o[cb] = f32x4{0, 0, 0, 0};
    const u32 va0 = (8 * g + qp) * 256 + ((pp >> 1) << 4) + 8 * (pp & 1) + (((qp | ((g & 1) << 2)) << 1) << 4);
#pragma unroll 1
    for (int q = cw; q < nsl; q += 6) {
      const int pos = (q + nsl + roff) & 7;
      if (isF && q >= 24 && q < 30) { lds_wait_ge(F, F_ALIVE, 1u); load_cbsb(w, cbp, sbp); }
      const u32x4 pf0 = lds128(L, L_SC + q * 256 + (lane & 1) * 128 + g * 16), pf1 = lds128(L, L_SC + q * 256 + (lane & 1) * 128 + 64 + g * 16);
      lds_wait_ge(F, F_LANDED, Wtot + 16u * (u32)(q + nsl + 1));
      const u32 sb = L_RING + pos * 16384 + va0;
      u32x4 vf[8], vg2[8];
#pragma unroll
      for (int cb = 0; cb < 8; ++cb) vf[cb] = tr_frag(L, sb ^ (cb << 5), 1024);
#pragma unroll
      for (int cb = 0; cb < 8; ++cb) vg2[cb] = tr_frag(L, (sb ^ (cb << 5)) + 8192, 1024);
      WAIT_LGKM0();
      if (lane == 0) F[F_FREE + pos] = (u32)(q + nsl + 1);
#pragma unroll
      for (int cb = 0; cb < 8; ++cb) o[cb] = mfma(pf0, vf[cb], o[cb]);
#pragma unroll
      for (int cb = 0; cb < 8; ++cb) o[cb] = mfma(pf1, vg2[cb], o[cb]);
    }
    STAMP(4);
    if (g == 0) {
#pragma unroll
      for (int cb = 0; cb < 8; ++cb) *(LAS u32x2*)(L + L_ACT + cw * 1024 + (cb * 16 + r16) * 8) = u32x2{__float_as_uint(o[cb][0]), __float_as_uint(o[cb][1])};
    }
    lds_add(F_OPART, 1);
  }
  if (w == 7) {
    lds_wait_ge(F, F_OPART, 6u);
    STAMP(8);
    f32x4 O = *(LAS f32x4*)(L + L_ACT + lane * 16);
#pragma unroll
    for (int i = 1; i < 6; ++i) O += *(LAS f32x4*)(L + L_ACT + i * 1024 + lane * 16);
    st128(rs, WS_OP + c * 1024 + lane * 16, u32x4{vld(O[0] * osA), vld(O[1] * osB), vld(O[2] * osA), vld(O[3] * osB)});
    STAMP(9);
  }
  if (isO) {
    constexpr int L_OS = L_SC;
    u32x4 ov[4];
    bool got[4] = {false, false, false, false};
    u32 spins = 0;
    lds_wait_ge(F, F_ALIVE, 1u);
    for (;;) {
      MEMBAR();
#pragma unroll
      for (int i = 0; i < 4; ++i) if (!got[i]) ov[i] = ld128(rs, WS_OP + (xg + 8 * ((i >> 1) * 16 + 2 * w + (i & 1))) * 1024 + lane * 16);
      bool all = true;
#pragma unroll
      for (int i = 0; i < 4; ++i) if (!got[i]) {
        got[i] = __all((ov[i].x & ov[i].y & ov[i].z & ov[i].w & 1u) != 0u);
        all = all && got[i];
      }
      if (all || !spin_ok(F, spins)) break;
    }
#pragma unroll
    for (int hk = 0; hk < 2; ++hk) {
      const f32x4 s = __builtin_bit_cast(f32x4, ov[2 * hk]) + __builtin_bit_cast(f32x4, ov[2 * hk + 1]);
      *(LAS u32*)(L + L_OS + w * 1040 + hk * 512 + lane * 4) = pack2(s[0], s[2]);
      *(LAS u32*)(L + L_OS + w * 1040 + hk * 512 + 256 + lane * 4) = pack2(s[1], s[3]);
    }
    BARRIER();
    if (w != 0) STAMP(10);
    if (w >= 1 && w <= 4) {
      const int ct = w - 1;
      const u32 ob = L_OS + (lane & 7) * 1040 + g * 16;
      u32x4 bfr[16];
#pragma unroll
      for (int s = 0; s < 16; ++s) bfr[s] = lds128(L, ob + s * 64);
      f32x4 a0 = {0, 0, 0, 0}, a1 = {0, 0, 0, 0}, a2 = {0, 0, 0, 0}, a3 = {0, 0, 0, 0};
#pragma unroll
      for (int s = 0; s < 16; s += 4) { a0 = mfma(wf[s], bfr[s], a0); a1 = mfma(wf[s + 1], bfr[s + 1], a1); a2 = mfma(wf[s + 2], bfr[s + 2], a2); a3 = mfma(wf[s + 3], bfr[s + 3], a3); }
      const f32x4 a = (a0 + a1) + (a2 + a3);
      if (r16 < 8) st128(rs, WS_WP + (xg * 144 + (u * 4 + ct) * 4 + g) * 128 + r16 * 16, u32x4{vld(a[0]), vld(a[1]), vld(a[2]), vld(a[3])});
      STAMP(11);
    } else if (w == 7) {
      u32x4 pa, pb; u32 spins2 = 0;
      const int wo_ = WS_WP + ((lane >> 3) * 144 + 2 * mu) * 128 + (lane & 7) * 16;
      for (;;) {
        MEMBAR();
        pa = ld128(rs, wo_); pb = ld128(rs, wo_ + 128);
        if (__all((pa.x & pa.y & pa.z & pa.w & pb.x & pb.y & pb.z & pb.w & 1u) != 0u) || !spin_ok(F, spins2)) break;
      }
      float a[8];
      a[0] = __uint_as_float(pa.x); a[1] = __uint_as_float(pa.y); a[2] = __uint_as_float(pa.z); a[3] = __uint_as_float(pa.w);
      a[4] = __uint_as_float(pb.x); a[5] = __uint_as_float(pb.y); a[6] = __uint_as_float(pb.z); a[7] = __uint_as_float(pb.w);
#pragma unroll
      for (int i = 0; i < 8; ++i) a[i] = sum_x8_16_32(a[i]);
      u32x4 xo;
#pragma unroll
      for (int i = 0; i < 4; ++i) {
        const u32 ap = pack2(a[2 * i], a[2 * i + 1]);
        const u32 tp = pack2(bflo(pre4a[i]) * bflo(ap), bfhi(pre4a[i]) * bfhi(ap));
        xo[i] = nosent(pack2(bflo(pre4b[i]) + bflo(tp), bfhi(pre4b[i]) + bfhi(tp)));
      }
      st128(rs, WS_X1 + (lane >> 3) * 9216 + mu * 128 + (lane & 7) * 16, xo);
      STAMP(12);
    }
  } else if (isF) {
    const u32 n0 = Wtot + 32u * (u32)nsl;
    if (w == 7) {
      u32x4 pv; u32 spins = 0;
      for (;;) {
        MEMBAR();
        pv = ld128(rs, WS_WP + ((lane >> 3) * 144 + f) * 128 + (lane & 7) * 16);
        if (__all((pv.x & pv.y & pv.z & pv.w & 1u) != 0u) || !spin_ok(F, spins)) break;
        if (WPPOLL_SLEEP) __builtin_amdgcn_s_sleep(WPPOLL_SLEEP);
      }
      F[F_X1] = 1u;
      STAMP(10);
    } else lds_wait_ge(F, F_X1, 1u);
    if (DELAY_C) __builtin_amdgcn_s_sleep(DELAY_C);
    u32x2 x1p = {0u, 0u};
    {
      if (w != 0) {
        const int e0 = (w - 1) * 64 + lane, xb = WS_X1 + xg * 9216;
        u32x4 xv, xw = {0u, 0u, 0u, 0u}; u32 spins = 0;
        for (;;) {
          MEMBAR();
          xv = ld128(rs, xb + e0 * 16);
          if (w <= 2) xw = ld128(rs, xb + (448 + e0) * 16);
          if (__all(umax(umax4(xv), umax4(xw)) != SENT) || !spin_ok(F, spins)) break;
        }
        STAMP(16);
        const u32 a = L_ACT + (e0 & 7) * ACT_STRIDE + (e0 >> 3) * 16;
        *(LAS u32x4*)(L + a) = xv;
        if (w <= 2) *(LAS u32x4*)(L + a + 56 * 16) = xw;
      }
      BARRIER();
      if (w != 0) STAMP(17);
      u32 xr[5];
#pragma unroll
      for (int i = 0; i < 4; ++i) xr[i] = *(LAS u32*)(L + L_ACT + w * ACT_STRIDE + (lane + 64 * i) * 4);
      xr[4] = *(LAS u32*)(L + L_ACT + w * ACT_STRIDE + (256 + (lane & 31)) * 4);
      if (w == 7) x1p = *(LAS u32x2*)(L + L_ACT + (lane & 7) * ACT_STRIDE + f * 8);
      float v[10];
#pragma unroll
      for (int i = 0; i < 5; ++i) { v[2 * i] = bflo(xr[i]); v[2 * i + 1] = bfhi(xr[i]); }
      float s = (v[0] + v[1]) + (v[2] + v[3]) + ((v[4] + v[5]) + (v[6] + v[7]));
      if (lane < 32) s += v[8] + v[9];
      const float mean = wave_sum(s) * (1.0f / DM);
      float q2 = 0.f, q3 = 0.f;
#pragma unroll
      for (int i = 0; i < 4; ++i) { const float d0 = v[2 * i] - mean, d1 = v[2 * i + 1] - mean; q2 += d0 * d0; q3 += d1 * d1; }
      if (lane < 32) { const float d0 = v[8] - mean, d1 = v[9] - mean; q2 += d0 * d0; q3 += d1 * d1; }
      const float var = wave_sum(q2 + q3) * (1.0f / DM);
      const float mean_r = rb(mean);
      const float rinv = __builtin_amdgcn_rcpf(rb(__builtin_amdgcn_sqrtf(rb(rb(var) + 1e-5f))));
#pragma unroll
      for (int i = 0; i < 5; ++i) {
        const u32 dp = pack2(v[2 * i] - mean_r, v[2 * i + 1] - mean_r);
        const u32 np = pack2(bflo(dp) * rinv, bfhi(dp) * rinv);
        const u32 tp = pack2(bflo(np) * bflo(cbp[i]), bfhi(np) * bfhi(cbp[i]));
        const u32 hp = pack2(bflo(tp) + bflo(sbp[i]), bfhi(tp) + bfhi(sbp[i]));
        if (i < 4) *(LAS u32*)(L + L_H2 + w * ACT_STRIDE + (lane + 64 * i) * 4) = hp;
        else if (lane < 32) *(LAS u32*)(L + L_H2 + w * ACT_STRIDE + (256 + lane) * 4) = hp;
      }
      if (w != 0) STAMP(18);
    }
    if (w == 0) { WAIT_VM0(); F[F_LANDED] = nfin; }
    BARRIER();
    if (w != 0) STAMP(11);
    if (w == 7) {
      BARRIER();
      BARRIER();
      const int off = WS_FP + (lane >> 3) * 9216 + ((f >> 3) * 4 + (f & 3)) * 128 + (lane & 7) * 16 + ((f >> 2) & 1) * 8;
      u32x2 e[18]; u32 spins = 0;
      u32 slo = 0x00003f80u, shi = 0x3f800000u;
      asm volatile("" : "+v"(slo), "+v"(shi));
      if (DELAY_E) __builtin_amdgcn_s_sleep(DELAY_E);
      for (;;) {
        MEMBAR();
#pragma unroll
        for (int i = 0; i < 18; ++i) e[i] = ld64(rs, off + i * 8 * 9216);
        u32 mx = 0;
#pragma unroll
        for (int i = 0; i < 18; ++i) mx = umax(mx, umax(e[i].x, e[i].y));
        if (__all(mx != SENT) || !spin_ok(F, spins)) break;
      }
      STAMP(23);
      f32x4 s = {0, 0, 0, 0}, s1 = {0, 0, 0, 0};
#pragma unroll
      for (int i = 0; i < 18; i += 2) {
        s[0] = add_sel(e[i].x, slo, s[0]); s[1] = add_sel(e[i].x, shi, s[1]); s[2] = add_sel(e[i].y, slo, s[2]); s[3] = add_sel(e[i].y, shi, s[3]);
        s1[0] = add_sel(e[i + 1].x, slo, s1[0]); s1[1] = add_sel(e[i + 1].x, shi, s1[1]); s1[2] = add_sel(e[i + 1].y, slo, s1[2]); s1[3] = add_sel(e[i + 1].y, shi, s1[3]);
      }
      s += s1;
      const u32 dA = pack2(sum_x8_16_32(s[0]), sum_x8_16_32(s[1])), dB = pack2(sum_x8_16_32(s[2]), sum_x8_16_32(s[3]));
      const u32 tA = pack2(bflo(pre4a.x) * bflo(dA), bfhi(pre4a.x) * bfhi(dA)), tB = pack2(bflo(pre4a.y) * bflo(dB), bfhi(pre4a.y) * bfhi(dB));
      if (lane < 8) *(GAS u32x2*)(p.out + lane * DM + f * 4) = u32x2{pack2(bflo(x1p.x) + bflo(tA), bfhi(x1p.x) + bfhi(tA)), pack2(bflo(x1p.y) + bflo(tB), bfhi(x1p.y) + bfhi(tB))};
      STAMP(13);
    } else {
      const int cw = w - 1;
      if (w >= 1) {
        const int kt = cw >> 1;
        const u32 hb = L_H2 + (lane & 7) * ACT_STRIDE + kt * 384 + g * 16;
        u32x4 bfr[6];
#pragma unroll
        for (int s = 0; s < 6; ++s) bfr[s] = lds128(L, hb + s * 64);
        f32x4 a0 = {0, 0, 0, 0}, a1 = {0, 0, 0, 0}, a2 = {0, 0, 0, 0}, a3 = {0, 0, 0, 0};
#pragma unroll
        for (int s = 0; s < 6; s += 2) { a0 = mfma(wf[2 * s], bfr[s], a0); a1 = mfma(wf[2 * s + 1], bfr[s], a1); a2 = mfma(wf[2 * s + 2], bfr[s + 1], a2); a3 = mfma(wf[2 * s + 3], bfr[s + 1], a3); }
        a0 += a2; a1 += a3;
        if (r16 < 8) { *(LAS f32x4*)(L + L_R2 + cw * 1024 + (g * 8 + r16) * 16) = a0; *(LAS f32x4*)(L + L_R2 + cw * 1024 + 512 + (g * 8 + r16) * 16) = a1; }
        if (DEFER_DOWN) {
          lds_wait_ge(F, F_LANDED, n0 + 36u);
          const u32 d0 = L_RING + posd * 16384 + (8 * g + qp) * 1152 + cw * 192 + 8 * pp;
#pragma unroll
          for (int t = 0; t < 6; ++t) wf[12 + t] = tr_frag(L, d0 + t * 32, 4 * 1152);
        }
      }
      if (w != 0) STAMP(19);
      BARRIER();
      if (w != 0) STAMP(20);
      if (w < 4) {
        const int ct = w >> 1, e = (w & 1) * 64 + lane;
        const u32 a = L_R2 + ct * 512 + e * 4;
        const float gg = rb(*(LAS float*)(L + a) + *(LAS float*)(L + a + 2048) + *(LAS float*)(L + a + 4096));
        const float uu = rb(*(LAS float*)(L + a + 1024) + *(LAS float*)(L + a + 3072) + *(LAS float*)(L + a + 5120));
        const float inner = 0.7978845608028654f * (gg + 0.044715f * gg * gg * gg);
        const float th = 1.0f - 2.0f * __builtin_amdgcn_rcpf(1.0f + __expf(2.0f * inner));
        const float ge = rb(0.5f * gg * (1.0f + th));
        const int ls = e >> 2, rr = e & 3, b_ = ls & 7, g_ = ls >> 3;
        *(LAS u16*)(L + L_HH + b_ * 80 + (ct * 16 + g_ * 4 + rr) * 2) = (u16)f2bf(ge * uu);
      }
      if (w != 0) STAMP(21);
      BARRIER();
      if (w != 0) STAMP(22);
      if (w >= 1) {
        const u32x4 bfr = lds128(L, L_HH + (lane & 7) * 80 + g * 16);
        f32x4 a[6];
#pragma unroll
        for (int t = 0; t < 6; ++t) a[t] = mfma(wf[12 + t], bfr, f32x4{0, 0, 0, 0});
#pragma unroll
        for (int t = 0; t < 6; t += 2)
          if (r16 < 8) st128(rs, WS_FP + f * 9216 + ((cw * 3 + (t >> 1)) * 4 + g) * 128 + r16 * 16,
                             u32x4{nosent(pack2(a[t][0], a[t][1])), nosent(pack2(a[t][2], a[t][3])), nosent(pack2(a[t + 1][0], a[t + 1][1])), nosent(pack2(a[t + 1][2], a[t + 1][3]))});
      }
      if (w != 0) STAMP(12);
    }
  }
}

}

extern "C" void kernel_launch(void* const* d_in, const int* in_sizes, int n_in, void* d_out, int out_size, void* d_ws, size_t ws_size,
                              hipStream_t stream) {
  (void)in_sizes; (void)n_in; (void)out_size; (void)ws_size;
  Params p;
  p.x = (const u16*)d_in[0];
  p.kc = (const unsigned char*)d_in[1];
  p.vc = (const unsigned char*)d_in[2];
  p.wq = (const unsigned char*)d_in[3];
  p.wo = (const unsigned char*)d_in[4];
  p.wg = (const unsigned char*)d_in[6];
  p.wu = (const unsigned char*)d_in[7];
  p.wd = (const unsigned char*)d_in[8];
  p.c = (const u16*)d_in[9];
  p.wmod = (const unsigned char*)d_in[10];
  p.out = (u16*)d_out;
  p.ws = (unsigned char*)d_ws;
  (void)hipFuncSetAttribute((const void*)fused_block, hipFuncAttributeMaxDynamicSharedMemorySize, LDS_BYTES);
  hipLaunchKernelGGL(fused_block, dim3(NCU), dim3(NT), LDS_BYTES, stream, p);
}
```

```cpp
#include <hip/hip_runtime.h>
#include <stdint.h>
#include <type_traits>
#pragma clang fp contract(off)

#define INFL 24
#define INFLW 320
#define EARLY_Q 16
#define EARLY_LVL 4
#define BASE_LVL 2
#define BOOST1 1
#define BOOST2 3
#define SCHED 1
#define SCHED_TE 7750
#define SCHED_TAU 876
#define SCHED_Q0 17
#define SCHED_L1 30
#define SCHED_L2 60
#define SCHED_LA 10
#define GROUPW 1
#define STAGE2 0
#define DEFER_DOWN 1
#define SAMP_LAG 6
#define GLOBAL_T0 1
#define ADAPT 0
#define SHIFT_O 1
#define SHIFT_H 2
#define DELAY_C 16
#define DELAY_E 24
#define WPPOLL_SLEEP 0
#define P1W 10
#define WOW 16
#define WQW 12
#define QGATE 0
#define HGATE 750
#define HPOLL_SLEEP 2
#define QPOLL_SLEEP 2
#define AUX_KV "nt"
#define AUX_W "nt"
#define STAMPS 0

#define LAS __attribute__((address_space(3)))
#define GAS __attribute__((address_space(1)))
typedef unsigned u32;
typedef unsigned long long u64;
typedef unsigned short u16;
typedef u32 u32x4 __attribute__((ext_vector_type(4)));
typedef u32 u32x2 __attribute__((ext_vector_type(2)));
typedef float f32x4 __attribute__((ext_vector_type(4)));
typedef __bf16 bf16x8 __attribute__((ext_vector_type(8)));
typedef short s16x4 __attribute__((ext_vector_type(4)));

namespace {

constexpr int NB = 8, DM = 576, HKV = 16, SK = 8192, DH = 128, DF = 4608;
constexpr int NCU = 256, NT = 512;

constexpr int REP_H = 8;
constexpr int WS_CTRL = 0;
constexpr int WS_ALIVE = 4096;
constexpr int WS_ST = 8192;
constexpr int WS_MODT = WS_ST + 4096;
constexpr int WS_X1 = WS_MODT + 36864;
constexpr int WS_H = WS_X1 + 8 * 9216;
constexpr int WS_Q = WS_H + REP_H * 18432;
constexpr int WS_OP = WS_Q + 131072;
constexpr int WS_WP = WS_OP + 262144;
constexpr int WS_FP = WS_WP + 147456;
constexpr int WS_SAMP = WS_FP + 144 * 9216;
constexpr int WS_T0 = WS_SAMP + 3 * 32 * 128;
constexpr int WS_END = WS_T0 + 8 * 128;
constexpr int WS_DBG = 4 << 20;
static_assert(WS_END <= WS_DBG, "ws");

constexpr int MAXSL = 64 + (SHIFT_O > SHIFT_H ? SHIFT_O : SHIFT_H);
constexpr int L_RING = 0;
constexpr int L_SC = 131072;
constexpr int ACT_STRIDE = 1168;
constexpr int L_P2 = L_SC + 8192;
constexpr int L_H2 = L_SC;
constexpr int L_R2 = L_SC + 9344;
constexpr int L_HH = L_R2 + 6144;
constexpr int L_ACT = L_SC + (MAXSL * 256 > 16384 ? MAXSL * 256 : 16384);
constexpr int L_QS = L_ACT + 9344;
constexpr int L_RED = L_QS + 512;
constexpr int L_STAT = L_RED + 3072;
constexpr int L_MX = L_STAT + 64;
constexpr int L_LW = L_MX + 64;
constexpr int L_FLG = L_LW + 64;
constexpr int LDS_BYTES = 163840;
static_assert(L_FLG + 256 <= LDS_BYTES && L_HH + 640 <= L_ACT, "lds");
constexpr int F_LANDED = 0, F_FREE = 1  , F_WREL = 9  , F_SC = 12, F_P1 = 13, F_INIT = 14, F_Q = 15, F_ABORT = 16, F_ALIVE = 17,
              F_P2 = 18, F_MX = 19, F_L = 20, F_X1 = 21, F_TAGOK = 22, F_TAG = 23, F_OPART = 24, F_CB0 = 25, F_EOWN = 26, F_EBAR = 27, F_T0 = 28;

constexpr u32 SENT = 0xFFFFFFFFu;
constexpr u32 LDS_SPIN_CAP = 1u << 21;
constexpr u32 GLB_SPIN_CAP = 1u << 17;
constexpr float LOG2E = 1.4426950408889634f;

struct Params {
  const u16* x; const unsigned char* kc; const unsigned char* vc; const unsigned char* wq; const unsigned char* wo;
  const unsigned char* wg; const unsigned char* wu; const unsigned char* wd; const u16* c; const unsigned char* wmod;
  u16* out; unsigned char* ws;
};

__device__ __forceinline__ float bf2f(u32 v16) { return __uint_as_float(v16 << 16); }
__device__ __forceinline__ float bflo(u32 d) { return __uint_as_float(d << 16); }
__device__ __forceinline__ float bfhi(u32 d) { return __uint_as_float(d & 0xffff0000u); }
__device__ __forceinline__ float rb(float f) { return (float)(__bf16)f; }
__device__ __forceinline__ u32 f2bf(float f) { return (u32)__builtin_bit_cast(u16, (__bf16)f); }
typedef float f32x2 __attribute__((ext_vector_type(2)));
typedef __bf16 bf16x2 __attribute__((ext_vector_type(2)));
__device__ __forceinline__ u32 pack2(float lo, float hi) { return __builtin_bit_cast(u32, __builtin_convertvector(f32x2{lo, hi}, bf16x2)); }
__device__ __forceinline__ u32 nosent(u32 p) { return p < 0xFFFEFFFFu ? p : 0xFFFEFFFFu; }
__device__ __forceinline__ u32 umax(u32 a, u32 b) { return a > b ? a : b; }
__device__ __forceinline__ u32 umax4(u32x4 v) { return umax(umax(v.x, v.y), umax(v.z, v.w)); }
typedef __bf16 bf16x2_ __attribute__((ext_vector_type(2)));
__device__ __forceinline__ float add_sel(u32 v, u32 sel, float acc) { return __builtin_amdgcn_fdot2_f32_bf16(__builtin_bit_cast(bf16x2_, v), __builtin_bit_cast(bf16x2_, sel), acc, false); }
__device__ __forceinline__ u32 vld(float f) { return __float_as_uint(f) | 1u; }
__device__ __forceinline__ f32x4 mfma(u32x4 a, u32x4 b, f32x4 c) {
  return __builtin_amdgcn_mfma_f32_16x16x32_bf16(__builtin_bit_cast(bf16x8, a), __builtin_bit_cast(bf16x8, b), c, 0, 0, 0);
}
__device__ __forceinline__ u32x4 mk_shift(u32x4 s, u32 by) {
  const u64 a = (((u64)s.y << 32) | s.x) + by;
  return u32x4{(u32)a, (u32)(a >> 32), s.z - by, s.w};
}
__device__ __forceinline__ u32x4 mk_srd(const void* p, u32 bytes) {
  const u64 a = (u64)p;
  u32x4 s;
  s.x = __builtin_amdgcn_readfirstlane((u32)a);
  s.y = __builtin_amdgcn_readfirstlane((u32)(a >> 32) & 0xffffu);
  s.z = __builtin_amdgcn_readfirstlane(bytes);
  s.w = 0x00020000u;
  return s;
}
#define MEMBAR() asm volatile("" ::: "memory")
#define WAIT_LGKM0() asm volatile("s_waitcnt lgkmcnt(0)" ::: "memory")
#define WAIT_VM0() asm volatile("s_waitcnt vmcnt(0)" ::: "memory")
#define BARRIER() do { asm volatile("s_waitcnt lgkmcnt(0)" ::: "memory"); __builtin_amdgcn_s_barrier(); asm volatile("" ::: "memory"); } while (0)

#define DMA1(AUX, srd, voff, soff, ldsaddr)                                                                      \
  do { u32 keep_;                                                                                                \
    asm volatile("s_mov_b32 %0, m0\n\ts_mov_b32 m0, %1\n\ts_nop 0\n\t"                                           \
                 "buffer_load_dwordx4 %4, %2, %3 offen " AUX " lds\n\ts_mov_b32 m0, %0"                          \
                 : "=&s"(keep_) : "s"(ldsaddr), "s"(srd), "s"(soff), "v"(voff) : "memory"); } while (0)
#define DMA4(AUX, srd, v0, v1, v2, v3, soff, ldsaddr)                                                            \
  do { u32 keep_;                                                                                                \
    asm volatile("s_mov_b32 %0, m0\n\ts_mov_b32 m0, %1\n\ts_nop 0\n\t"                                           \
                 "buffer_load_dwordx4 %4, %2, %3 offen " AUX " lds\n\t"                                          \
                 "buffer_load_dwordx4 %5, %2, %3 offen offset:1024 " AUX " lds\n\t"                              \
                 "buffer_load_dwordx4 %6, %2, %3 offen offset:2048 " AUX " lds\n\t"                              \
                 "buffer_load_dwordx4 %7, %2, %3 offen offset:3072 " AUX " lds\n\ts_mov_b32 m0, %0"              \
                 : "=&s"(keep_) : "s"(ldsaddr), "s"(srd), "s"(soff), "v"(v0), "v"(v1), "v"(v2), "v"(v3) : "memory"); } while (0)

#define DMA4W(AUX, srd, voff, so, step, ldsaddr)                                                                 \
  do { u32 keep_; const u32 d_ = (step) - 1024u, s0_ = (so), s1_ = s0_ + d_, s2_ = s1_ + d_, s3_ = s2_ + d_;     \
    asm volatile("s_mov_b32 %0, m0\n\ts_mov_b32 m0, %1\n\ts_nop 0\n\t"                                          \
                 "buffer_load_dwordx4 %7, %2, %3 offen " AUX " lds\n\t"                                          \
                 "buffer_load_dwordx4 %7, %2, %4 offen offset:1024 " AUX " lds\n\t"                              \
                 "buffer_load_dwordx4 %7, %2, %5 offen offset:2048 " AUX " lds\n\t"                              \
                 "buffer_load_dwordx4 %7, %2, %6 offen offset:3072 " AUX " lds\n\ts_mov_b32 m0, %0"              \
                 : "=&s"(keep_) : "s"(ldsaddr), "s"(srd), "s"(s0_), "s"(s1_), "s"(s2_), "s"(s3_), "v"(voff) : "memory"); } while (0)

template <int CTRL> __device__ __forceinline__ float dpp(float x) {
  return __builtin_bit_cast(float, __builtin_amdgcn_mov_dpp(__builtin_bit_cast(int, x), CTRL, 0xf, 0xf, true));
}
constexpr int ROR8 = 0x128, ROR4 = 0x124, ROR2 = 0x122, ROR1 = 0x121;
__device__ __forceinline__ float xrow_max(float x) {
  auto s = __builtin_amdgcn_permlane16_swap(__float_as_uint(x), __float_as_uint(x), false, false);
  x = fmaxf(__uint_as_float(s[0]), __uint_as_float(s[1]));
  auto t = __builtin_amdgcn_permlane32_swap(__float_as_uint(x), __float_as_uint(x), false, false);
  return fmaxf(__uint_as_float(t[0]), __uint_as_float(t[1]));
}
__device__ __forceinline__ float xrow_sum(float x) {
  auto s = __builtin_amdgcn_permlane16_swap(__float_as_uint(x), __float_as_uint(x), false, false);
  x = __uint_as_float(s[0]) + __uint_as_float(s[1]);
  auto t = __builtin_amdgcn_permlane32_swap(__float_as_uint(x), __float_as_uint(x), false, false);
  return __uint_as_float(t[0]) + __uint_as_float(t[1]);
}
__device__ __forceinline__ float wave_max(float x) {
  x = fmaxf(x, dpp<ROR8>(x)); x = fmaxf(x, dpp<ROR4>(x)); x = fmaxf(x, dpp<ROR2>(x)); x = fmaxf(x, dpp<ROR1>(x));
  return xrow_max(x);
}
__device__ __forceinline__ float wave_sum(float x) {
  x += dpp<ROR8>(x); x += dpp<ROR4>(x); x += dpp<ROR2>(x); x += dpp<ROR1>(x);
  return xrow_sum(x);
}
__device__ __forceinline__ float sum_x8_16_32(float x) {
  x += dpp<ROR8>(x);
  return xrow_sum(x);
}

typedef volatile LAS u32* flagp;
__device__ __forceinline__ void lds_wait_ge(flagp F, int idx, u32 need) {
  (void)F;
  const u32 a = L_FLG + idx * 4, ab = L_FLG + F_ABORT * 4;
  u32 v, z, cnt;
  asm volatile("s_mov_b32 %2, 0\n"
               "1:\n\tds_read_b32 %0, %3\n\tds_read_b32 %1, %4\n\ts_waitcnt lgkmcnt(0)\n\t"
               "v_cmp_le_u32 vcc, %5, %0\n\ts_cbranch_vccnz 2f\n\t"
               "v_cmp_ne_u32 vcc, 0, %1\n\ts_cbranch_vccnz 2f\n\t"
               "s_sleep 1\n\ts_add_u32 %2, %2, 1\n\ts_cmp_lt_u32 %2, %6\n\ts_cbranch_scc1 1b\n\t"
               "v_mov_b32 %0, 1\n\tds_write_b32 %4, %0\n"
               "2:"
               : "=&v"(v), "=&v"(z), "=&s"(cnt) : "v"(a), "v"(ab), "v"(need), "s"(LDS_SPIN_CAP) : "vcc", "scc", "memory");
}
__device__ __forceinline__ void lds_add(int idx, u32 n) {
  const u32 a = L_FLG + idx * 4;
  asm volatile("s_waitcnt lgkmcnt(0)\n\ts_mov_b64 vcc, exec\n\ts_mov_b64 exec, 1\n\tds_add_u32 %0, %1\n\ts_mov_b64 exec, vcc"
               :: "v"(a), "v"(n) : "vcc", "memory");
}
__device__ __forceinline__ bool spin_ok(flagp F, u32& spins) {
  if (++spins > GLB_SPIN_CAP) { F[F_ABORT] = 2u; return false; }
  if ((spins & 255u) == 0u && F[F_ABORT] != 0u) return false;
  return true;
}

constexpr int SC1 = 16;
typedef __amdgpu_buffer_rsrc_t rsrc_t;
__device__ __forceinline__ u32x4 ld128(rsrc_t r, int off) { return __builtin_amdgcn_raw_buffer_load_b128(r, off, 0, SC1); }
__device__ __forceinline__ u32x2 ld64(rsrc_t r, int off) { return __builtin_amdgcn_raw_buffer_load_b64(r, off, 0, SC1); }
__device__ __forceinline__ u32 ld32(rsrc_t r, int off) { return __builtin_amdgcn_raw_buffer_load_b32(r, off, 0, SC1); }
__device__ __forceinline__ void st128(rsrc_t r, int off, u32x4 v) { __builtin_amdgcn_raw_buffer_store_b128(v, r, off, 0, SC1); }
__device__ __forceinline__ void st64(rsrc_t r, int off, u32x2 v) { __builtin_amdgcn_raw_buffer_store_b64(v, r, off, 0, SC1); }

__device__ __forceinline__ u32x4 tr_frag(LAS unsigned char* L, u32 addr, u32 stride4) {
  const s16x4 a = __builtin_amdgcn_ds_read_tr16_b64_v4i16((LAS s16x4*)(L + addr));
  const s16x4 b = __builtin_amdgcn_ds_read_tr16_b64_v4i16((LAS s16x4*)(L + addr + stride4));
  const u32x2 a2 = __builtin_bit_cast(u32x2, a), b2 = __builtin_bit_cast(u32x2, b);
  return u32x4{a2.x, a2.y, b2.x, b2.y};
}
__device__ __forceinline__ u32x4 lds128(LAS unsigned char* L, u32 addr) { return *(LAS u32x4*)(L + addr); }

#define STAMP(i) do {} while (0)

__device__ __forceinline__ void ln_stats(const float (&v)[10], bool tail, float& mean_r, float& sd_r) {
  float s = 0.f;
#pragma unroll
  for (int i = 0; i < 8; ++i) s += v[i];
  if (tail) s += v[8] + v[9];
  const float mean = wave_sum(s) * (1.0f / DM);
  float q = 0.f;
#pragma unroll
  for (int i = 0; i < 8; ++i) { const float d = v[i] - mean; q += d * d; }
  if (tail) { const float d0 = v[8] - mean, d1 = v[9] - mean; q += d0 * d0 + d1 * d1; }
  const float var = wave_sum(q) * (1.0f / DM);
  mean_r = rb(mean);
  sd_r = rb(sqrtf(rb(rb(var) + 1e-5f)));
}
__device__ __forceinline__ float ln_mod(float x, float mean_r, float sd_r, float scale, float shift) {
  const float n = rb(rb(x - mean_r) / sd_r);
  return rb(rb(n * rb(1.0f + scale)) + shift);
}

__global__ void __launch_bounds__(NT, 2) fused_block(Params p) {
  extern __shared__ __attribute__((aligned(16))) unsigned char smem[];
  LAS unsigned char* L = (LAS unsigned char*)smem;
  flagp F = (flagp)(L + L_FLG);
  const int tid = threadIdx.x, lane = tid & 63, w = __builtin_amdgcn_readfirstlane(tid >> 6);
  STAMP(0);
  const u32 t_start = (u32)__builtin_amdgcn_s_memrealtime();
  const int c = blockIdx.x, xg = c & 7, ci = c >> 3;
  const int hkv = 2 * xg + (ci >> 4), cj = ci & 15, bb = cj >> 1, half = cj & 1;
  const int pj = ci >> 1;
  const bool isF = half == 0 || pj >= 14, isO = !isF && pj < 9;
  const int hu = xg * 5 + (pj - 9);
  const bool isH = !isF && !isO && hu < 36;
  const int f = xg * 18 + (half == 0 ? pj : pj + 2);
  const int u = pj;
  const int mu = xg * 9 + u;
  const bool isQ = isO && u < 8;
  const int shift = pj < 9 ? (pj < 8 ? SHIFT_O : SHIFT_O / 2) : pj < 14 ? (xg * 5 + (pj - 9) < 36 ? SHIFT_H : 0) : 0;
  const int nsl = half == 0 ? 64 + shift : 64 - shift;
  const int key0 = half == 0 ? 0 : (64 + shift) * 64;
  const u32 Wtot = isF ? (DEFER_DOWN ? 72u : 108u) : isO ? 136u : isH ? 108u : 0u;
  const int roff = isF ? (DEFER_DOWN ? (shift == 1 ? 6 : 5) : 7) : isH ? 7 : 0;
  const int posd = (2 * nsl + roff) & 7;
  const int g = lane >> 4, r16 = lane & 15, qp = (lane & 15) >> 2, pp = lane & 3;
  const rsrc_t rs = __builtin_amdgcn_make_buffer_rsrc((void*)p.ws, 0, WS_END, 0x00020000);

  u32x4 wf[18];
  u32 cbp[5], sbp[5];
  u32x4 pre4a, pre4b;
  float osA = 0.f, osB = 0.f;
  u32 nfin = 0;

  auto load_cbsb = [&](int row, u32 (&cb)[5], u32 (&sb)[5]) {
    u32 spins = 0;
    for (;;) {
      MEMBAR();
      const int base = WS_MODT + row * 4608;
#pragma unroll
      for (int i = 0; i < 4; ++i) { sb[i] = ld32(rs, base + (288 + lane + 64 * i) * 4); cb[i] = ld32(rs, base + (576 + lane + 64 * i) * 4); }
      sb[4] = ld32(rs, base + (288 + 256 + (lane & 31)) * 4); cb[4] = ld32(rs, base + (576 + 256 + (lane & 31)) * 4);
      u32 mx = 0;
#pragma unroll
      for (int i = 0; i < 5; ++i) mx = umax(mx, umax(sb[i], cb[i]));
      if (__all(mx != SENT) || !spin_ok(F, spins)) break;
    }
#pragma unroll
    for (int i = 0; i < 5; ++i) cb[i] = pack2(1.0f + bflo(cb[i]), 1.0f + bfhi(cb[i]));
  };
  auto get_tag = [&]() { lds_wait_ge(F, F_TAGOK, 1u); return (u32)F[F_TAG]; };
  auto q_partial = [&](int kt, f32x4 (&acc)[4]) {
    lds_wait_ge(F, F_LANDED, 72u);
    u32x4 pq[24];
    const u32 a0 = L_RING + (kt * 192 + 8 * g + qp) * 128 + 8 * pp;
#pragma unroll
    for (int s = 0; s < 6; ++s)
#pragma unroll
      for (int ct = 0; ct < 4; ++ct) pq[s * 4 + ct] = tr_frag(L, a0 + s * 4096 + ct * 32, 4 * 128);
    lds_add(F_WREL + 0, 1);
    const u32 tag = get_tag();
    STAMP(6);
    u32x4 hv[6];
    u32 spins = 0;
    const int hb = WS_H + xg * 18432 + kt * 768;
    int go[6], lo[6];
#pragma unroll
    for (int i = 0; i < 6; ++i) { const int e = i * 64 + lane, row = e / 48, j = e % 48; go[i] = hb + row * 2304 + j * 16; lo[i] = L_ACT + row * ACT_STRIDE + kt * 384 + j * 8; }
    if (HGATE) { while ((u32)__builtin_amdgcn_s_memrealtime() - t_start < (u32)HGATE) __builtin_amdgcn_s_sleep(2); }
    for (;;) {
      MEMBAR();
#pragma unroll
      for (int i = 0; i < 6; ++i) hv[i] = ld128(rs, go[i]);
      u32 bad = 0;
#pragma unroll
      for (int i = 0; i < 6; ++i) bad |= (hv[i].y ^ tag) | (hv[i].w ^ tag);
      if (__all(bad == 0u) || !spin_ok(F, spins)) break;
      __builtin_amdgcn_s_sleep(HPOLL_SLEEP);
    }
#pragma unroll
    for (int i = 0; i < 6; ++i) *(LAS u32x2*)(L + lo[i]) = u32x2{hv[i].x, hv[i].z};
    WAIT_LGKM0();
    STAMP(3);
    const u32 ab = L_ACT + (lane & 7) * ACT_STRIDE + kt * 384 + g * 16;
#pragma unroll
    for (int ct = 0; ct < 4; ++ct) acc[ct] = f32x4{0, 0, 0, 0};
#pragma unroll
    for (int s = 0; s < 6; ++s) {
      const u32x4 bfr = lds128(L, ab + s * 64);
#pragma unroll
      for (int ct = 0; ct < 4; ++ct) acc[ct] = mfma(pq[s * 4 + ct], bfr, acc[ct]);
    }
  };

  if (w == 0) {
    const u32 t_entry = t_start;
    __builtin_amdgcn_s_setprio(3);
    if (lane < 32) F[lane] = 0u;
    u32 n = 0;
    u32x4 srd0; u32 v0, step0, pre;
    if (isF) { srd0 = mk_srd(p.wg + f * 64, DM * 9216 - f * 64); v0 = (lane >> 2) * 9216 + (lane & 3) * 16; step0 = 16 * 9216; pre = 16; }
    else if (isQ) { srd0 = mk_srd(p.wq + (xg * 512 + u * 64) * 2, DM * 8192 - (xg * 512 + u * 64) * 2); v0 = (lane >> 3) * 8192 + (lane & 7) * 16; step0 = 8 * 8192; pre = WQW - 4; }
    else if (isH) { srd0 = mk_srd(p.wmod + hu * 32, DM * 6912 - hu * 32); v0 = (lane >> 1) * 6912 + (lane & 1) * 16; step0 = 32 * 6912; pre = 8; }
    else { srd0 = mk_srd(p.wq, 0); v0 = 0; step0 = 1024; pre = 0; }
    STAMP(24);
#pragma unroll 1
    for (; n < pre; n += 4) DMA4W(AUX_W, srd0, v0, n * step0, step0, L_RING + n * 1024);
    STAMP(25);
    BARRIER();
    STAMP(4);
    int pub = 0;
    auto run = [&](auto inflp, const u32x4& srd, u32 voff, u32 step, u32 cnt, u32 lds0, u32 first = 0) {
      constexpr int NP = decltype(inflp)::value;
      constexpr int KEEP = NP >= 8 ? NP - 4 : NP / 2;
      u32 s = first;
#pragma unroll 1
      for (; s + 4 <= cnt; s += 4) {
        DMA4W(AUX_W, srd, voff, s * step, step, lds0 + s * 1024);
        n += 4;
        asm volatile("s_waitcnt vmcnt(%0)" :: "i"(KEEP) : "memory");
        pub = max(pub, (int)n - KEEP); F[F_LANDED] = (u32)pub;
      }
#pragma unroll 1
      for (; s < cnt; ++s) { DMA1(AUX_W, srd, voff, s * step, lds0 + s * 1024); ++n; }
    };
    typedef std::integral_constant<int, INFLW / 16> P16;
    typedef std::integral_constant<int, (INFLW / 8 > 32 ? 32 : INFLW / 8)> P8;
    if (isF) {
      const u32 vg = (lane >> 2) * 9216 + (lane & 3) * 16;
      run(P16(), srd0, v0, step0, 36, L_RING, pre);
      run(P16(), mk_srd(p.wu + f * 64, DM * 9216 - f * 64), vg, 16 * 9216, 36, L_RING + 36 * 1024);
      if (!DEFER_DOWN) run(P8(), mk_srd(p.wd + f * 36864, 36864), (u32)(lane * 16), 1024, 36, L_RING + 72 * 1024);
    } else if (isO) {
      if (isQ) run(std::integral_constant<int, WQW>(), srd0, v0, step0, 72, L_RING, pre);
      else n = 72;
      run(std::integral_constant<int, WOW>(), mk_srd(p.wo + xg * 512 * 1152 + u * 128, 512 * 1152 - u * 128), (lane >> 3) * 1152 + (lane & 7) * 16, 8 * 1152, 64, L_RING + 72 * 1024);
    } else if (isH) {
      const u32 vm32 = (lane >> 1) * 6912 + (lane & 1) * 16;
      run(std::integral_constant<int, P1W>(), srd0, v0, step0, 18, L_RING, pre);
      run(std::integral_constant<int, P1W>(), mk_srd(p.wmod + 1152 + hu * 32, DM * 6912 - 1152 - hu * 32), vm32, 32 * 6912, 18, L_RING + 18 * 1024);
      run(P8(), mk_srd(p.wmod + 2304 + hu * 128, DM * 6912 - 2304 - hu * 128), (lane >> 3) * 6912 + (lane & 7) * 16, 8 * 6912, 72, L_RING + 36 * 1024);
    }
    STAMP(1);
    const size_t kvoff = ((size_t)(bb * HKV + hkv) * SK + (size_t)key0) * 256;
    const u32x4 srd_k = mk_srd(p.kc + kvoff, (u32)nsl * 16384u), srd_v = mk_srd(p.vc + kvoff, (u32)nsl * 16384u);
    u32 vk[4], vv[2];
#pragma unroll
    for (int j = 0; j < 4; ++j) vk[j] = g * 256 + ((r16 ^ (4 * j + g)) << 4);
#pragma unroll
    for (int j = 0; j < 2; ++j) vv[j] = g * 256 + ((r16 ^ ((g | (j << 2)) << 1)) << 4);
    const u32 wneed = isF ? (DEFER_DOWN ? 0x00022211u : 0x03332211u) : isO ? 0x33221111u : isH ? 0x02222211u : 0u;
    auto wait_window = [&](u32 N) {
      asm volatile("s_waitcnt vmcnt(28)\n\ts_cmp_ge_u32 %0, 28\n\ts_cbranch_scc1 9f\n\t"
                   "s_waitcnt vmcnt(24)\n\ts_cmp_ge_u32 %0, 24\n\ts_cbranch_scc1 9f\n\t"
                   "s_waitcnt vmcnt(20)\n\ts_cmp_ge_u32 %0, 20\n\ts_cbranch_scc1 9f\n\t"
                   "s_waitcnt vmcnt(16)\n9:" :: "s"(N) : "scc", "memory");
    };
    u32 fr_next = 0;
    u32 t_prev = t_entry, eb_next = 0, t_ref = t_entry;
    auto wait_free = [&](int pos, u32 need, u32 seen) {
      if (seen < need && F[F_FREE + pos] < need) { WAIT_VM0(); F[F_LANDED] = n; pub = (int)n; lds_wait_ge(F, F_FREE + pos, need); }
    };
#pragma unroll 1
    for (int q = 0; q < 2 * nsl; ++q) {
      const int pos = (q + roff) & 7;
      if (q >= 8) wait_free(pos, (u32)(q - 7), fr_next);
      else {
        const int nb = (wneed >> (4 * pos)) & 15;
#pragma unroll 1
        for (int i = 0; i < nb; ++i)
          if (F[F_WREL + i] < 7u) { WAIT_VM0(); F[F_LANDED] = n; pub = (int)n; lds_wait_ge(F, F_WREL + i, 7u); }
      }
      int lvl = q < EARLY_Q ? EARLY_LVL : BASE_LVL;
      if (q == SCHED_Q0) { const u32 t0_ = __builtin_amdgcn_readfirstlane((u32)F[F_T0]); if (t0_) t_ref = t0_; }
      if (q >= SCHED_Q0) {
        const u32 eown = t_prev + (u32)(((2 * nsl - q + 1) * SCHED_TAU) >> 4);
        F[F_EOWN] = eown;
        const u32 eb = __builtin_amdgcn_readfirstlane(eb_next);
        const int lag = eb ? (int)(eown - eb) - SCHED_LA : (int)(eown - t_ref) - SCHED_TE - SCHED_L1;
        if (lag > 0) lvl = BASE_LVL + BOOST1;
        if (lag > SCHED_L2) lvl = BASE_LVL + BOOST2;
      }
      eb_next = F[F_EBAR];
      t_prev = (u32)__builtin_amdgcn_s_memrealtime();
      const u32 N2 = 16u + 4u * (u32)((lvl + 1) >> 1), N = 16u + 4u * (u32)(lvl >> 1);
      fr_next = F[F_FREE + ((pos + 1) & 7)];
      const u32 lb = L_RING + pos * 16384;
      const bool isk = q < nsl;
      const u32x4 srd = isk ? srd_k : srd_v;
      const u32 v0_ = isk ? vk[0] : vv[0], v1_ = isk ? vk[1] : vv[0], v2_ = isk ? vk[2] : vv[1], v3_ = isk ? vk[3] : vv[1];
      const u32 so = (u32)(isk ? q : q - nsl) * 16384u;
#pragma unroll
      for (int gq = 0; gq < 4; ++gq) {
        DMA4(AUX_KV, srd, v0_, v1_, v2_, v3_, so + gq * 4096, lb + gq * 4096);
        n += 4;
        const u32 Ng = gq < 2 ? N2 : N;
        wait_window(Ng);
        pub = max(pub, (int)n - (int)Ng); F[F_LANDED] = (u32)pub;
      }
    }
    if (isF && DEFER_DOWN) {
      STAMP(5);
      for (int i = 0; i < 3; ++i) wait_free(posd + i, (u32)(2 * nsl - 7 + i), 0u);
      run(P8(), mk_srd(p.wd + f * 36864, 36864), (u32)(lane * 16), 1024, 36, L_RING + posd * 16384);
    }
    if (!isF || !DEFER_DOWN) { WAIT_VM0(); F[F_LANDED] = n; }
    nfin = n;
    STAMP(3);
    __builtin_amdgcn_s_setprio(0);
    if (isF) {
      lds_wait_ge(F, F_CB0, 1u);
#pragma unroll
      for (int i = 0; i < 5; ++i) { cbp[i] = *(LAS u32*)(L + L_RED + i * 256 + lane * 4); sbp[i] = *(LAS u32*)(L + L_RED + 1280 + i * 256 + lane * 4); }
    }
  } else if (w == 7) {
    BARRIER();
    u64 epoch = *(const __attribute__((address_space(4))) u64*)(p.ws + WS_CTRL) + 1ull;
    asm volatile("" : "+s"(epoch));
    const u32 tag = (u32)epoch;
    F[F_TAG] = tag;
    WAIT_LGKM0();
    F[F_TAGOK] = 1u;
    if (GLOBAL_T0 && c == 0 && lane < 8) st64(rs, WS_T0 + lane * 128, u32x2{t_start, tag});
    const u32x4 z4 = {0u, 0u, 0u, 0u}, s4 = {SENT, SENT, SENT, SENT};
    if (isH) {
#pragma unroll
      for (int i = 0; i < 2; ++i) st64(rs, WS_MODT + (lane & 7) * 4608 + hu * 128 + (2 * i + (r16 >> 3)) * 32 + g * 8, u32x2{SENT, SENT});
    }
    st128(rs, WS_OP + c * 1024 + lane * 16, z4);
    if (lane == 0) st128(rs, WS_ST + c * 16, z4);
    if (isO) {
#pragma unroll
      for (int i = 0; i < 2; ++i) st128(rs, WS_WP + (xg * 144 + u * 16) * 128 + i * 1024 + lane * 16, z4);
      st128(rs, WS_X1 + (lane >> 3) * 9216 + mu * 128 + (lane & 7) * 16, s4);
    }
    if (isF) {
#pragma unroll 1
      for (int i = 0; i < 9; ++i) st128(rs, WS_FP + f * 9216 + i * 1024 + lane * 16, s4);
    }
    WAIT_VM0();
    F[F_INIT] = 1u;
    if (lane == 0) __hip_atomic_store((GAS u64*)(p.ws + WS_ALIVE) + c, epoch, __ATOMIC_RELAXED, __HIP_MEMORY_SCOPE_AGENT);
    STAMP(1);
    if (isQ) {
      f32x4 acc[4];
      q_partial(2, acc);
      lds_add(F_WREL + 1, 1); lds_add(F_WREL + 2, 1);
      lds_wait_ge(F, F_P2, 2u);
      const int ls = (g * 8 + (lane & 7)) * 16;
#pragma unroll
      for (int ct = 0; ct < 4; ++ct) {
        const f32x4 a = (*(LAS f32x4*)(L + L_P2 + ct * 512 + ls) + *(LAS f32x4*)(L + L_P2 + 2048 + ct * 512 + ls)) + acc[ct];
        if (r16 < 8) st128(rs, WS_Q + ((2 * xg + (u >> 2)) * 8 + r16) * 1024 + ((u & 3) * 16 + ct * 4 + g) * 16, u32x4{pack2(a[0], a[1]), tag, pack2(a[2], a[3]), tag});
      }
      STAMP(4);
    } else {
      lds_add(F_WREL + 0, 1); lds_add(F_WREL + 1, 1); lds_add(F_WREL + 2, 1);
    }
    {
      u32x4 qv; u32x2 t0v = {0u, 0u}; u32 spins = 0;
      if (QGATE) { while ((u32)__builtin_amdgcn_s_memrealtime() - t_start < (u32)QGATE) __builtin_amdgcn_s_sleep(2); }
      for (;;) {
        MEMBAR();
        qv = ld128(rs, WS_Q + (hkv * 8 + bb) * 1024 + lane * 16);
        if (GLOBAL_T0) t0v = ld64(rs, WS_T0 + xg * 128);
        if (__all(((qv.y ^ tag) | (qv.w ^ tag)) == 0u) || !spin_ok(F, spins)) break;
        __builtin_amdgcn_s_sleep(QPOLL_SLEEP);
      }
      *(LAS u32x2*)(L + L_QS + lane * 8) = u32x2{qv.x, qv.z};
      F[F_T0] = (GLOBAL_T0 && t0v.y == tag) ? t0v.x : t_start;
      WAIT_LGKM0();
      F[F_Q] = 1u;
    }
    STAMP(5);
    auto sample = [&](int r, int seq) {
    };
    sample(0, 26);
    lds_wait_ge(F, F_LANDED, Wtot + 16u * (u32)(nsl - 8));
    {
      const u32 ehi = (u32)(epoch >> 32);
      u32 spins = 0;
      for (;;) {
        MEMBAR();
        const u32x4 a = ld128(rs, WS_ALIVE + lane * 32), b2 = ld128(rs, WS_ALIVE + lane * 32 + 16);
        const u32 bad = (a.x ^ tag) | (a.y ^ ehi) | (a.z ^ tag) | (a.w ^ ehi) | (b2.x ^ tag) | (b2.y ^ ehi) | (b2.z ^ tag) | (b2.w ^ ehi);
        if (__all(bad == 0u) || !spin_ok(F, spins)) break;
        __builtin_amdgcn_s_sleep(64);
      }
    }
    F[F_ALIVE] = 1u;
    if (c == 0 && lane == 0) __hip_atomic_store((GAS u64*)(p.ws + WS_CTRL), epoch, __ATOMIC_RELAXED, __HIP_MEMORY_SCOPE_AGENT);
    {
      lds_wait_ge(F, F_L, 6u);
      const int hd = lane & 1;
      float M = *(LAS float*)(L + L_MX + hd * 4), Ls = *(LAS float*)(L + L_LW + hd * 4);
#pragma unroll
      for (int i = 1; i < 6; ++i) { M = fmaxf(M, *(LAS float*)(L + L_MX + i * 8 + hd * 4)); Ls += *(LAS float*)(L + L_LW + i * 8 + hd * 4); }
      if (lane < 2) st64(rs, WS_ST + c * 16 + hd * 8, u32x2{vld(M), vld(Ls)});
      u32x2 o; u32 spins = 0;
      for (;;) {
        MEMBAR();
        o = ld64(rs, WS_ST + (c ^ 8) * 16 + hd * 8);
        if (__all((o.x & o.y & 1u) != 0u) || !spin_ok(F, spins)) break;
        __builtin_amdgcn_s_sleep(8);
      }
      const float m0 = __uint_as_float(vld(M)), l0 = __uint_as_float(vld(Ls)), m1 = __uint_as_float(o.x), l1 = __uint_as_float(o.y);
      const float Mg = fmaxf(m0, m1);
      const float e0 = __builtin_amdgcn_exp2f((m0 - Mg) * LOG2E), e1 = __builtin_amdgcn_exp2f((m1 - Mg) * LOG2E);
      const float sc = e0 / (e0 * l0 + e1 * l1);
      osA = __builtin_bit_cast(float, __builtin_amdgcn_readlane(__builtin_bit_cast(int, sc), 0));
      osB = __builtin_bit_cast(float, __builtin_amdgcn_readlane(__builtin_bit_cast(int, sc), 1));
    }
    if (isF) {
      load_cbsb(0, cbp, sbp);
#pragma unroll
      for (int i = 0; i < 5; ++i) { *(LAS u32*)(L + L_RED + i * 256 + lane * 4) = cbp[i]; *(LAS u32*)(L + L_RED + 1280 + i * 256 + lane * 4) = sbp[i]; }
      WAIT_LGKM0();
      F[F_CB0] = 1u;
      load_cbsb(7, cbp, sbp);
      u32x2 gb; u32 spins = 0;
      for (;;) {
        MEMBAR();
        gb = ld64(rs, WS_MODT + (lane & 7) * 4608 + 864 * 4 + f * 8);
        if (__all(umax(gb.x, gb.y) != SENT) || !spin_ok(F, spins)) break;
      }
      pre4a = u32x4{gb.x, gb.y, 0u, 0u};
    } else if (isO) {
      u32 spins = 0;
      for (;;) {
        MEMBAR();
        pre4a = ld128(rs, WS_MODT + (lane & 7) * 4608 + mu * 16);
        if (__all(umax4(pre4a) != SENT) || !spin_ok(F, spins)) break;
      }
      pre4b = *(const GAS u32x4*)(p.x + (lane & 7) * DM + mu * 8);
    }
    STAMP(7);
    sample(1, nsl + 12);
    sample(2, nsl + 34);
  } else {
    const int cw = w - 1;
    if (isF) {
      BARRIER();
      const int m = cw & 1, kt = cw >> 1;
      const u32 a0 = L_RING + 36 * m * 1024 + (kt * 192 + 8 * g + qp) * 64 + 8 * pp;
      if (m == 1) lds_add(F_WREL + 0, 1);
      lds_wait_ge(F, F_LANDED, m == 0 ? 36u : 72u);
#pragma unroll
      for (int s = 0; s < 6; ++s) { wf[2 * s] = tr_frag(L, a0 + s * 2048, 4 * 64); wf[2 * s + 1] = tr_frag(L, a0 + s * 2048 + 32, 4 * 64); }
      if (m == 0) lds_add(F_WREL + 0, 1);
      lds_add(F_WREL + 1, 1);
      if (!DEFER_DOWN) {
        lds_wait_ge(F, F_LANDED, 108u);
        const u32 d0 = L_RING + 72 * 1024 + (8 * g + qp) * 1152 + cw * 192 + 8 * pp;
#pragma unroll
        for (int t = 0; t < 6; ++t) wf[12 + t] = tr_frag(L, d0 + t * 32, 4 * 1152);
      }
      lds_add(F_WREL + 2, 1);
    } else if (isO) {
      BARRIER();
      if (cw < 4) {
        lds_add(F_WREL + 0, 1);
        const u32 a0 = L_RING + 72 * 1024 + (8 * g + qp) * 128 + cw * 32 + 8 * pp;
        lds_wait_ge(F, F_LANDED, 104u);
#pragma unroll
        for (int s = 0; s < 8; ++s) wf[s] = tr_frag(L, a0 + s * 4096, 4 * 128);
        lds_add(F_WREL + 1, 1);
        lds_wait_ge(F, F_LANDED, 136u);
#pragma unroll
        for (int s = 8; s < 16; ++s) wf[s] = tr_frag(L, a0 + s * 4096, 4 * 128);
        lds_add(F_WREL + 2, 1);
      } else {
        if (isQ) {
          f32x4 acc[4];
          q_partial(cw - 4, acc);
          if (r16 < 8) {
#pragma unroll
            for (int ct = 0; ct < 4; ++ct) *(LAS f32x4*)(L + L_P2 + (cw - 4) * 2048 + ct * 512 + (g * 8 + r16) * 16) = acc[ct];
          }
          lds_add(F_P2, 1);
        } else lds_add(F_WREL + 0, 1);
        lds_add(F_WREL + 1, 1); lds_add(F_WREL + 2, 1);
      }
    } else if (isH) {
      u32 cv[6], xa[5], xb[5];
#pragma unroll
      for (int i = 0; i < 6; ++i) cv[i] = ((const GAS u32*)p.c)[cw * 64 + lane + 384 * i];
      const int rowb = 6 + (cw & 1);
      const GAS u32* xr = (const GAS u32*)p.x + cw * 288;
      const GAS u32* xs = (const GAS u32*)p.x + rowb * 288;
#pragma unroll
      for (int i = 0; i < 4; ++i) { xa[i] = xr[lane + 64 * i]; xb[i] = xs[lane + 64 * i]; }
      xa[4] = xr[256 + (lane & 31)]; xb[4] = xs[256 + (lane & 31)];
      const u32x2 xq = *(const GAS u32x2*)(p.x + (lane & 7) * DM + hu * 16 + g * 4);
      BARRIER();
#pragma unroll
      for (int i = 0; i < 6; ++i) {
        const int e = cw * 64 + lane + 384 * i;
        const float a = bflo(cv[i]), b2 = bfhi(cv[i]);
        *(LAS u32*)(L + L_SC + (e / 288) * ACT_STRIDE + (e % 288) * 4) = pack2(a / (1.0f + __expf(-a)), b2 / (1.0f + __expf(-b2)));
      }
      {
        float v[10], mean_r, sd_r;
#pragma unroll
        for (int i = 0; i < 5; ++i) { v[2 * i] = bflo(xa[i]); v[2 * i + 1] = bfhi(xa[i]); }
        ln_stats(v, lane < 32, mean_r, sd_r);
        if (lane == 0) { *(LAS float*)(L + L_STAT + cw * 8) = mean_r; *(LAS float*)(L + L_STAT + cw * 8 + 4) = sd_r; }
        if (cw < 2) {
#pragma unroll
          for (int i = 0; i < 5; ++i) { v[2 * i] = bflo(xb[i]); v[2 * i + 1] = bfhi(xb[i]); }
          ln_stats(v, lane < 32, mean_r, sd_r);
          if (lane == 0) { *(LAS float*)(L + L_STAT + rowb * 8) = mean_r; *(LAS float*)(L + L_STAT + rowb * 8 + 4) = sd_r; }
        }
      }
      lds_add(F_SC, 1);
      STAMP(24);
      const u32 sb0 = L_SC + (lane & 7) * ACT_STRIDE + g * 16;
      {
        const int m = cw & 1, kt = cw >> 1;
        lds_wait_ge(F, F_SC, 6u);
        const float mean_r = *(LAS float*)(L + L_STAT + (lane & 7) * 8), sd_r = *(LAS float*)(L + L_STAT + (lane & 7) * 8 + 4);
        float n0 = rb(rb(bflo(xq.x) - mean_r) / sd_r), n1 = rb(rb(bfhi(xq.x) - mean_r) / sd_r);
        float n2 = rb(rb(bflo(xq.y) - mean_r) / sd_r), n3 = rb(rb(bfhi(xq.y) - mean_r) / sd_r);
        asm volatile("" : "+v"(n0), "+v"(n1), "+v"(n2), "+v"(n3));
        u32 tag = 0;
        if (cw == 5) tag = get_tag();
        u32x4 bfr[6];
#pragma unroll
        for (int s = 0; s < 6; ++s) bfr[s] = lds128(L, sb0 + (kt * 6 + s) * 64);
        lds_wait_ge(F, F_LANDED, 36u);
        STAMP(25);
        const u32 a0 = L_RING + (m * 18 + kt * 6) * 1024 + (8 * g + qp) * 32 + 8 * pp;
        f32x4 acc = {0, 0, 0, 0}, acc2 = {0, 0, 0, 0};
#pragma unroll
        for (int s = 0; s < 6; s += 2) { acc = mfma(tr_frag(L, a0 + s * 1024, 4 * 32), bfr[s], acc); acc2 = mfma(tr_frag(L, a0 + (s + 1) * 1024, 4 * 32), bfr[s + 1], acc2); }
        acc += acc2;
        if (r16 < 8) *(LAS f32x4*)(L + L_RED + cw * 512 + (g * 8 + r16) * 16) = acc;
        lds_add(F_P1, 1);
        lds_add(F_WREL + 0, 1);
        if (cw == 5) {
          lds_wait_ge(F, F_P1, 6u);
          const int ls = (g * 8 + (lane & 7)) * 16;
          const f32x4 sa = *(LAS f32x4*)(L + L_RED + 0 * 512 + ls) + *(LAS f32x4*)(L + L_RED + 2 * 512 + ls) + *(LAS f32x4*)(L + L_RED + 4 * 512 + ls);
          const f32x4 ca = *(LAS f32x4*)(L + L_RED + 1 * 512 + ls) + *(LAS f32x4*)(L + L_RED + 3 * 512 + ls) + *(LAS f32x4*)(L + L_RED + 5 * 512 + ls);
          const float h0 = rb(rb(n0 * rb(1.0f + rb(ca[0]))) + rb(sa[0])), h1 = rb(rb(n1 * rb(1.0f + rb(ca[1]))) + rb(sa[1]));
          const float h2 = rb(rb(n2 * rb(1.0f + rb(ca[2]))) + rb(sa[2])), h3 = rb(rb(n3 * rb(1.0f + rb(ca[3]))) + rb(sa[3]));
          const u32x4 hu4 = {pack2(h0, h1), tag, pack2(h2, h3), tag};
#pragma unroll
          for (int i = 0; i < REP_H / 2; ++i)
            st128(rs, WS_H + (2 * i + (r16 >> 3)) * 18432 + ((lane & 7) * 144 + hu * 4 + g) * 16, hu4);
          STAMP(6);
        }
      }
      if (cw < 4) {
        lds_wait_ge(F, F_LANDED, 108u);
        const u32 a0 = L_RING + 36 * 1024 + (8 * g + qp) * 128 + cw * 32 + 8 * pp;
        f32x4 a0c = {0, 0, 0, 0}, a1c = {0, 0, 0, 0};
#pragma unroll 3
        for (int s = 0; s < 18; s += 2) {
          a0c = mfma(tr_frag(L, a0 + s * 4096, 4 * 128), lds128(L, sb0 + s * 64), a0c);
          a1c = mfma(tr_frag(L, a0 + (s + 1) * 4096, 4 * 128), lds128(L, sb0 + (s + 1) * 64), a1c);
        }
        const f32x4 a = a0c + a1c;
        lds_add(F_WREL + 1, 1);
        lds_wait_ge(F, F_INIT, 1u);
        if (r16 < 8) st64(rs, WS_MODT + r16 * 4608 + hu * 128 + cw * 32 + g * 8, u32x2{nosent(pack2(a[0], a[1])), nosent(pack2(a[2], a[3]))});
      } else lds_add(F_WREL + 1, 1);
      lds_add(F_WREL + 2, 1);
    } else {
      BARRIER();
      lds_add(F_WREL + 0, 1); lds_add(F_WREL + 1, 1); lds_add(F_WREL + 2, 1);
    }
    STAMP(1);
    u32x4 qf[4];
    u32 ka[4];
#pragma unroll
    for (int s = 0; s < 4; ++s) ka[s] = r16 * 256 + (((4 * s + g) ^ r16) << 4);
    float mx0 = -INFINITY, mx1 = -INFINITY;
    auto k_read = [&](int q, u32x4* kf) {
      const int pos = (q + roff) & 7;
      lds_wait_ge(F, F_LANDED, Wtot + 16u * (u32)(q + 1));
      const u32 sb = L_RING + pos * 16384;
#pragma unroll
      for (int t = 0; t < 4; ++t)
#pragma unroll
        for (int s = 0; s < 4; ++s) kf[t * 4 + s] = lds128(L, sb + ka[s] + t * 4096);
      WAIT_LGKM0();
      if (lane == 0) F[F_FREE + pos] = (u32)(q + 1);
    };
    auto k_scores = [&](int q, const u32x4* kf) {
      f32x4 acc[4];
#pragma unroll
      for (int t = 0; t < 4; ++t) {
        acc[t] = f32x4{0, 0, 0, 0};
#pragma unroll
        for (int s = 0; s < 4; ++s) acc[t] = mfma(qf[s], kf[t * 4 + s], acc[t]);
      }
      float s0 = g == 0 ? acc[0][0] : g == 1 ? acc[1][0] : g == 2 ? acc[2][0] : acc[3][0];
      float s1 = g == 0 ? acc[0][1] : g == 1 ? acc[1][1] : g == 2 ? acc[2][1] : acc[3][1];
      s0 = rb(rb(s0) * 0.08838834764831845f);
      s1 = rb(rb(s1) * 0.08838834764831845f);
      mx0 = fmaxf(mx0, s0); mx1 = fmaxf(mx1, s1);
      *(LAS u16*)(L + L_SC + q * 256 + lane * 2) = (u16)(__float_as_uint(s0) >> 16);
      *(LAS u16*)(L + L_SC + q * 256 + 128 + lane * 2) = (u16)(__float_as_uint(s1) >> 16);
    };
    int q0 = cw + 6;
    {
      u32x4 kf[16], kg[16];
      k_read(cw, kf);
      lds_wait_ge(F, F_Q, 1u);
      lds_wait_ge(F, F_WREL + 1, 7u); lds_wait_ge(F, F_WREL + 2, 7u);
      STAMP(2);
#pragma unroll
      for (int s = 0; s < 4; ++s) qf[s] = lds128(L, L_QS + (lane & 1) * 256 + s * 64 + g * 16);
      k_scores(cw, kf);
    }
#pragma unroll 1
    for (int q = q0; q < nsl; q += 6) {
      u32x4 kf[16];
      k_read(q, kf);
      k_scores(q, kf);
    }
    STAMP(3);
    {
      mx0 = wave_max(mx0); mx1 = wave_max(mx1);
      if (lane == 0) *(LAS u32x2*)(L + L_MX + cw * 8) = u32x2{__float_as_uint(mx0), __float_as_uint(mx1)};
      lds_add(F_MX, 1);
      lds_wait_ge(F, F_MX, 6u);
#pragma unroll
      for (int i = 0; i < 6; ++i) { mx0 = fmaxf(mx0, *(LAS float*)(L + L_MX + i * 8)); mx1 = fmaxf(mx1, *(LAS float*)(L + L_MX + i * 8 + 4)); }
      float l0 = 0.f, l1 = 0.f;
#pragma unroll 1
      for (int q = cw; q < nsl; q += 6) {
        const float s0 = bf2f(*(LAS u16*)(L + L_SC + q * 256 + lane * 2)), s1 = bf2f(*(LAS u16*)(L + L_SC + q * 256 + 128 + lane * 2));
        const float p0 = __builtin_amdgcn_exp2f((s0 - mx0) * LOG2E), p1 = __builtin_amdgcn_exp2f((s1 - mx1) * LOG2E);
        l0 += p0; l1 += p1;
        *(LAS u16*)(L + L_SC + q * 256 + lane * 2) = (u16)f2bf(p0);
        *(LAS u16*)(L + L_SC + q * 256 + 128 + lane * 2) = (u16)f2bf(p1);
      }
      l0 = wave_sum(l0); l1 = wave_sum(l1);
      if (lane == 0) *(LAS u32x2*)(L + L_LW + cw * 8) = u32x2{__float_as_uint(l0), __float_as_uint(l1)};
      lds_add(F_L, 1);
    }
    f32x4 o[8];
#pragma unroll
    for (int cb = 0; cb < 8; ++cb) o[cb] = f32x4{0, 0, 0, 0};
    const u32 va0 = (8 * g + qp) * 256 + ((pp >> 1) << 4) + 8 * (pp & 1) + (((qp | ((g & 1) << 2)) << 1) << 4);
#pragma unroll 1
    for (int q = cw; q < nsl; q += 6) {
      const int pos = (q + nsl + roff) & 7;
      if (isF && q >= 24 && q < 30) { lds_wait_ge(F, F_ALIVE, 1u); load_cbsb(w, cbp, sbp); }
      const u32x4 pf0 = lds128(L, L_SC + q * 256 + (lane & 1) * 128 + g * 16), pf1 = lds128(L, L_SC + q * 256 + (lane & 1) * 128 + 64 + g * 16);
      lds_wait_ge(F, F_LANDED, Wtot + 16u * (u32)(q + nsl + 1));
      const u32 sb = L_RING + pos * 16384 + va0;
      u32x4 vf[8], vg2[8];
#pragma unroll
      for (int cb = 0; cb < 8; ++cb) vf[cb] = tr_frag(L, sb ^ (cb << 5), 1024);
#pragma unroll
      for (int cb = 0; cb < 8; ++cb) vg2[cb] = tr_frag(L, (sb ^ (cb << 5)) + 8192, 1024);
      WAIT_LGKM0();
      if (lane == 0) F[F_FREE + pos] = (u32)(q + nsl + 1);
#pragma unroll
      for (int cb = 0; cb < 8; ++cb) o[cb] = mfma(pf0, vf[cb], o[cb]);
#pragma unroll
      for (int cb = 0; cb < 8; ++cb) o[cb] = mfma(pf1, vg2[cb], o[cb]);
    }
    STAMP(4);
    if (g == 0) {
#pragma unroll
      for (int cb = 0; cb < 8; ++cb) *(LAS u32x2*)(L + L_ACT + cw * 1024 + (cb * 16 + r16) * 8) = u32x2{__float_as_uint(o[cb][0]), __float_as_uint(o[cb][1])};
    }
    lds_add(F_OPART, 1);
  }
  if (w == 7) {
    lds_wait_ge(F, F_OPART, 6u);
    STAMP(8);
    f32x4 O = *(LAS f32x4*)(L + L_ACT + lane * 16);
#pragma unroll
    for (int i = 1; i < 6; ++i) O += *(LAS f32x4*)(L + L_ACT + i * 1024 + lane * 16);
    st128(rs, WS_OP + c * 1024 + lane * 16, u32x4{vld(O[0] * osA), vld(O[1] * osB), vld(O[2] * osA), vld(O[3] * osB)});
    STAMP(9);
  }
  if (isO) {
    constexpr int L_OS = L_SC;
    u32x4 ov[4];
    bool got[4] = {false, false, false, false};
    u32 spins = 0;
    lds_wait_ge(F, F_ALIVE, 1u);
    for (;;) {
      MEMBAR();
#pragma unroll
      for (int i = 0; i < 4; ++i) if (!got[i]) ov[i] = ld128(rs, WS_OP + (xg + 8 * ((i >> 1) * 16 + 2 * w + (i & 1))) * 1024 + lane * 16);
      bool all = true;
#pragma unroll
      for (int i = 0; i < 4; ++i) if (!got[i]) {
        got[i] = __all((ov[i].x & ov[i].y & ov[i].z & ov[i].w & 1u) != 0u);
        all = all && got[i];
      }
      if (all || !spin_ok(F, spins)) break;
    }
#pragma unroll
    for (int hk = 0; hk < 2; ++hk) {
      const f32x4 s = __builtin_bit_cast(f32x4, ov[2 * hk]) + __builtin_bit_cast(f32x4, ov[2 * hk + 1]);
      *(LAS u32*)(L + L_OS + w * 1040 + hk * 512 + lane * 4) = pack2(s[0], s[2]);
      *(LAS u32*)(L + L_OS + w * 1040 + hk * 512 + 256 + lane * 4) = pack2(s[1], s[3]);
    }
    BARRIER();
    if (w != 0) STAMP(10);
    if (w >= 1 && w <= 4) {
      const int ct = w - 1;
      const u32 ob = L_OS + (lane & 7) * 1040 + g * 16;
      u32x4 bfr[16];
#pragma unroll
      for (int s = 0; s < 16; ++s) bfr[s] = lds128(L, ob + s * 64);
      f32x4 a0 = {0, 0, 0, 0}, a1 = {0, 0, 0, 0}, a2 = {0, 0, 0, 0}, a3 = {0, 0, 0, 0};
#pragma unroll
      for (int s = 0; s < 16; s += 4) { a0 = mfma(wf[s], bfr[s], a0); a1 = mfma(wf[s + 1], bfr[s + 1], a1); a2 = mfma(wf[s + 2], bfr[s + 2], a2); a3 = mfma(wf[s + 3], bfr[s + 3], a3); }
      const f32x4 a = (a0 + a1) + (a2 + a3);
      if (r16 < 8) st128(rs, WS_WP + (xg * 144 + (u * 4 + ct) * 4 + g) * 128 + r16 * 16, u32x4{vld(a[0]), vld(a[1]), vld(a[2]), vld(a[3])});
      STAMP(11);
    } else if (w == 7) {
      u32x4 pa, pb; u32 spins2 = 0;
      const int wo_ = WS_WP + ((lane >> 3) * 144 + 2 * mu) * 128 + (lane & 7) * 16;
      for (;;) {
        MEMBAR();
        pa = ld128(rs, wo_); pb = ld128(rs, wo_ + 128);
        if (__all((pa.x & pa.y & pa.z & pa.w & pb.x & pb.y & pb.z & pb.w & 1u) != 0u) || !spin_ok(F, spins2)) break;
      }
      float a[8];
      a[0] = __uint_as_float(pa.x); a[1] = __uint_as_float(pa.y); a[2] = __uint_as_float(pa.z); a[3] = __uint_as_float(pa.w);
      a[4] = __uint_as_float(pb.x); a[5] = __uint_as_float(pb.y); a[6] = __uint_as_float(pb.z); a[7] = __uint_as_float(pb.w);
#pragma unroll
      for (int i = 0; i < 8; ++i) a[i] = sum_x8_16_32(a[i]);
      u32x4 xo;
#pragma unroll
      for (int i = 0; i < 4; ++i) {
        const u32 ap = pack2(a[2 * i], a[2 * i + 1]);
        const u32 tp = pack2(bflo(pre4a[i]) * bflo(ap), bfhi(pre4a[i]) * bfhi(ap));
        xo[i] = nosent(pack2(bflo(pre4b[i]) + bflo(tp), bfhi(pre4b[i]) + bfhi(tp)));
      }
      st128(rs, WS_X1 + (lane >> 3) * 9216 + mu * 128 + (lane & 7) * 16, xo);
      STAMP(12);
    }
  } else if (isF) {
    const u32 n0 = Wtot + 32u * (u32)nsl;
    if (w == 7) {
      u32x4 pv; u32 spins = 0;
      for (;;) {
        MEMBAR();
        pv = ld128(rs, WS_WP + ((lane >> 3) * 144 + f) * 128 + (lane & 7) * 16);
        if (__all((pv.x & pv.y & pv.z & pv.w & 1u) != 0u) || !spin_ok(F, spins)) break;
        if (WPPOLL_SLEEP) __builtin_amdgcn_s_sleep(WPPOLL_SLEEP);
      }
      F[F_X1] = 1u;
      STAMP(10);
    } else lds_wait_ge(F, F_X1, 1u);
    if (DELAY_C) __builtin_amdgcn_s_sleep(DELAY_C);
    u32x2 x1p = {0u, 0u};
    {
      if (w != 0) {
        const int e0 = (w - 1) * 64 + lane, xb = WS_X1 + xg * 9216;
        u32x4 xv, xw = {0u, 0u, 0u, 0u}; u32 spins = 0;
        for (;;) {
          MEMBAR();
          xv = ld128(rs, xb + e0 * 16);
          if (w <= 2) xw = ld128(rs, xb + (448 + e0) * 16);
          if (__all(umax(umax4(xv), umax4(xw)) != SENT) || !spin_ok(F, spins)) break;
        }
        STAMP(16);
        const u32 a = L_ACT + (e0 & 7) * ACT_STRIDE + (e0 >> 3) * 16;
        *(LAS u32x4*)(L + a) = xv;
        if (w <= 2) *(LAS u32x4*)(L + a + 56 * 16) = xw;
      }
      BARRIER();
      if (w != 0) STAMP(17);
      u32 xr[5];
#pragma unroll
      for (int i = 0; i < 4; ++i) xr[i] = *(LAS u32*)(L + L_ACT + w * ACT_STRIDE + (lane + 64 * i) * 4);
      xr[4] = *(LAS u32*)(L + L_ACT + w * ACT_STRIDE + (256 + (lane & 31)) * 4);
      if (w == 7) x1p = *(LAS u32x2*)(L + L_ACT + (lane & 7) * ACT_STRIDE + f * 8);
      float v[10];
#pragma unroll
      for (int i = 0; i < 5; ++i) { v[2 * i] = bflo(xr[i]); v[2 * i + 1] = bfhi(xr[i]); }
      float s = (v[0] + v[1]) + (v[2] + v[3]) + ((v[4] + v[5]) + (v[6] + v[7]));
      if (lane < 32) s += v[8] + v[9];
      const float mean = wave_sum(s) * (1.0f / DM);
      float q2 = 0.f, q3 = 0.f;
#pragma unroll
      for (int i = 0; i < 4; ++i) { const float d0 = v[2 * i] - mean, d1 = v[2 * i + 1] - mean; q2 += d0 * d0; q3 += d1 * d1; }
      if (lane < 32) { const float d0 = v[8] - mean, d1 = v[9] - mean; q2 += d0 * d0; q3 += d1 * d1; }
      const float var = wave_sum(q2 + q3) * (1.0f / DM);
      const float mean_r = rb(mean);
      const float rinv = __builtin_amdgcn_rcpf(rb(__builtin_amdgcn_sqrtf(rb(rb(var) + 1e-5f))));
#pragma unroll
      for (int i = 0; i < 5; ++i) {
        const u32 dp = pack2(v[2 * i] - mean_r, v[2 * i + 1] - mean_r);
        const u32 np = pack2(bflo(dp) * rinv, bfhi(dp) * rinv);
        const u32 tp = pack2(bflo(np) * bflo(cbp[i]), bfhi(np) * bfhi(cbp[i]));
        const u32 hp = pack2(bflo(tp) + bflo(sbp[i]), bfhi(tp) + bfhi(sbp[i]));
        if (i < 4) *(LAS u32*)(L + L_H2 + w * ACT_STRIDE + (lane + 64 * i) * 4) = hp;
        else if (lane < 32) *(LAS u32*)(L + L_H2 + w * ACT_STRIDE + (256 + lane) * 4) = hp;
      }
      if (w != 0) STAMP(18);
    }
    if (w == 0) { WAIT_VM0(); F[F_LANDED] = nfin; }
    BARRIER();
    if (w != 0) STAMP(11);
    if (w == 7) {
      BARRIER();
      BARRIER();
      const int off = WS_FP + (lane >> 3) * 9216 + ((f >> 3) * 4 + (f & 3)) * 128 + (lane & 7) * 16 + ((f >> 2) & 1) * 8;
      u32x2 e[18]; u32 spins = 0;
      u32 slo = 0x00003f80u, shi = 0x3f800000u;
      asm volatile("" : "+v"(slo), "+v"(shi));
      if (DELAY_E) __builtin_amdgcn_s_sleep(DELAY_E);
      for (;;) {
        MEMBAR();
#pragma unroll
        for (int i = 0; i < 18; ++i) e[i] = ld64(rs, off + i * 8 * 9216);
        u32 mx = 0;
#pragma unroll
        for (int i = 0; i < 18; ++i) mx = umax(mx, umax(e[i].x, e[i].y));
        if (__all(mx != SENT) || !spin_ok(F, spins)) break;
      }
      STAMP(23);
      f32x4 s = {0, 0, 0, 0}, s1 = {0, 0, 0, 0};
#pragma unroll
      for (int i = 0; i < 18; i += 2) {
        s[0] = add_sel(e[i].x, slo, s[0]); s[1] = add_sel(e[i].x, shi, s[1]); s[2] = add_sel(e[i].y, slo, s[2]); s[3] = add_sel(e[i].y, shi, s[3]);
        s1[0] = add_sel(e[i + 1].x, slo, s1[0]); s1[1] = add_sel(e[i + 1].x, shi, s1[1]); s1[2] = add_sel(e[i + 1].y, slo, s1[2]); s1[3] = add_sel(e[i + 1].y, shi, s1[3]);
      }
      s += s1;
      const u32 dA = pack2(sum_x8_16_32(s[0]), sum_x8_16_32(s[1])), dB = pack2(sum_x8_16_32(s[2]), sum_x8_16_32(s[3]));
      const u32 tA = pack2(bflo(pre4a.x) * bflo(dA), bfhi(pre4a.x) * bfhi(dA)), tB = pack2(bflo(pre4a.y) * bflo(dB), bfhi(pre4a.y) * bfhi(dB));
      if (lane < 8) *(GAS u32x2*)(p.out + lane * DM + f * 4) = u32x2{pack2(bflo(x1p.x) + bflo(tA), bfhi(x1p.x) + bfhi(tA)), pack2(bflo(x1p.y) + bflo(tB), bfhi(x1p.y) + bfhi(tB))};
      STAMP(13);
    } else {
      const int cw = w - 1;
      if (w >= 1) {
        const int kt = cw >> 1;
        const u32 hb = L_H2 + (lane & 7) * ACT_STRIDE + kt * 384 + g * 16;
        u32x4 bfr[6];
#pragma unroll
        for (int s = 0; s < 6; ++s) bfr[s] = lds128(L, hb + s * 64);
        f32x4 a0 = {0, 0, 0, 0}, a1 = {0, 0, 0, 0}, a2 = {0, 0, 0, 0}, a3 = {0, 0, 0, 0};
#pragma unroll
        for (int s = 0; s < 6; s += 2) { a0 = mfma(wf[2 * s], bfr[s], a0); a1 = mfma(wf[2 * s + 1], bfr[s], a1); a2 = mfma(wf[2 * s + 2], bfr[s + 1], a2); a3 = mfma(wf[2 * s + 3], bfr[s + 1], a3); }
        a0 += a2; a1 += a3;
        if (r16 < 8) { *(LAS f32x4*)(L + L_R2 + cw * 1024 + (g * 8 + r16) * 16) = a0; *(LAS f32x4*)(L + L_R2 + cw * 1024 + 512 + (g * 8 + r16) * 16) = a1; }
        if (DEFER_DOWN) {
          lds_wait_ge(F, F_LANDED, n0 + 36u);
          const u32 d0 = L_RING + posd * 16384 + (8 * g + qp) * 1152 + cw * 192 + 8 * pp;
#pragma unroll
          for (int t = 0; t < 6; ++t) wf[12 + t] = tr_frag(L, d0 + t * 32, 4 * 1152);
        }
      }
      if (w != 0) STAMP(19);
      BARRIER();
      if (w != 0) STAMP(20);
      if (w < 4) {
        const int ct = w >> 1, e = (w & 1) * 64 + lane;
        const u32 a = L_R2 + ct * 512 + e * 4;
        const float gg = rb(*(LAS float*)(L + a) + *(LAS float*)(L + a + 2048) + *(LAS float*)(L + a + 4096));
        const float uu = rb(*(LAS float*)(L + a + 1024) + *(LAS float*)(L + a + 3072) + *(LAS float*)(L + a + 5120));
        const float inner = 0.7978845608028654f * (gg + 0.044715f * gg * gg * gg);
        const float th = 1.0f - 2.0f * __builtin_amdgcn_rcpf(1.0f + __expf(2.0f * inner));
        const float ge = rb(0.5f * gg * (1.0f + th));
        const int ls = e >> 2, rr = e & 3, b_ = ls & 7, g_ = ls >> 3;
        *(LAS u16*)(L + L_HH + b_ * 80 + (ct * 16 + g_ * 4 + rr) * 2) = (u16)f2bf(ge * uu);
      }
      if (w != 0) STAMP(21);
      BARRIER();
      if (w != 0) STAMP(22);
      if (w >= 1) {
        const u32x4 bfr = lds128(L, L_HH + (lane & 7) * 80 + g * 16);
        f32x4 a[6];
#pragma unroll
        for (int t = 0; t < 6; ++t) a[t] = mfma(wf[12 + t], bfr, f32x4{0, 0, 0, 0});
#pragma unroll
        for (int t = 0; t < 6; t += 2)
          if (r16 < 8) st128(rs, WS_FP + f * 9216 + ((cw * 3 + (t >> 1)) * 4 + g) * 128 + r16 * 16,
                             u32x4{nosent(pack2(a[t][0], a[t][1])), nosent(pack2(a[t][2], a[t][3])), nosent(pack2(a[t + 1][0], a[t + 1][1])), nosent(pack2(a[t + 1][2], a[t + 1][3]))});
      }
      if (w != 0) STAMP(12);
    }
  }
}

}

extern "C" void kernel_launch(void* const* d_in, const int* in_sizes, int n_in, void* d_out, int out_size, void* d_ws, size_t ws_size,
                              hipStream_t stream) {
  (void)in_sizes; (void)n_in; (void)out_size; (void)ws_size;
  Params p;
  p.x = (const u16*)d_in[0];
  p.kc = (const unsigned char*)d_in[1];
  p.vc = (const unsigned char*)d_in[2];
  p.wq = (const unsigned char*)d_in[3];
  p.wo = (const unsigned char*)d_in[4];
  p.wg = (const unsigned char*)d_in[6];
  p.wu = (const unsigned char*)d_in[7];
  p.wd = (const unsigned char*)d_in[8];
  p.c = (const u16*)d_in[9];
  p.wmod = (const unsigned char*)d_in[10];
  p.out = (u16*)d_out;
  p.ws = (unsigned char*)d_ws;
  (void)hipFuncSetAttribute((const void*)fused_block, hipFuncAttributeMaxDynamicSharedMemorySize, LDS_BYTES);
  hipLaunchKernelGGL(fused_block, dim3(NCU), dim3(NT), LDS_BYTES, stream, p);
}
```

```cpp
#include <hip/hip_runtime.h>
#include <stdint.h>
#include <type_traits>
#pragma clang fp contract(off)

#define INFL 24
#define INFLW 320
#define EARLY_Q 16
#define EARLY_LVL 4
#define BASE_LVL 2
#define BOOST1 2
#define BOOST2 4
#define SCHED 1
#define SCHED_TE 7750
#define SCHED_TAU 876
#define SCHED_Q0 17
#define SCHED_L1 30
#define SCHED_L2 70
#define SCHED_L3 50
#define SLOW1 2
#define SCHED_LA 10
#define GROUPW 1
#define STAGE2 0
#define DEFER_DOWN 1
#define SAMP_LAG 6
#define GLOBAL_T0 1
#define ADAPT 0
#define SHIFT_O 1
#define SHIFT_H 2
#define DELAY_A 12
#define DELAY_B 14
#define DELAY_W 36
#define DELAY_C 16
#define DELAY_E 24
#define WPPOLL_SLEEP 0
#define W2W 24
#define P1W 12
#define WOW 16
#define WQW 16
#define QGATE 1000
#define HGATE 800
#define HPOLL_SLEEP 2
#define QPOLL_SLEEP 2
#define AUX_KV "nt"
#define AUX_W "nt"
#define STAMPS 0

#define LAS __attribute__((address_space(3)))
#define GAS __attribute__((address_space(1)))
typedef unsigned u32;
typedef unsigned long long u64;
typedef unsigned short u16;
typedef u32 u32x4 __attribute__((ext_vector_type(4)));
typedef u32 u32x2 __attribute__((ext_vector_type(2)));
typedef float f32x4 __attribute__((ext_vector_type(4)));
typedef __bf16 bf16x8 __attribute__((ext_vector_type(8)));
typedef short s16x4 __attribute__((ext_vector_type(4)));

namespace {

constexpr int NB = 8, DM = 576, HKV = 16, SK = 8192, DH = 128, DF = 4608;
constexpr int NCU = 256, NT = 512;

constexpr int REP_H = 8;
constexpr int WS_CTRL = 0;
constexpr int WS_ALIVE = 4096;
constexpr int WS_ST = 8192;
constexpr int WS_MODT = WS_ST + 256 * 128;
constexpr int WS_X1 = WS_MODT + 36864;
constexpr int WS_H = WS_X1 + 8 * 9216;
constexpr int WS_Q = WS_H + REP_H * 18432;
constexpr int WS_OP = WS_Q + 131072;
constexpr int WS_WP = WS_OP + 262144;
constexpr int WS_FP = WS_WP + 147456;
constexpr int WS_SAMP = WS_FP + 144 * 9216;
constexpr int WS_T0 = WS_SAMP + 3 * 32 * 128;
constexpr int WS_END = WS_T0 + 8 * 128;
constexpr int WS_DBG = 4 << 20;
static_assert(WS_END <= WS_DBG, "ws");

constexpr int MAXSL = 64 + (SHIFT_O > SHIFT_H ? SHIFT_O : SHIFT_H);
constexpr int L_RING = 0;
constexpr int L_SC = 131072;
constexpr int ACT_STRIDE = 1168;
constexpr int L_P2 = L_SC + 8192;
constexpr int L_H2 = L_SC;
constexpr int L_R2 = L_SC + 9344;
constexpr int L_HH = L_R2 + 6144;
constexpr int L_ACT = L_SC + (MAXSL * 256 > 16384 ? MAXSL * 256 : 16384);
constexpr int L_QS = L_ACT + 9344;
constexpr int L_RED = L_QS + 512;
constexpr int L_STAT = L_RED + 3072;
constexpr int L_MX = L_STAT + 64;
constexpr int L_LW = L_MX + 64;
constexpr int L_FLG = L_LW + 64;
constexpr int LDS_BYTES = 163840;
static_assert(L_FLG + 256 <= LDS_BYTES && L_HH + 640 <= L_ACT, "lds");
constexpr int F_LANDED = 0, F_FREE = 1  , F_WREL = 9  , F_SC = 12, F_P1 = 13, F_INIT = 14, F_Q = 15, F_ABORT = 16, F_ALIVE = 17,
              F_P2 = 18, F_MX = 19, F_L = 20, F_X1 = 21, F_TAGOK = 22, F_TAG = 23, F_OPART = 24, F_CB0 = 25, F_EOWN = 26, F_EBAR = 27, F_T0 = 28, F_OPUB = 29;

constexpr u32 SENT = 0xFFFFFFFFu;
constexpr u32 LDS_SPIN_CAP = 1u << 21;
constexpr u32 GLB_SPIN_CAP = 1u << 17;
constexpr float LOG2E = 1.4426950408889634f;

struct Params {
  const u16* x; const unsigned char* kc; const unsigned char* vc; const unsigned char* wq; const unsigned char* wo;
  const unsigned char* wg; const unsigned char* wu; const unsigned char* wd; const u16* c; const unsigned char* wmod;
  u16* out; unsigned char* ws;
};

__device__ __forceinline__ float bf2f(u32 v16) { return __uint_as_float(v16 << 16); }
__device__ __forceinline__ float bflo(u32 d) { return __uint_as_float(d << 16); }
__device__ __forceinline__ float bfhi(u32 d) { return __uint_as_float(d & 0xffff0000u); }
__device__ __forceinline__ float rb(float f) { return (float)(__bf16)f; }
__device__ __forceinline__ u32 f2bf(float f) { return (u32)__builtin_bit_cast(u16, (__bf16)f); }
typedef float f32x2 __attribute__((ext_vector_type(2)));
typedef __bf16 bf16x2 __attribute__((ext_vector_type(2)));
__device__ __forceinline__ u32 pack2(float lo, float hi) { return __builtin_bit_cast(u32, __builtin_convertvector(f32x2{lo, hi}, bf16x2)); }
__device__ __forceinline__ u32 nosent(u32 p) { return p < 0xFFFEFFFFu ? p : 0xFFFEFFFFu; }
__device__ __forceinline__ u32 umax(u32 a, u32 b) { return a > b ? a : b; }
__device__ __forceinline__ u32 umax4(u32x4 v) { return umax(umax(v.x, v.y), umax(v.z, v.w)); }
typedef __bf16 bf16x2_ __attribute__((ext_vector_type(2)));
__device__ __forceinline__ float add_sel(u32 v, u32 sel, float acc) { return __builtin_amdgcn_fdot2_f32_bf16(__builtin_bit_cast(bf16x2_, v), __builtin_bit_cast(bf16x2_, sel), acc, false); }
__device__ __forceinline__ u32 vld(float f) { return __float_as_uint(f) | 1u; }
__device__ __forceinline__ f32x4 mfma(u32x4 a, u32x4 b, f32x4 c) {
  return __builtin_amdgcn_mfma_f32_16x16x32_bf16(__builtin_bit_cast(bf16x8, a), __builtin_bit_cast(bf16x8, b), c, 0, 0, 0);
}
__device__ __forceinline__ u32x4 mk_shift(u32x4 s, u32 by) {
  const u64 a = (((u64)s.y << 32) | s.x) + by;
  return u32x4{(u32)a, (u32)(a >> 32), s.z - by, s.w};
}
__device__ __forceinline__ u32x4 mk_srd(const void* p, u32 bytes) {
  const u64 a = (u64)p;
  u32x4 s;
  s.x = __builtin_amdgcn_readfirstlane((u32)a);
  s.y = __builtin_amdgcn_readfirstlane((u32)(a >> 32) & 0xffffu);
  s.z = __builtin_amdgcn_readfirstlane(bytes);
  s.w = 0x00020000u;
  return s;
}
#define MEMBAR() asm volatile("" ::: "memory")
#define WAIT_LGKM0() asm volatile("s_waitcnt lgkmcnt(0)" ::: "memory")
#define WAIT_VM0() asm volatile("s_waitcnt vmcnt(0)" ::: "memory")
#define BARRIER() do { asm volatile("s_waitcnt lgkmcnt(0)" ::: "memory"); __builtin_amdgcn_s_barrier(); asm volatile("" ::: "memory"); } while (0)

#define DMA1(AUX, srd, voff, soff, ldsaddr)                                                                      \
  do { u32 keep_;                                                                                                \
    asm volatile("s_mov_b32 %0, m0\n\ts_mov_b32 m0, %1\n\ts_nop 0\n\t"                                           \
                 "buffer_load_dwordx4 %4, %2, %3 offen " AUX " lds\n\ts_mov_b32 m0, %0"                          \
                 : "=&s"(keep_) : "s"(ldsaddr), "s"(srd), "s"(soff), "v"(voff) : "memory"); } while (0)
#define DMA4(AUX, srd, v0, v1, v2, v3, soff, ldsaddr)                                                            \
  do { u32 keep_;                                                                                                \
    asm volatile("s_mov_b32 %0, m0\n\ts_mov_b32 m0, %1\n\ts_nop 0\n\t"                                           \
                 "buffer_load_dwordx4 %4, %2, %3 offen " AUX " lds\n\t"                                          \
                 "buffer_load_dwordx4 %5, %2, %3 offen offset:1024 " AUX " lds\n\t"                              \
                 "buffer_load_dwordx4 %6, %2, %3 offen offset:2048 " AUX " lds\n\t"                              \
                 "buffer_load_dwordx4 %7, %2, %3 offen offset:3072 " AUX " lds\n\ts_mov_b32 m0, %0"              \
                 : "=&s"(keep_) : "s"(ldsaddr), "s"(srd), "s"(soff), "v"(v0), "v"(v1), "v"(v2), "v"(v3) : "memory"); } while (0)

#define DMA4W(AUX, srd, voff, so, step, ldsaddr)                                                                 \
  do { u32 keep_; const u32 d_ = (step) - 1024u, s0_ = (so), s1_ = s0_ + d_, s2_ = s1_ + d_, s3_ = s2_ + d_;     \
    asm volatile("s_mov_b32 %0, m0\n\ts_mov_b32 m0, %1\n\ts_nop 0\n\t"                                          \
                 "buffer_load_dwordx4 %7, %2, %3 offen " AUX " lds\n\t"                                          \
                 "buffer_load_dwordx4 %7, %2, %4 offen offset:1024 " AUX " lds\n\t"                              \
                 "buffer_load_dwordx4 %7, %2, %5 offen offset:2048 " AUX " lds\n\t"                              \
                 "buffer_load_dwordx4 %7, %2, %6 offen offset:3072 " AUX " lds\n\ts_mov_b32 m0, %0"              \
                 : "=&s"(keep_) : "s"(ldsaddr), "s"(srd), "s"(s0_), "s"(s1_), "s"(s2_), "s"(s3_), "v"(voff) : "memory"); } while (0)

template <int CTRL> __device__ __forceinline__ float dpp(float x) {
  return __builtin_bit_cast(float, __builtin_amdgcn_mov_dpp(__builtin_bit_cast(int, x), CTRL, 0xf, 0xf, true));
}
constexpr int ROR8 = 0x128, ROR4 = 0x124, ROR2 = 0x122, ROR1 = 0x121;
__device__ __forceinline__ float xrow_max(float x) {
  auto s = __builtin_amdgcn_permlane16_swap(__float_as_uint(x), __float_as_uint(x), false, false);
  x = fmaxf(__uint_as_float(s[0]), __uint_as_float(s[1]));
  auto t = __builtin_amdgcn_permlane32_swap(__float_as_uint(x), __float_as_uint(x), false, false);
  return fmaxf(__uint_as_float(t[0]), __uint_as_float(t[1]));
}
__device__ __forceinline__ float xrow_sum(float x) {
  auto s = __builtin_amdgcn_permlane16_swap(__float_as_uint(x), __float_as_uint(x), false, false);
  x = __uint_as_float(s[0]) + __uint_as_float(s[1]);
  auto t = __builtin_amdgcn_permlane32_swap(__float_as_uint(x), __float_as_uint(x), false, false);
  return __uint_as_float(t[0]) + __uint_as_float(t[1]);
}
__device__ __forceinline__ float wave_max(float x) {
  x = fmaxf(x, dpp<ROR8>(x)); x = fmaxf(x, dpp<ROR4>(x)); x = fmaxf(x, dpp<ROR2>(x)); x = fmaxf(x, dpp<ROR1>(x));
  return xrow_max(x);
}
__device__ __forceinline__ float wave_sum(float x) {
  x += dpp<ROR8>(x); x += dpp<ROR4>(x); x += dpp<ROR2>(x); x += dpp<ROR1>(x);
  return xrow_sum(x);
}
__device__ __forceinline__ float sum_x8_16_32(float x) {
  x += dpp<ROR8>(x);
  return xrow_sum(x);
}

typedef volatile LAS u32* flagp;
__device__ __forceinline__ void lds_wait_ge(flagp F, int idx, u32 need) {
  (void)F;
  const u32 a = L_FLG + idx * 4, ab = L_FLG + F_ABORT * 4;
  u32 v, z, cnt;
  asm volatile("s_mov_b32 %2, 0\n"
               "1:\n\tds_read_b32 %0, %3\n\tds_read_b32 %1, %4\n\ts_waitcnt lgkmcnt(0)\n\t"
               "v_cmp_le_u32 vcc, %5, %0\n\ts_cbranch_vccnz 2f\n\t"
               "v_cmp_ne_u32 vcc, 0, %1\n\ts_cbranch_vccnz 2f\n\t"
               "s_sleep 1\n\ts_add_u32 %2, %2, 1\n\ts_cmp_lt_u32 %2, %6\n\ts_cbranch_scc1 1b\n\t"
               "v_mov_b32 %0, 1\n\tds_write_b32 %4, %0\n"
               "2:"
               : "=&v"(v), "=&v"(z), "=&s"(cnt) : "v"(a), "v"(ab), "v"(need), "s"(LDS_SPIN_CAP) : "vcc", "scc", "memory");
}
__device__ __forceinline__ void lds_add(int idx, u32 n) {
  const u32 a = L_FLG + idx * 4;
  asm volatile("s_waitcnt lgkmcnt(0)\n\ts_mov_b64 vcc, exec\n\ts_mov_b64 exec, 1\n\tds_add_u32 %0, %1\n\ts_mov_b64 exec, vcc"
               :: "v"(a), "v"(n) : "vcc", "memory");
}
__device__ __forceinline__ bool spin_ok(flagp F, u32& spins) {
  if (++spins > GLB_SPIN_CAP) { F[F_ABORT] = 2u; return false; }
  if ((spins & 255u) == 0u && F[F_ABORT] != 0u) return false;
  return true;
}

constexpr int SC1 = 16;
typedef __amdgpu_buffer_rsrc_t rsrc_t;
__device__ __forceinline__ u32x4 ld128(rsrc_t r, int off) { return __builtin_amdgcn_raw_buffer_load_b128(r, off, 0, SC1); }
__device__ __forceinline__ u32x2 ld64(rsrc_t r, int off) { return __builtin_amdgcn_raw_buffer_load_b64(r, off, 0, SC1); }
__device__ __forceinline__ u32 ld32(rsrc_t r, int off) { return __builtin_amdgcn_raw_buffer_load_b32(r, off, 0, SC1); }
__device__ __forceinline__ void st128(rsrc_t r, int off, u32x4 v) { __builtin_amdgcn_raw_buffer_store_b128(v, r, off, 0, SC1); }
__device__ __forceinline__ void st64(rsrc_t r, int off, u32x2 v) { __builtin_amdgcn_raw_buffer_store_b64(v, r, off, 0, SC1); }

__device__ __forceinline__ u32x4 tr_frag(LAS unsigned char* L, u32 addr, u32 stride4) {
  const s16x4 a = __builtin_amdgcn_ds_read_tr16_b64_v4i16((LAS s16x4*)(L + addr));
  const s16x4 b = __builtin_amdgcn_ds_read_tr16_b64_v4i16((LAS s16x4*)(L + addr + stride4));
  const u32x2 a2 = __builtin_bit_cast(u32x2, a), b2 = __builtin_bit_cast(u32x2, b);
  return u32x4{a2.x, a2.y, b2.x, b2.y};
}
__device__ __forceinline__ u32x4 lds128(LAS unsigned char* L, u32 addr) { return *(LAS u32x4*)(L + addr); }

#define STAMP(i) do {} while (0)

__device__ __forceinline__ void ln_stats(const float (&v)[10], bool tail, float& mean_r, float& sd_r) {
  float s = 0.f;
#pragma unroll
  for (int i = 0; i < 8; ++i) s += v[i];
  if (tail) s += v[8] + v[9];
  const float mean = wave_sum(s) * (1.0f / DM);
  float q = 0.f;
#pragma unroll
  for (int i = 0; i < 8; ++i) { const float d = v[i] - mean; q += d * d; }
  if (tail) { const float d0 = v[8] - mean, d1 = v[9] - mean; q += d0 * d0 + d1 * d1; }
  const float var = wave_sum(q) * (1.0f / DM);
  mean_r = rb(mean);
  sd_r = rb(sqrtf(rb(rb(var) + 1e-5f)));
}
__device__ __forceinline__ float ln_mod(float x, float mean_r, float sd_r, float scale, float shift) {
  const float n = rb(rb(x - mean_r) / sd_r);
  return rb(rb(n * rb(1.0f + scale)) + shift);
}

__global__ void __launch_bounds__(NT, 2) fused_block(Params p) {
  extern __shared__ __attribute__((aligned(16))) unsigned char smem[];
  LAS unsigned char* L = (LAS unsigned char*)smem;
  flagp F = (flagp)(L + L_FLG);
  const int tid = threadIdx.x, lane = tid & 63, w = __builtin_amdgcn_readfirstlane(tid >> 6);
  STAMP(0);
  const u32 t_start = (u32)__builtin_amdgcn_s_memrealtime();
  const int c = blockIdx.x, xg = c & 7, ci = c >> 3;
  const int hkv = 2 * xg + (ci >> 4), cj = ci & 15, bb = cj >> 1, half = cj & 1;
  const int pj = ci >> 1;
  const bool isF = half == 0 || pj >= 14, isO = !isF && pj < 9;
  const int hu = xg * 5 + (pj - 9);
  const bool isH = !isF && !isO && hu < 36;
  const int f = xg * 18 + (half == 0 ? pj : pj + 2);
  const int u = pj;
  const int mu = xg * 9 + u;
  const bool isQ = isO && u < 8;
  const int shift = pj < 9 ? (pj < 8 ? SHIFT_O : SHIFT_O / 2) : pj < 14 ? (xg * 5 + (pj - 9) < 36 ? SHIFT_H : 0) : 0;
  const int nsl = half == 0 ? 64 + shift : 64 - shift;
  const int key0 = half == 0 ? 0 : (64 + shift) * 64;
  const u32 Wtot = isF ? (DEFER_DOWN == 2 ? 0u : DEFER_DOWN ? 72u : 108u) : isO ? 136u : isH ? 108u : 0u;
  const int roff = isF ? (DEFER_DOWN == 2 ? (8 - ((2 * nsl) & 7)) & 7 : DEFER_DOWN ? (shift == 1 ? 6 : 5) : 7) : isH ? 7 : 0;
  const int posd = (2 * nsl + roff) & 7;
  const int g = lane >> 4, r16 = lane & 15, qp = (lane & 15) >> 2, pp = lane & 3;
  const rsrc_t rs = __builtin_amdgcn_make_buffer_rsrc((void*)p.ws, 0, WS_END, 0x00020000);

  u32x4 wf[18];
  u32 cbp[5], sbp[5];
  u32x4 pre4a, pre4b;
  float osA = 0.f, osB = 0.f;
  u32 nfin = 0;

  auto load_cbsb = [&](int row, u32 (&cb)[5], u32 (&sb)[5]) {
    u32 spins = 0;
    for (;;) {
      MEMBAR();
      const int base = WS_MODT + row * 4608;
#pragma unroll
      for (int i = 0; i < 4; ++i) { sb[i] = ld32(rs, base + (288 + lane + 64 * i) * 4); cb[i] = ld32(rs, base + (576 + lane + 64 * i) * 4); }
      sb[4] = ld32(rs, base + (288 + 256 + (lane & 31)) * 4); cb[4] = ld32(rs, base + (576 + 256 + (lane & 31)) * 4);
      u32 mx = 0;
#pragma unroll
      for (int i = 0; i < 5; ++i) mx = umax(mx, umax(sb[i], cb[i]));
      if (__all(mx != SENT) || !spin_ok(F, spins)) break;
    }
#pragma unroll
    for (int i = 0; i < 5; ++i) cb[i] = pack2(1.0f + bflo(cb[i]), 1.0f + bfhi(cb[i]));
  };
  auto get_tag = [&]() { lds_wait_ge(F, F_TAGOK, 1u); return (u32)F[F_TAG]; };
  auto q_partial = [&](int kt, f32x4 (&acc)[4]) {
    lds_wait_ge(F, F_LANDED, 72u);
    u32x4 pq[24];
    const u32 a0 = L_RING + (kt * 192 + 8 * g + qp) * 128 + 8 * pp;
#pragma unroll
    for (int s = 0; s < 6; ++s)
#pragma unroll
      for (int ct = 0; ct < 4; ++ct) pq[s * 4 + ct] = tr_frag(L, a0 + s * 4096 + ct * 32, 4 * 128);
    lds_add(F_WREL + 0, 1);
    const u32 tag = get_tag();
    STAMP(6);
    u32x4 hv[6];
    u32 spins = 0;
    const int hb = WS_H + xg * 18432 + kt * 6144 + lane * 16;
    int lo[6];
#pragma unroll
    for (int i = 0; i < 6; ++i) { const int e = i * 64 + lane; lo[i] = L_ACT + ((e >> 2) & 7) * ACT_STRIDE + kt * 384 + ((e >> 5) * 4 + (e & 3)) * 8; }
    if (HGATE) { while ((u32)__builtin_amdgcn_s_memrealtime() - t_start < (u32)HGATE) __builtin_amdgcn_s_sleep(2); }
    for (;;) {
      MEMBAR();
#pragma unroll
      for (int i = 0; i < 6; ++i) hv[i] = ld128(rs, hb + i * 1024);
      u32 bad = 0;
#pragma unroll
      for (int i = 0; i < 6; ++i) bad |= (hv[i].y ^ tag) | (hv[i].w ^ tag);
      if (__all(bad == 0u) || !spin_ok(F, spins)) break;
      __builtin_amdgcn_s_sleep(HPOLL_SLEEP);
    }
#pragma unroll
    for (int i = 0; i < 6; ++i) *(LAS u32x2*)(L + lo[i]) = u32x2{hv[i].x, hv[i].z};
    WAIT_LGKM0();
    STAMP(3);
    const u32 ab = L_ACT + (lane & 7) * ACT_STRIDE + kt * 384 + g * 16;
#pragma unroll
    for (int ct = 0; ct < 4; ++ct) acc[ct] = f32x4{0, 0, 0, 0};
#pragma unroll
    for (int s = 0; s < 6; ++s) {
      const u32x4 bfr = lds128(L, ab + s * 64);
#pragma unroll
      for (int ct = 0; ct < 4; ++ct) acc[ct] = mfma(pq[s * 4 + ct], bfr, acc[ct]);
    }
  };

  if (w == 0) {
    const u32 t_entry = t_start;
    __builtin_amdgcn_s_setprio(3);
    if (lane < 32) F[lane] = 0u;
    u32 n = 0;
    u32x4 srd0; u32 v0, step0, pre;
    if (isF) { srd0 = mk_srd(p.wg + f * 64, DM * 9216 - f * 64); v0 = (lane >> 2) * 9216 + (lane & 3) * 16; step0 = 16 * 9216; pre = DEFER_DOWN == 2 ? 0 : 16; }
    else if (isQ) { srd0 = mk_srd(p.wq + (xg * 512 + u * 64) * 2, DM * 8192 - (xg * 512 + u * 64) * 2); v0 = (lane >> 3) * 8192 + (lane & 7) * 16; step0 = 8 * 8192; pre = WQW - 4; }
    else if (isH) { srd0 = mk_srd(p.wmod + hu * 32, DM * 6912 - hu * 32); v0 = (lane >> 1) * 6912 + (lane & 1) * 16; step0 = 32 * 6912; pre = 8; }
    else { srd0 = mk_srd(p.wq, 0); v0 = 0; step0 = 1024; pre = 0; }
    STAMP(24);
#pragma unroll 1
    for (; n < pre; n += 4) DMA4W(AUX_W, srd0, v0, n * step0, step0, L_RING + n * 1024);
    STAMP(25);
    BARRIER();
    STAMP(4);
    int pub = 0;
    auto run = [&](auto inflp, const u32x4& srd, u32 voff, u32 step, u32 cnt, u32 lds0, u32 first = 0) {
      constexpr int NP = decltype(inflp)::value;
      constexpr int KEEP = NP >= 8 ? NP - 4 : NP / 2;
      u32 s = first;
#pragma unroll 1
      for (; s + 4 <= cnt; s += 4) {
        DMA4W(AUX_W, srd, voff, s * step, step, lds0 + s * 1024);
        n += 4;
        asm volatile("s_waitcnt vmcnt(%0)" :: "i"(KEEP) : "memory");
        pub = max(pub, (int)n - KEEP); F[F_LANDED] = (u32)pub;
      }
#pragma unroll 1
      for (; s < cnt; ++s) { DMA1(AUX_W, srd, voff, s * step, lds0 + s * 1024); ++n; }
    };
    typedef std::integral_constant<int, INFLW / 16> P16;
    typedef std::integral_constant<int, (INFLW / 8 > 32 ? 32 : INFLW / 8)> P8;
    if (isF) {
      if (DEFER_DOWN != 2) {
      const u32 vg = (lane >> 2) * 9216 + (lane & 3) * 16;
      run(P16(), srd0, v0, step0, 36, L_RING, pre);
      run(P16(), mk_srd(p.wu + f * 64, DM * 9216 - f * 64), vg, 16 * 9216, 36, L_RING + 36 * 1024);
      if (!DEFER_DOWN) run(P8(), mk_srd(p.wd + f * 36864, 36864), (u32)(lane * 16), 1024, 36, L_RING + 72 * 1024);
      }
    } else if (isO) {
      if (isQ) run(std::integral_constant<int, WQW>(), srd0, v0, step0, 72, L_RING, pre);
      else n = 72;
      run(std::integral_constant<int, WOW>(), mk_srd(p.wo + xg * 512 * 1152 + u * 128, 512 * 1152 - u * 128), (lane >> 3) * 1152 + (lane & 7) * 16, 8 * 1152, 64, L_RING + 72 * 1024);
    } else if (isH) {
      const u32 vm32 = (lane >> 1) * 6912 + (lane & 1) * 16;
      run(std::integral_constant<int, P1W>(), srd0, v0, step0, 18, L_RING, pre);
      run(std::integral_constant<int, P1W>(), mk_srd(p.wmod + 1152 + hu * 32, DM * 6912 - 1152 - hu * 32), vm32, 32 * 6912, 18, L_RING + 18 * 1024);
      run(P8(), mk_srd(p.wmod + 2304 + hu * 128, DM * 6912 - 2304 - hu * 128), (lane >> 3) * 6912 + (lane & 7) * 16, 8 * 6912, 72, L_RING + 36 * 1024);
    }
    STAMP(1);
    const size_t kvoff = ((size_t)(bb * HKV + hkv) * SK + (size_t)key0) * 256;
    const u32x4 srd_k = mk_srd(p.kc + kvoff, (u32)nsl * 16384u), srd_v = mk_srd(p.vc + kvoff, (u32)nsl * 16384u);
    u32 vk[4], vv[2];
#pragma unroll
    for (int j = 0; j < 4; ++j) vk[j] = g * 256 + ((r16 ^ (4 * j + g)) << 4);
#pragma unroll
    for (int j = 0; j < 2; ++j) vv[j] = g * 256 + ((r16 ^ ((g | (j << 2)) << 1)) << 4);
    const u32 wneed = isF ? (DEFER_DOWN == 2 ? 0u : DEFER_DOWN ? 0x00022211u : 0x03332211u) : isO ? 0x33221111u : isH ? 0x02222211u : 0u;
    auto wait_window = [&](u32 N) {
      asm volatile("s_waitcnt vmcnt(28)\n\ts_cmp_ge_u32 %0, 28\n\ts_cbranch_scc1 9f\n\t"
                   "s_waitcnt vmcnt(24)\n\ts_cmp_ge_u32 %0, 24\n\ts_cbranch_scc1 9f\n\t"
                   "s_waitcnt vmcnt(20)\n\ts_cmp_ge_u32 %0, 20\n\ts_cbranch_scc1 9f\n\t"
                   "s_waitcnt vmcnt(16)\n9:" :: "s"(N) : "scc", "memory");
    };
    u32 fr_next = 0;
    u32 t_prev = t_entry, eb_next = 0, t_ref = t_entry;
    auto wait_free = [&](int pos, u32 need, u32 seen) {
      if (seen < need && F[F_FREE + pos] < need) { WAIT_VM0(); F[F_LANDED] = n; pub = (int)n; lds_wait_ge(F, F_FREE + pos, need); }
    };
#pragma unroll 1
    for (int q = 0; q < 2 * nsl; ++q) {
      const int pos = (q + roff) & 7;
      if (q >= 8) wait_free(pos, (u32)(q - 7), fr_next);
      else {
        const int nb = (wneed >> (4 * pos)) & 15;
#pragma unroll 1
        for (int i = 0; i < nb; ++i)
          if (F[F_WREL + i] < 7u) { WAIT_VM0(); F[F_LANDED] = n; pub = (int)n; lds_wait_ge(F, F_WREL + i, 7u); }
      }
      int lvl = q < EARLY_Q ? EARLY_LVL : BASE_LVL;
      if (q == SCHED_Q0) { const u32 t0_ = __builtin_amdgcn_readfirstlane((u32)F[F_T0]); if (t0_) t_ref = t0_; }
      if (q >= SCHED_Q0) {
        const u32 eown = t_prev + (u32)(((2 * nsl - q + 1) * SCHED_TAU) >> 4);
        F[F_EOWN] = eown;
        const u32 eb = __builtin_amdgcn_readfirstlane(eb_next);
        const int lag = eb ? (int)(eown - eb) - SCHED_LA : (int)(eown - t_ref) - SCHED_TE - SCHED_L1;
        if (lag > 0) lvl = BASE_LVL + BOOST1;
        if (lag > SCHED_L2) lvl = BASE_LVL + BOOST2;
        if (lag < -SCHED_L3) lvl = BASE_LVL - SLOW1;
      }
      eb_next = F[F_EBAR];
      t_prev = (u32)__builtin_amdgcn_s_memrealtime();
      const u32 N2 = 16u + 4u * (u32)((lvl + 1) >> 1), N = 16u + 4u * (u32)(lvl >> 1);
      fr_next = F[F_FREE + ((pos + 1) & 7)];
      const u32 lb = L_RING + pos * 16384;
      const bool isk = q < nsl;
      const u32x4 srd = isk ? srd_k : srd_v;
      const u32 v0_ = isk ? vk[0] : vv[0], v1_ = isk ? vk[1] : vv[0], v2_ = isk ? vk[2] : vv[1], v3_ = isk ? vk[3] : vv[1];
      const u32 so = (u32)(isk ? q : q - nsl) * 16384u;
#pragma unroll
      for (int gq = 0; gq < 4; ++gq) {
        DMA4(AUX_KV, srd, v0_, v1_, v2_, v3_, so + gq * 4096, lb + gq * 4096);
        n += 4;
        const u32 Ng = gq < 2 ? N2 : N;
        wait_window(Ng);
        pub = max(pub, (int)n - (int)Ng); F[F_LANDED] = (u32)pub;
      }
    }
    if (isF && DEFER_DOWN == 1) {
      STAMP(5);
      for (int i = 0; i < 3; ++i) wait_free(posd + i, (u32)(2 * nsl - 7 + i), 0u);
      run(P8(), mk_srd(p.wd + f * 36864, 36864), (u32)(lane * 16), 1024, 36, L_RING + posd * 16384);
    }
    if (isF && DEFER_DOWN == 2) {
      STAMP(5);
      const u32 vg = (lane >> 2) * 9216 + (lane & 3) * 16;
      const u32x4 srd_u = mk_srd(p.wu + f * 64, DM * 9216 - f * 64), srd_d = mk_srd(p.wd + f * 36864, 36864);
      const u32 fbase = (u32)(2 * nsl - 7);
      auto need_pos = [&](int s0, int s1) { for (int s_ = s0; s_ <= s1; ++s_) wait_free(s_, fbase + (u32)s_, 0u); };
      typedef std::integral_constant<int, W2W> PW;
      need_pos(0, 0); run(PW(), srd0, vg, 16 * 9216, 16, L_RING);
      need_pos(1, 2); run(PW(), mk_shift(srd0, 16 * 16 * 9216), vg, 16 * 9216, 20, L_RING + 16 * 1024);
      run(PW(), srd_u, vg, 16 * 9216, 12, L_RING + 36 * 1024);
      need_pos(3, 4); run(PW(), mk_shift(srd_u, 12 * 16 * 9216), vg, 16 * 9216, 24, L_RING + 48 * 1024);
      run(P8(), srd_d, (u32)(lane * 16), 1024, 8, L_RING + 72 * 1024);
      need_pos(5, 6); run(P8(), mk_shift(srd_d, 8 * 1024), (u32)(lane * 16), 1024, 28, L_RING + 80 * 1024);
    }
    if (!isF || !DEFER_DOWN) { WAIT_VM0(); F[F_LANDED] = n; }
    nfin = n;
    STAMP(3);
    __builtin_amdgcn_s_setprio(0);
    if (isF) {
      lds_wait_ge(F, F_CB0, 1u);
#pragma unroll
      for (int i = 0; i < 5; ++i) { cbp[i] = *(LAS u32*)(L + L_RED + i * 256 + lane * 4); sbp[i] = *(LAS u32*)(L + L_RED + 1280 + i * 256 + lane * 4); }
    }
  } else if (w == 7) {
    BARRIER();
    u64 epoch = *(const __attribute__((address_space(4))) u64*)(p.ws + WS_CTRL) + 1ull;
    asm volatile("" : "+s"(epoch));
    const u32 tag = (u32)epoch;
    F[F_TAG] = tag;
    WAIT_LGKM0();
    F[F_TAGOK] = 1u;
    if (GLOBAL_T0 && c == 0 && lane < 8) st64(rs, WS_T0 + lane * 128, u32x2{t_start, tag});
    const u32x4 z4 = {0u, 0u, 0u, 0u}, s4 = {SENT, SENT, SENT, SENT};
    if (isH) {
#pragma unroll
      for (int i = 0; i < 2; ++i) st64(rs, WS_MODT + (lane & 7) * 4608 + hu * 128 + (2 * i + (r16 >> 3)) * 32 + g * 8, u32x2{SENT, SENT});
    }
    st128(rs, WS_OP + c * 1024 + lane * 16, z4);
    if (lane == 0) st128(rs, WS_ST + c * 128, z4);
    if (isO) {
#pragma unroll
      for (int i = 0; i < 2; ++i) st128(rs, WS_WP + (xg * 144 + u * 16) * 128 + i * 1024 + lane * 16, z4);
      st128(rs, WS_X1 + (lane >> 3) * 9216 + mu * 128 + (lane & 7) * 16, s4);
    }
    if (isF) {
#pragma unroll 1
      for (int i = 0; i < 9; ++i) st128(rs, WS_FP + f * 9216 + i * 1024 + lane * 16, s4);
    }
    WAIT_VM0();
    F[F_INIT] = 1u;
    if (lane == 0) __hip_atomic_store((GAS u64*)(p.ws + WS_ALIVE) + xg * 32 + ci, epoch, __ATOMIC_RELAXED, __HIP_MEMORY_SCOPE_AGENT);
    STAMP(1);
    if (isQ) {
      f32x4 acc[4];
      q_partial(2, acc);
      lds_add(F_WREL + 1, 1); lds_add(F_WREL + 2, 1);
      lds_wait_ge(F, F_P2, 2u);
      const int ls = (g * 8 + (lane & 7)) * 16;
#pragma unroll
      for (int ct = 0; ct < 4; ++ct) {
        const f32x4 a = (*(LAS f32x4*)(L + L_P2 + ct * 512 + ls) + *(LAS f32x4*)(L + L_P2 + 2048 + ct * 512 + ls)) + acc[ct];
        if (r16 < 8) st128(rs, WS_Q + ((2 * xg + (u >> 2)) * 8 + r16) * 1024 + ((u & 3) * 16 + ct * 4 + g) * 16, u32x4{pack2(a[0], a[1]), tag, pack2(a[2], a[3]), tag});
      }
      STAMP(4);
    } else {
      lds_add(F_WREL + 0, 1); lds_add(F_WREL + 1, 1); lds_add(F_WREL + 2, 1);
    }
    {
      u32x4 qv; u32x2 t0v = {0u, 0u}; u32 spins = 0;
      if (QGATE) { while ((u32)__builtin_amdgcn_s_memrealtime() - t_start < (u32)QGATE) __builtin_amdgcn_s_sleep(2); }
      for (;;) {
        MEMBAR();
        qv = ld128(rs, WS_Q + (hkv * 8 + bb) * 1024 + lane * 16);
        if (GLOBAL_T0) t0v = ld64(rs, WS_T0 + xg * 128);
        if (__all(((qv.y ^ tag) | (qv.w ^ tag)) == 0u) || !spin_ok(F, spins)) break;
        __builtin_amdgcn_s_sleep(QPOLL_SLEEP);
      }
      *(LAS u32x2*)(L + L_QS + lane * 8) = u32x2{qv.x, qv.z};
      F[F_T0] = (GLOBAL_T0 && t0v.y == tag) ? t0v.x : t_start;
      WAIT_LGKM0();
      F[F_Q] = 1u;
    }
    STAMP(5);
    auto sample = [&](int r, int seq) {
    };
    sample(0, 26);
    lds_wait_ge(F, F_LANDED, Wtot + 16u * (u32)(nsl - 8));
    {
      const u32 ehi = (u32)(epoch >> 32);
      u32 spins = 0;
      for (;;) {
        MEMBAR();
        const u32x4 a = ld128(rs, WS_ALIVE + lane * 32), b2 = ld128(rs, WS_ALIVE + lane * 32 + 16);
        const u32 bad = (a.x ^ tag) | (a.y ^ ehi) | (a.z ^ tag) | (a.w ^ ehi) | (b2.x ^ tag) | (b2.y ^ ehi) | (b2.z ^ tag) | (b2.w ^ ehi);
        if (__all(bad == 0u) || !spin_ok(F, spins)) break;
        __builtin_amdgcn_s_sleep(64);
      }
    }
    F[F_ALIVE] = 1u;
    if (c == 0 && lane == 0) __hip_atomic_store((GAS u64*)(p.ws + WS_CTRL), epoch, __ATOMIC_RELAXED, __HIP_MEMORY_SCOPE_AGENT);
    {
      lds_wait_ge(F, F_L, 6u);
      const int hd = lane & 1;
      float M = *(LAS float*)(L + L_MX + hd * 4), Ls = *(LAS float*)(L + L_LW + hd * 4);
#pragma unroll
      for (int i = 1; i < 6; ++i) { M = fmaxf(M, *(LAS float*)(L + L_MX + i * 8 + hd * 4)); Ls += *(LAS float*)(L + L_LW + i * 8 + hd * 4); }
      if (lane < 2) st64(rs, WS_ST + c * 128 + hd * 8, u32x2{vld(M), vld(Ls)});
      u32x2 o; u32 spins = 0;
      for (;;) {
        MEMBAR();
        o = ld64(rs, WS_ST + (c ^ 8) * 128 + hd * 8);
        if (__all((o.x & o.y & 1u) != 0u) || !spin_ok(F, spins)) break;
        __builtin_amdgcn_s_sleep(8);
      }
      const float m0 = __uint_as_float(vld(M)), l0 = __uint_as_float(vld(Ls)), m1 = __uint_as_float(o.x), l1 = __uint_as_float(o.y);
      const float Mg = fmaxf(m0, m1);
      const float e0 = __builtin_amdgcn_exp2f((m0 - Mg) * LOG2E), e1 = __builtin_amdgcn_exp2f((m1 - Mg) * LOG2E);
      const float sc = e0 / (e0 * l0 + e1 * l1);
      osA = __builtin_bit_cast(float, __builtin_amdgcn_readlane(__builtin_bit_cast(int, sc), 0));
      osB = __builtin_bit_cast(float, __builtin_amdgcn_readlane(__builtin_bit_cast(int, sc), 1));
    }
    if (isF) {
      load_cbsb(0, cbp, sbp);
#pragma unroll
      for (int i = 0; i < 5; ++i) { *(LAS u32*)(L + L_RED + i * 256 + lane * 4) = cbp[i]; *(LAS u32*)(L + L_RED + 1280 + i * 256 + lane * 4) = sbp[i]; }
      WAIT_LGKM0();
      F[F_CB0] = 1u;
      load_cbsb(7, cbp, sbp);
      u32x2 gb; u32 spins = 0;
      for (;;) {
        MEMBAR();
        gb = ld64(rs, WS_MODT + (lane & 7) * 4608 + 864 * 4 + f * 8);
        if (__all(umax(gb.x, gb.y) != SENT) || !spin_ok(F, spins)) break;
      }
      pre4a = u32x4{gb.x, gb.y, 0u, 0u};
    } else if (isO) {
      u32 spins = 0;
      for (;;) {
        MEMBAR();
        pre4a = ld128(rs, WS_MODT + (lane & 7) * 4608 + mu * 16);
        if (__all(umax4(pre4a) != SENT) || !spin_ok(F, spins)) break;
      }
      pre4b = *(const GAS u32x4*)(p.x + (lane & 7) * DM + mu * 8);
    }
    STAMP(7);
    sample(1, nsl + 12);
    sample(2, nsl + 34);
  } else {
    const int cw = w - 1;
    if (isF && DEFER_DOWN != 2) {
      BARRIER();
      const int m = cw & 1, kt = cw >> 1;
      const u32 a0 = L_RING + 36 * m * 1024 + (kt * 192 + 8 * g + qp) * 64 + 8 * pp;
      if (m == 1) lds_add(F_WREL + 0, 1);
      lds_wait_ge(F, F_LANDED, m == 0 ? 36u : 72u);
#pragma unroll
      for (int s = 0; s < 6; ++s) { wf[2 * s] = tr_frag(L, a0 + s * 2048, 4 * 64); wf[2 * s + 1] = tr_frag(L, a0 + s * 2048 + 32, 4 * 64); }
      if (m == 0) lds_add(F_WREL + 0, 1);
      lds_add(F_WREL + 1, 1);
      if (!DEFER_DOWN) {
        lds_wait_ge(F, F_LANDED, 108u);
        const u32 d0 = L_RING + 72 * 1024 + (8 * g + qp) * 1152 + cw * 192 + 8 * pp;
#pragma unroll
        for (int t = 0; t < 6; ++t) wf[12 + t] = tr_frag(L, d0 + t * 32, 4 * 1152);
      }
      lds_add(F_WREL + 2, 1);
    } else if (isO) {
      BARRIER();
      if (cw < 4) {
        lds_add(F_WREL + 0, 1);
        const u32 a0 = L_RING + 72 * 1024 + (8 * g + qp) * 128 + cw * 32 + 8 * pp;
        lds_wait_ge(F, F_LANDED, 104u);
#pragma unroll
        for (int s = 0; s < 8; ++s) wf[s] = tr_frag(L, a0 + s * 4096, 4 * 128);
        lds_add(F_WREL + 1, 1);
        lds_wait_ge(F, F_LANDED, 136u);
#pragma unroll
        for (int s = 8; s < 16; ++s) wf[s] = tr_frag(L, a0 + s * 4096, 4 * 128);
        lds_add(F_WREL + 2, 1);
      } else {
        if (isQ) {
          f32x4 acc[4];
          q_partial(cw - 4, acc);
          if (r16 < 8) {
#pragma unroll
            for (int ct = 0; ct < 4; ++ct) *(LAS f32x4*)(L + L_P2 + (cw - 4) * 2048 + ct * 512 + (g * 8 + r16) * 16) = acc[ct];
          }
          lds_add(F_P2, 1);
        } else lds_add(F_WREL + 0, 1);
        lds_add(F_WREL + 1, 1); lds_add(F_WREL + 2, 1);
      }
    } else if (isH) {
      u32 cv[6], xa[5], xb[5];
#pragma unroll
      for (int i = 0; i < 6; ++i) cv[i] = ((const GAS u32*)p.c)[cw * 64 + lane + 384 * i];
      const int rowb = 6 + (cw & 1);
      const GAS u32* xr = (const GAS u32*)p.x + cw * 288;
      const GAS u32* xs = (const GAS u32*)p.x + rowb * 288;
#pragma unroll
      for (int i = 0; i < 4; ++i) { xa[i] = xr[lane + 64 * i]; xb[i] = xs[lane + 64 * i]; }
      xa[4] = xr[256 + (lane & 31)]; xb[4] = xs[256 + (lane & 31)];
      const u32x2 xq = *(const GAS u32x2*)(p.x + (lane & 7) * DM + hu * 16 + g * 4);
      BARRIER();
#pragma unroll
      for (int i = 0; i < 6; ++i) {
        const int e = cw * 64 + lane + 384 * i;
        const float a = bflo(cv[i]), b2 = bfhi(cv[i]);
        *(LAS u32*)(L + L_SC + (e / 288) * ACT_STRIDE + (e % 288) * 4) = pack2(a / (1.0f + __expf(-a)), b2 / (1.0f + __expf(-b2)));
      }
      {
        float v[10], mean_r, sd_r;
#pragma unroll
        for (int i = 0; i < 5; ++i) { v[2 * i] = bflo(xa[i]); v[2 * i + 1] = bfhi(xa[i]); }
        ln_stats(v, lane < 32, mean_r, sd_r);
        if (lane == 0) { *(LAS float*)(L + L_STAT + cw * 8) = mean_r; *(LAS float*)(L + L_STAT + cw * 8 + 4) = sd_r; }
        if (cw < 2) {
#pragma unroll
          for (int i = 0; i < 5; ++i) { v[2 * i] = bflo(xb[i]); v[2 * i + 1] = bfhi(xb[i]); }
          ln_stats(v, lane < 32, mean_r, sd_r);
          if (lane == 0) { *(LAS float*)(L + L_STAT + rowb * 8) = mean_r; *(LAS float*)(L + L_STAT + rowb * 8 + 4) = sd_r; }
        }
      }
      lds_add(F_SC, 1);
      STAMP(24);
      const u32 sb0 = L_SC + (lane & 7) * ACT_STRIDE + g * 16;
      {
        const int m = cw & 1, kt = cw >> 1;
        lds_wait_ge(F, F_SC, 6u);
        const float mean_r = *(LAS float*)(L + L_STAT + (lane & 7) * 8), sd_r = *(LAS float*)(L + L_STAT + (lane & 7) * 8 + 4);
        float n0 = rb(rb(bflo(xq.x) - mean_r) / sd_r), n1 = rb(rb(bfhi(xq.x) - mean_r) / sd_r);
        float n2 = rb(rb(bflo(xq.y) - mean_r) / sd_r), n3 = rb(rb(bfhi(xq.y) - mean_r) / sd_r);
        asm volatile("" : "+v"(n0), "+v"(n1), "+v"(n2), "+v"(n3));
        u32 tag = 0;
        if (cw == 5) tag = get_tag();
        u32x4 bfr[6];
#pragma unroll
        for (int s = 0; s < 6; ++s) bfr[s] = lds128(L, sb0 + (kt * 6 + s) * 64);
        lds_wait_ge(F, F_LANDED, 36u);
        STAMP(25);
        const u32 a0 = L_RING + (m * 18 + kt * 6) * 1024 + (8 * g + qp) * 32 + 8 * pp;
        f32x4 acc = {0, 0, 0, 0}, acc2 = {0, 0, 0, 0};
#pragma unroll
        for (int s = 0; s < 6; s += 2) { acc = mfma(tr_frag(L, a0 + s * 1024, 4 * 32), bfr[s], acc); acc2 = mfma(tr_frag(L, a0 + (s + 1) * 1024, 4 * 32), bfr[s + 1], acc2); }
        acc += acc2;
        if (r16 < 8) *(LAS f32x4*)(L + L_RED + cw * 512 + (g * 8 + r16) * 16) = acc;
        lds_add(F_P1, 1);
        lds_add(F_WREL + 0, 1);
        if (cw == 5) {
          lds_wait_ge(F, F_P1, 6u);
          const int ls = (g * 8 + (lane & 7)) * 16;
          const f32x4 sa = *(LAS f32x4*)(L + L_RED + 0 * 512 + ls) + *(LAS f32x4*)(L + L_RED + 2 * 512 + ls) + *(LAS f32x4*)(L + L_RED + 4 * 512 + ls);
          const f32x4 ca = *(LAS f32x4*)(L + L_RED + 1 * 512 + ls) + *(LAS f32x4*)(L + L_RED + 3 * 512 + ls) + *(LAS f32x4*)(L + L_RED + 5 * 512 + ls);
          const float h0 = rb(rb(n0 * rb(1.0f + rb(ca[0]))) + rb(sa[0])), h1 = rb(rb(n1 * rb(1.0f + rb(ca[1]))) + rb(sa[1]));
          const float h2 = rb(rb(n2 * rb(1.0f + rb(ca[2]))) + rb(sa[2])), h3 = rb(rb(n3 * rb(1.0f + rb(ca[3]))) + rb(sa[3]));
          const u32x4 hu4 = {pack2(h0, h1), tag, pack2(h2, h3), tag};
#pragma unroll
          for (int i = 0; i < REP_H / 2; ++i)
            st128(rs, WS_H + (2 * i + (r16 >> 3)) * 18432 + hu * 512 + (lane & 7) * 64 + g * 16, hu4);
          STAMP(6);
        }
      }
      if (cw < 4) {
        lds_wait_ge(F, F_LANDED, 108u);
        const u32 a0 = L_RING + 36 * 1024 + (8 * g + qp) * 128 + cw * 32 + 8 * pp;
        f32x4 a0c = {0, 0, 0, 0}, a1c = {0, 0, 0, 0};
#pragma unroll 3
        for (int s = 0; s < 18; s += 2) {
          a0c = mfma(tr_frag(L, a0 + s * 4096, 4 * 128), lds128(L, sb0 + s * 64), a0c);
          a1c = mfma(tr_frag(L, a0 + (s + 1) * 4096, 4 * 128), lds128(L, sb0 + (s + 1) * 64), a1c);
        }
        const f32x4 a = a0c + a1c;
        lds_add(F_WREL + 1, 1);
        lds_wait_ge(F, F_INIT, 1u);
        if (r16 < 8) st64(rs, WS_MODT + r16 * 4608 + hu * 128 + cw * 32 + g * 8, u32x2{nosent(pack2(a[0], a[1])), nosent(pack2(a[2], a[3]))});
      } else lds_add(F_WREL + 1, 1);
      lds_add(F_WREL + 2, 1);
    } else {
      BARRIER();
      lds_add(F_WREL + 0, 1); lds_add(F_WREL + 1, 1); lds_add(F_WREL + 2, 1);
    }
    STAMP(1);
    u32x4 qf[4];
    u32 ka[4];
#pragma unroll
    for (int s = 0; s < 4; ++s) ka[s] = r16 * 256 + (((4 * s + g) ^ r16) << 4);
    float mx0 = -INFINITY, mx1 = -INFINITY;
    auto k_read = [&](int q, u32x4* kf) {
      const int pos = (q + roff) & 7;
      lds_wait_ge(F, F_LANDED, Wtot + 16u * (u32)(q + 1));
      const u32 sb = L_RING + pos * 16384;
#pragma unroll
      for (int t = 0; t < 4; ++t)
#pragma unroll
        for (int s = 0; s < 4; ++s) kf[t * 4 + s] = lds128(L, sb + ka[s] + t * 4096);
      WAIT_LGKM0();
      if (lane == 0) F[F_FREE + pos] = (u32)(q + 1);
    };
    auto k_scores = [&](int q, const u32x4* kf) {
      f32x4 acc[4];
#pragma unroll
      for (int t = 0; t < 4; ++t) {
        acc[t] = f32x4{0, 0, 0, 0};
#pragma unroll
        for (int s = 0; s < 4; ++s) acc[t] = mfma(qf[s], kf[t * 4 + s], acc[t]);
      }
      float s0 = g == 0 ? acc[0][0] : g == 1 ? acc[1][0] : g == 2 ? acc[2][0] : acc[3][0];
      float s1 = g == 0 ? acc[0][1] : g == 1 ? acc[1][1] : g == 2 ? acc[2][1] : acc[3][1];
      s0 = rb(rb(s0) * 0.08838834764831845f);
      s1 = rb(rb(s1) * 0.08838834764831845f);
      mx0 = fmaxf(mx0, s0); mx1 = fmaxf(mx1, s1);
      *(LAS u16*)(L + L_SC + q * 256 + lane * 2) = (u16)(__float_as_uint(s0) >> 16);
      *(LAS u16*)(L + L_SC + q * 256 + 128 + lane * 2) = (u16)(__float_as_uint(s1) >> 16);
    };
    int q0 = cw + 6;
    {
      u32x4 kf[16];
      k_read(cw, kf);
      const bool two = DEFER_DOWN == 2 && isF;
      if (two) k_read(cw + 6, wf);
      lds_wait_ge(F, F_Q, 1u);
      lds_wait_ge(F, F_WREL + 1, 7u); lds_wait_ge(F, F_WREL + 2, 7u);
      STAMP(2);
#pragma unroll
      for (int s = 0; s < 4; ++s) qf[s] = lds128(L, L_QS + (lane & 1) * 256 + s * 64 + g * 16);
      k_scores(cw, kf);
      if (two) { k_scores(cw + 6, wf); q0 = cw + 12; }
    }
#pragma unroll 1
    for (int q = q0; q < nsl; q += 6) {
      u32x4 kf[16];
      k_read(q, kf);
      k_scores(q, kf);
    }
    STAMP(3);
    {
      mx0 = wave_max(mx0); mx1 = wave_max(mx1);
      if (lane == 0) *(LAS u32x2*)(L + L_MX + cw * 8) = u32x2{__float_as_uint(mx0), __float_as_uint(mx1)};
      lds_add(F_MX, 1);
      lds_wait_ge(F, F_MX, 6u);
#pragma unroll
      for (int i = 0; i < 6; ++i) { mx0 = fmaxf(mx0, *(LAS float*)(L + L_MX + i * 8)); mx1 = fmaxf(mx1, *(LAS float*)(L + L_MX + i * 8 + 4)); }
      float l0 = 0.f, l1 = 0.f;
#pragma unroll 1
      for (int q = cw; q < nsl; q += 6) {
        const float s0 = bf2f(*(LAS u16*)(L + L_SC + q * 256 + lane * 2)), s1 = bf2f(*(LAS u16*)(L + L_SC + q * 256 + 128 + lane * 2));
        const float p0 = __builtin_amdgcn_exp2f((s0 - mx0) * LOG2E), p1 = __builtin_amdgcn_exp2f((s1 - mx1) * LOG2E);
        l0 += p0; l1 += p1;
        *(LAS u16*)(L + L_SC + q * 256 + lane * 2) = (u16)f2bf(p0);
        *(LAS u16*)(L + L_SC + q * 256 + 128 + lane * 2) = (u16)f2bf(p1);
      }
      l0 = wave_sum(l0); l1 = wave_sum(l1);
      if (lane == 0) *(LAS u32x2*)(L + L_LW + cw * 8) = u32x2{__float_as_uint(l0), __float_as_uint(l1)};
      lds_add(F_L, 1);
    }
    f32x4 o[8];
#pragma unroll
    for (int cb = 0; cb < 8; ++cb) o[cb] = f32x4{0, 0, 0, 0};
    const u32 va0 = (8 * g + qp) * 256 + ((pp >> 1) << 4) + 8 * (pp & 1) + (((qp | ((g & 1) << 2)) << 1) << 4);
#pragma unroll 1
    for (int q = cw; q < nsl; q += 6) {
      const int pos = (q + nsl + roff) & 7;
      if (isF && q >= 24 && q < 30) { lds_wait_ge(F, F_ALIVE, 1u); load_cbsb(w, cbp, sbp); }
      const u32x4 pf0 = lds128(L, L_SC + q * 256 + (lane & 1) * 128 + g * 16), pf1 = lds128(L, L_SC + q * 256 + (lane & 1) * 128 + 64 + g * 16);
      lds_wait_ge(F, F_LANDED, Wtot + 16u * (u32)(q + nsl + 1));
      const u32 sb = L_RING + pos * 16384 + va0;
      u32x4 vf[8], vg2[8];
#pragma unroll
      for (int cb = 0; cb < 8; ++cb) vf[cb] = tr_frag(L, sb ^ (cb << 5), 1024);
#pragma unroll
      for (int cb = 0; cb < 8; ++cb) vg2[cb] = tr_frag(L, (sb ^ (cb << 5)) + 8192, 1024);
      WAIT_LGKM0();
      if (lane == 0) F[F_FREE + pos] = (u32)(q + nsl + 1);
#pragma unroll
      for (int cb = 0; cb < 8; ++cb) o[cb] = mfma(pf0, vf[cb], o[cb]);
#pragma unroll
      for (int cb = 0; cb < 8; ++cb) o[cb] = mfma(pf1, vg2[cb], o[cb]);
    }
    STAMP(4);
    if (g == 0) {
#pragma unroll
      for (int cb = 0; cb < 8; ++cb) *(LAS u32x2*)(L + L_ACT + cw * 1024 + (cb * 16 + r16) * 8) = u32x2{__float_as_uint(o[cb][0]), __float_as_uint(o[cb][1])};
    }
    lds_add(F_OPART, 1);
  }
  if (w == 7) {
    lds_wait_ge(F, F_OPART, 6u);
    STAMP(8);
    f32x4 O = *(LAS f32x4*)(L + L_ACT + lane * 16);
#pragma unroll
    for (int i = 1; i < 6; ++i) O += *(LAS f32x4*)(L + L_ACT + i * 1024 + lane * 16);
    st128(rs, WS_OP + c * 1024 + lane * 16, u32x4{vld(O[0] * osA), vld(O[1] * osB), vld(O[2] * osA), vld(O[3] * osB)});
    F[F_OPUB] = 1u;
    STAMP(9);
  }
  if (isO) {
    constexpr int L_OS = L_SC;
    u32x4 ov[4];
    bool got[4] = {false, false, false, false};
    u32 spins = 0;
    lds_wait_ge(F, F_ALIVE, 1u);
    if (DELAY_A) { lds_wait_ge(F, F_OPUB, 1u); __builtin_amdgcn_s_sleep(DELAY_A); }
    for (;;) {
      MEMBAR();
#pragma unroll
      for (int i = 0; i < 4; ++i) if (!got[i]) ov[i] = ld128(rs, WS_OP + (xg + 8 * ((i >> 1) * 16 + 2 * w + (i & 1))) * 1024 + lane * 16);
      bool all = true;
#pragma unroll
      for (int i = 0; i < 4; ++i) if (!got[i]) {
        got[i] = __all((ov[i].x & ov[i].y & ov[i].z & ov[i].w & 1u) != 0u);
        all = all && got[i];
      }
      if (all || !spin_ok(F, spins)) break;
    }
#pragma unroll
    for (int hk = 0; hk < 2; ++hk) {
      const f32x4 s = __builtin_bit_cast(f32x4, ov[2 * hk]) + __builtin_bit_cast(f32x4, ov[2 * hk + 1]);
      *(LAS u32*)(L + L_OS + w * 1040 + hk * 512 + lane * 4) = pack2(s[0], s[2]);
      *(LAS u32*)(L + L_OS + w * 1040 + hk * 512 + 256 + lane * 4) = pack2(s[1], s[3]);
    }
    BARRIER();
    if (w != 0) STAMP(10);
    if (w >= 1 && w <= 4) {
      const int ct = w - 1;
      const u32 ob = L_OS + (lane & 7) * 1040 + g * 16;
      u32x4 bfr[16];
#pragma unroll
      for (int s = 0; s < 16; ++s) bfr[s] = lds128(L, ob + s * 64);
      f32x4 a0 = {0, 0, 0, 0}, a1 = {0, 0, 0, 0}, a2 = {0, 0, 0, 0}, a3 = {0, 0, 0, 0};
#pragma unroll
      for (int s = 0; s < 16; s += 4) { a0 = mfma(wf[s], bfr[s], a0); a1 = mfma(wf[s + 1], bfr[s + 1], a1); a2 = mfma(wf[s + 2], bfr[s + 2], a2); a3 = mfma(wf[s + 3], bfr[s + 3], a3); }
      const f32x4 a = (a0 + a1) + (a2 + a3);
      if (r16 < 8) st128(rs, WS_WP + (xg * 144 + (u * 4 + ct) * 4 + g) * 128 + r16 * 16, u32x4{vld(a[0]), vld(a[1]), vld(a[2]), vld(a[3])});
      STAMP(11);
    } else if (w == 7) {
      u32x4 pa, pb; u32 spins2 = 0;
      if (DELAY_B) __builtin_amdgcn_s_sleep(DELAY_B);
      const int wo_ = WS_WP + ((lane >> 3) * 144 + 2 * mu) * 128 + (lane & 7) * 16;
      for (;;) {
        MEMBAR();
        pa = ld128(rs, wo_); pb = ld128(rs, wo_ + 128);
        if (__all((pa.x & pa.y & pa.z & pa.w & pb.x & pb.y & pb.z & pb.w & 1u) != 0u) || !spin_ok(F, spins2)) break;
      }
      float a[8];
      a[0] = __uint_as_float(pa.x); a[1] = __uint_as_float(pa.y); a[2] = __uint_as_float(pa.z); a[3] = __uint_as_float(pa.w);
      a[4] = __uint_as_float(pb.x); a[5] = __uint_as_float(pb.y); a[6] = __uint_as_float(pb.z); a[7] = __uint_as_float(pb.w);
#pragma unroll
      for (int i = 0; i < 8; ++i) a[i] = sum_x8_16_32(a[i]);
      u32x4 xo;
#pragma unroll
      for (int i = 0; i < 4; ++i) {
        const u32 ap = pack2(a[2 * i], a[2 * i + 1]);
        const u32 tp = pack2(bflo(pre4a[i]) * bflo(ap), bfhi(pre4a[i]) * bfhi(ap));
        xo[i] = nosent(pack2(bflo(pre4b[i]) + bflo(tp), bfhi(pre4b[i]) + bfhi(tp)));
      }
      st128(rs, WS_X1 + (lane >> 3) * 9216 + mu * 128 + (lane & 7) * 16, xo);
      STAMP(12);
    }
  } else if (isF) {
    const u32 n0 = Wtot + 32u * (u32)nsl;
    bool have_gu = DEFER_DOWN != 2 || w == 0 || w == 7;
    auto read_gu = [&]() {
      const int cw = w - 1, m = cw & 1, kt = cw >> 1;
      const u32 a0 = L_RING + 36 * m * 1024 + (kt * 192 + 8 * g + qp) * 64 + 8 * pp;
#pragma unroll
      for (int s = 0; s < 6; ++s) { wf[2 * s] = tr_frag(L, a0 + s * 2048, 4 * 64); wf[2 * s + 1] = tr_frag(L, a0 + s * 2048 + 32, 4 * 64); }
    };
    if (w == 7) {
      u32x4 pv; u32 spins = 0;
      if (DELAY_W) __builtin_amdgcn_s_sleep(DELAY_W);
      for (;;) {
        MEMBAR();
        pv = ld128(rs, WS_WP + ((lane >> 3) * 144 + f) * 128 + (lane & 7) * 16);
        if (__all((pv.x & pv.y & pv.z & pv.w & 1u) != 0u) || !spin_ok(F, spins)) break;
        if (WPPOLL_SLEEP) __builtin_amdgcn_s_sleep(WPPOLL_SLEEP);
      }
      F[F_X1] = 1u;
      STAMP(10);
    } else {
      lds_wait_ge(F, F_X1, 1u);
      if (!have_gu && F[F_LANDED] >= n0 + 72u) { MEMBAR(); read_gu(); have_gu = true; }
    }
    if (DELAY_C) __builtin_amdgcn_s_sleep(DELAY_C);
    u32x2 x1p = {0u, 0u};
    {
      if (w != 0) {
        const int e0 = (w - 1) * 64 + lane, xb = WS_X1 + xg * 9216;
        u32x4 xv, xw = {0u, 0u, 0u, 0u}; u32 spins = 0;
        for (;;) {
          MEMBAR();
          xv = ld128(rs, xb + e0 * 16);
          if (w <= 2) xw = ld128(rs, xb + (448 + e0) * 16);
          if (__all(umax(umax4(xv), umax4(xw)) != SENT) || !spin_ok(F, spins)) break;
        }
        STAMP(16);
        const u32 a = L_ACT + (e0 & 7) * ACT_STRIDE + (e0 >> 3) * 16;
        *(LAS u32x4*)(L + a) = xv;
        if (w <= 2) *(LAS u32x4*)(L + a + 56 * 16) = xw;
      }
      BARRIER();
      if (w != 0) STAMP(17);
      u32 xr[5];
#pragma unroll
      for (int i = 0; i < 4; ++i) xr[i] = *(LAS u32*)(L + L_ACT + w * ACT_STRIDE + (lane + 64 * i) * 4);
      xr[4] = *(LAS u32*)(L + L_ACT + w * ACT_STRIDE + (256 + (lane & 31)) * 4);
      if (w == 7) x1p = *(LAS u32x2*)(L + L_ACT + (lane & 7) * ACT_STRIDE + f * 8);
      float v[10];
#pragma unroll
      for (int i = 0; i < 5; ++i) { v[2 * i] = bflo(xr[i]); v[2 * i + 1] = bfhi(xr[i]); }
      float s = (v[0] + v[1]) + (v[2] + v[3]) + ((v[4] + v[5]) + (v[6] + v[7]));
      if (lane < 32) s += v[8] + v[9];
      const float mean = wave_sum(s) * (1.0f / DM);
      float q2 = 0.f, q3 = 0.f;
#pragma unroll
      for (int i = 0; i < 4; ++i) { const float d0 = v[2 * i] - mean, d1 = v[2 * i + 1] - mean; q2 += d0 * d0; q3 += d1 * d1; }
      if (lane < 32) { const float d0 = v[8] - mean, d1 = v[9] - mean; q2 += d0 * d0; q3 += d1 * d1; }
      const float var = wave_sum(q2 + q3) * (1.0f / DM);
      const float mean_r = rb(mean);
      const float rinv = __builtin_amdgcn_rcpf(rb(__builtin_amdgcn_sqrtf(rb(rb(var) + 1e-5f))));
#pragma unroll
      for (int i = 0; i < 5; ++i) {
        const u32 dp = pack2(v[2 * i] - mean_r, v[2 * i + 1] - mean_r);
        const u32 np = pack2(bflo(dp) * rinv, bfhi(dp) * rinv);
        const u32 tp = pack2(bflo(np) * bflo(cbp[i]), bfhi(np) * bfhi(cbp[i]));
        const u32 hp = pack2(bflo(tp) + bflo(sbp[i]), bfhi(tp) + bfhi(sbp[i]));
        if (i < 4) *(LAS u32*)(L + L_H2 + w * ACT_STRIDE + (lane + 64 * i) * 4) = hp;
        else if (lane < 32) *(LAS u32*)(L + L_H2 + w * ACT_STRIDE + (256 + lane) * 4) = hp;
      }
      if (w != 0) STAMP(18);
    }
    if (w == 0) { WAIT_VM0(); F[F_LANDED] = nfin; }
    else if (!have_gu) { lds_wait_ge(F, F_LANDED, n0 + 72u); read_gu(); }
    BARRIER();
    if (w != 0) STAMP(11);
    if (w == 7) {
      BARRIER();
      BARRIER();
      const int off = WS_FP + (lane >> 3) * 9216 + ((f >> 3) * 4 + (f & 3)) * 128 + (lane & 7) * 16 + ((f >> 2) & 1) * 8;
      u32x2 e[18]; u32 spins = 0;
      u32 slo = 0x00003f80u, shi = 0x3f800000u;
      asm volatile("" : "+v"(slo), "+v"(shi));
      if (DELAY_E) __builtin_amdgcn_s_sleep(DELAY_E);
      for (;;) {
        MEMBAR();
#pragma unroll
        for (int i = 0; i < 18; ++i) e[i] = ld64(rs, off + i * 8 * 9216);
        u32 mx = 0;
#pragma unroll
        for (int i = 0; i < 18; ++i) mx = umax(mx, umax(e[i].x, e[i].y));
        if (__all(mx != SENT) || !spin_ok(F, spins)) break;
      }
      STAMP(23);
      f32x4 s = {0, 0, 0, 0}, s1 = {0, 0, 0, 0};
#pragma unroll
      for (int i = 0; i < 18; i += 2) {
        s[0] = add_sel(e[i].x, slo, s[0]); s[1] = add_sel(e[i].x, shi, s[1]); s[2] = add_sel(e[i].y, slo, s[2]); s[3] = add_sel(e[i].y, shi, s[3]);
        s1[0] = add_sel(e[i + 1].x, slo, s1[0]); s1[1] = add_sel(e[i + 1].x, shi, s1[1]); s1[2] = add_sel(e[i + 1].y, slo, s1[2]); s1[3] = add_sel(e[i + 1].y, shi, s1[3]);
      }
      s += s1;
      const u32 dA = pack2(sum_x8_16_32(s[0]), sum_x8_16_32(s[1])), dB = pack2(sum_x8_16_32(s[2]), sum_x8_16_32(s[3]));
      const u32 tA = pack2(bflo(pre4a.x) * bflo(dA), bfhi(pre4a.x) * bfhi(dA)), tB = pack2(bflo(pre4a.y) * bflo(dB), bfhi(pre4a.y) * bfhi(dB));
      if (lane < 8) *(GAS u32x2*)(p.out + lane * DM + f * 4) = u32x2{pack2(bflo(x1p.x) + bflo(tA), bfhi(x1p.x) + bfhi(tA)), pack2(bflo(x1p.y) + bflo(tB), bfhi(x1p.y) + bfhi(tB))};
      STAMP(13);
    } else {
      const int cw = w - 1;
      if (w >= 1) {
        const int kt = cw >> 1;
        const u32 hb = L_H2 + (lane & 7) * ACT_STRIDE + kt * 384 + g * 16;
        u32x4 bfr[6];
#pragma unroll
        for (int s = 0; s < 6; ++s) bfr[s] = lds128(L, hb + s * 64);
        f32x4 a0 = {0, 0, 0, 0}, a1 = {0, 0, 0, 0}, a2 = {0, 0, 0, 0}, a3 = {0, 0, 0, 0};
#pragma unroll
        for (int s = 0; s < 6; s += 2) { a0 = mfma(wf[2 * s], bfr[s], a0); a1 = mfma(wf[2 * s + 1], bfr[s], a1); a2 = mfma(wf[2 * s + 2], bfr[s + 1], a2); a3 = mfma(wf[2 * s + 3], bfr[s + 1], a3); }
        a0 += a2; a1 += a3;
        if (r16 < 8) { *(LAS f32x4*)(L + L_R2 + cw * 1024 + (g * 8 + r16) * 16) = a0; *(LAS f32x4*)(L + L_R2 + cw * 1024 + 512 + (g * 8 + r16) * 16) = a1; }
        if (DEFER_DOWN) {
          lds_wait_ge(F, F_LANDED, n0 + (DEFER_DOWN == 2 ? 108u : 36u));
          const u32 d0 = L_RING + (DEFER_DOWN == 2 ? 72 * 1024 : posd * 16384) + (8 * g + qp) * 1152 + cw * 192 + 8 * pp;
#pragma unroll
          for (int t = 0; t < 6; ++t) wf[12 + t] = tr_frag(L, d0 + t * 32, 4 * 1152);
        }
      }
      if (w != 0) STAMP(19);
      BARRIER();
      if (w != 0) STAMP(20);
      if (w < 4) {
        const int ct = w >> 1, e = (w & 1) * 64 + lane;
        const u32 a = L_R2 + ct * 512 + e * 4;
        const float gg = rb(*(LAS float*)(L + a) + *(LAS float*)(L + a + 2048) + *(LAS float*)(L + a + 4096));
        const float uu = rb(*(LAS float*)(L + a + 1024) + *(LAS float*)(L + a + 3072) + *(LAS float*)(L + a + 5120));
        const float inner = 0.7978845608028654f * (gg + 0.044715f * gg * gg * gg);
        const float th = 1.0f - 2.0f * __builtin_amdgcn_rcpf(1.0f + __expf(2.0f * inner));
        const float ge = rb(0.5f * gg * (1.0f + th));
        const int ls = e >> 2, rr = e & 3, b_ = ls & 7, g_ = ls >> 3;
        *(LAS u16*)(L + L_HH + b_ * 80 + (ct * 16 + g_ * 4 + rr) * 2) = (u16)f2bf(ge * uu);
      }
      if (w != 0) STAMP(21);
      BARRIER();
      if (w != 0) STAMP(22);
      if (w >= 1) {
        const u32x4 bfr = lds128(L, L_HH + (lane & 7) * 80 + g * 16);
        f32x4 a[6];
#pragma unroll
        for (int t = 0; t < 6; ++t) a[t] = mfma(wf[12 + t], bfr, f32x4{0, 0, 0, 0});
#pragma unroll
        for (int t = 0; t < 6; t += 2)
          if (r16 < 8) st128(rs, WS_FP + f * 9216 + ((cw * 3 + (t >> 1)) * 4 + g) * 128 + r16 * 16,
                             u32x4{nosent(pack2(a[t][0], a[t][1])), nosent(pack2(a[t][2], a[t][3])), nosent(pack2(a[t + 1][0], a[t + 1][1])), nosent(pack2(a[t + 1][2], a[t + 1][3]))});
      }
      if (w != 0) STAMP(12);
    }
  }
}

}

extern "C" void kernel_launch(void* const* d_in, const int* in_sizes, int n_in, void* d_out, int out_size, void* d_ws, size_t ws_size,
                              hipStream_t stream) {
  (void)in_sizes; (void)n_in; (void)out_size; (void)ws_size;
  Params p;
  p.x = (const u16*)d_in[0];
  p.kc = (const unsigned char*)d_in[1];
  p.vc = (const unsigned char*)d_in[2];
  p.wq = (const unsigned char*)d_in[3];
  p.wo = (const unsigned char*)d_in[4];
  p.wg = (const unsigned char*)d_in[6];
  p.wu = (const unsigned char*)d_in[7];
  p.wd = (const unsigned char*)d_in[8];
  p.c = (const u16*)d_in[9];
  p.wmod = (const unsigned char*)d_in[10];
  p.out = (u16*)d_out;
  p.ws = (unsigned char*)d_ws;
  (void)hipFuncSetAttribute((const void*)fused_block, hipFuncAttributeMaxDynamicSharedMemorySize, LDS_BYTES);
  hipLaunchKernelGGL(fused_block, dim3(NCU), dim3(NT), LDS_BYTES, stream, p);
}
```

```cpp
#include <hip/hip_runtime.h>
#include <stdint.h>
#include <type_traits>
#pragma clang fp contract(off)

#define LAS __attribute__((address_space(3)))
#define GAS __attribute__((address_space(1)))
typedef unsigned u32;
typedef unsigned long long u64;
typedef unsigned short u16;
typedef u32 u32x4 __attribute__((ext_vector_type(4)));
typedef u32 u32x2 __attribute__((ext_vector_type(2)));
typedef float f32x4 __attribute__((ext_vector_type(4)));
typedef __bf16 bf16x8 __attribute__((ext_vector_type(8)));
typedef short s16x4 __attribute__((ext_vector_type(4)));

namespace {

constexpr int DM = 576, HKV = 16, SK = 8192;
constexpr int NCU = 256, NT = 512;

constexpr int INFLW = 320;
constexpr int P1W = 12;
constexpr int WQW = 16, WOW = 16;
constexpr int BASE_LVL = 2, EARLY_LVL = 4, EARLY_Q = 16;
constexpr int BOOST1 = 2, BOOST2 = 4, SLOW1 = 1;
constexpr int SCHED_TE = 7750;
constexpr int SCHED_TAU = 876;
constexpr int SCHED_Q0 = 17, SCHED_L1 = 30, SCHED_L2 = 70, SCHED_L3 = 120;
constexpr int SHIFT_O = 1, SHIFT_H = 2;
constexpr int HGATE = 800, QGATE = 1000;
constexpr int DELAY_A = 12, DELAY_B = 14, DELAY_W = 36, DELAY_C = 16, DELAY_E = 24;
constexpr int HPOLL_SLEEP = 2, QPOLL_SLEEP = 2;
#define AUX_KV "nt"
#define AUX_W "nt"

constexpr int REP_H = 8;
constexpr int WS_CTRL = 0;
constexpr int WS_ALIVE = 4096;
constexpr int WS_ST = 8192;
constexpr int WS_MODT = WS_ST + 256 * 128;
constexpr int WS_X1 = WS_MODT + 36864;
constexpr int WS_H = WS_X1 + 8 * 9216;
constexpr int WS_Q = WS_H + REP_H * 18432;
constexpr int WS_OP = WS_Q + 131072;
constexpr int WS_WP = WS_OP + 262144;
constexpr int WS_FP = WS_WP + 147456;
constexpr int WS_T0 = WS_FP + 144 * 9216;
constexpr int WS_END = WS_T0 + 8 * 128;

constexpr int MAXSL = 64 + (SHIFT_O > SHIFT_H ? SHIFT_O : SHIFT_H);
constexpr int L_RING = 0;
constexpr int L_SC = 131072;
constexpr int ACT_STRIDE = 1168;
constexpr int L_P2 = L_SC + 8192;
constexpr int L_H2 = L_SC;
constexpr int L_R2 = L_SC + 9344;
constexpr int L_HH = L_R2 + 6144;
constexpr int L_ACT = L_SC + (MAXSL * 256 > 16384 ? MAXSL * 256 : 16384);
constexpr int L_QS = L_ACT + 9344;
constexpr int L_RED = L_QS + 512;
constexpr int L_STAT = L_RED + 3072;
constexpr int L_MX = L_STAT + 64;
constexpr int L_LW = L_MX + 64;
constexpr int L_FLG = L_LW + 64;
constexpr int LDS_BYTES = 163840;
static_assert(L_FLG + 256 <= LDS_BYTES && L_HH + 640 <= L_ACT, "lds");
constexpr int F_LANDED = 0, F_FREE = 1  , F_WREL = 9  , F_SC = 12, F_P1 = 13, F_INIT = 14, F_Q = 15, F_ABORT = 16, F_ALIVE = 17,
              F_P2 = 18, F_MX = 19, F_L = 20, F_X1 = 21, F_TAGOK = 22, F_TAG = 23, F_OPART = 24, F_CB0 = 25, F_T0 = 26, F_OPUB = 27;

constexpr u32 SENT = 0xFFFFFFFFu;
constexpr u32 LDS_SPIN_CAP = 1u << 21;
constexpr u32 GLB_SPIN_CAP = 1u << 17;
constexpr float LOG2E = 1.4426950408889634f;

struct Params {
  const u16* x; const unsigned char* kc; const unsigned char* vc; const unsigned char* wq; const unsigned char* wo;
  const unsigned char* wg; const unsigned char* wu; const unsigned char* wd; const u16* c; const unsigned char* wmod;
  u16* out; unsigned char* ws;
};

__device__ __forceinline__ float bf2f(u32 v16) { return __uint_as_float(v16 << 16); }
__device__ __forceinline__ float bflo(u32 d) { return __uint_as_float(d << 16); }
__device__ __forceinline__ float bfhi(u32 d) { return __uint_as_float(d & 0xffff0000u); }
__device__ __forceinline__ float rb(float f) { return (float)(__bf16)f; }
__device__ __forceinline__ u32 f2bf(float f) { return (u32)__builtin_bit_cast(u16, (__bf16)f); }
typedef float f32x2 __attribute__((ext_vector_type(2)));
typedef __bf16 bf16x2 __attribute__((ext_vector_type(2)));
__device__ __forceinline__ u32 pack2(float lo, float hi) { return __builtin_bit_cast(u32, __builtin_convertvector(f32x2{lo, hi}, bf16x2)); }
__device__ __forceinline__ u32 nosent(u32 p) { return p < 0xFFFEFFFFu ? p : 0xFFFEFFFFu; }
__device__ __forceinline__ u32 umax(u32 a, u32 b) { return a > b ? a : b; }
__device__ __forceinline__ u32 umax4(u32x4 v) { return umax(umax(v.x, v.y), umax(v.z, v.w)); }
__device__ __forceinline__ float add_sel(u32 v, u32 sel, float acc) { return __builtin_amdgcn_fdot2_f32_bf16(__builtin_bit_cast(bf16x2, v), __builtin_bit_cast(bf16x2, sel), acc, false); }
__device__ __forceinline__ u32 vld(float f) { return __float_as_uint(f) | 1u; }
__device__ __forceinline__ f32x4 mfma(u32x4 a, u32x4 b, f32x4 c) {
  return __builtin_amdgcn_mfma_f32_16x16x32_bf16(__builtin_bit_cast(bf16x8, a), __builtin_bit_cast(bf16x8, b), c, 0, 0, 0);
}
__device__ __forceinline__ u32x4 mk_srd(const void* p, u32 bytes) {
  const u64 a = (u64)p;
  u32x4 s;
  s.x = __builtin_amdgcn_readfirstlane((u32)a);
  s.y = __builtin_amdgcn_readfirstlane((u32)(a >> 32) & 0xffffu);
  s.z = __builtin_amdgcn_readfirstlane(bytes);
  s.w = 0x00020000u;
  return s;
}
#define MEMBAR() asm volatile("" ::: "memory")
#define WAIT_LGKM0() asm volatile("s_waitcnt lgkmcnt(0)" ::: "memory")
#define WAIT_VM0() asm volatile("s_waitcnt vmcnt(0)" ::: "memory")
#define BARRIER() do { asm volatile("s_waitcnt lgkmcnt(0)" ::: "memory"); __builtin_amdgcn_s_barrier(); asm volatile("" ::: "memory"); } while (0)

#define DMA1(AUX, srd, voff, soff, ldsaddr)                                                                      \
  do { u32 keep_;                                                                                                \
    asm volatile("s_mov_b32 %0, m0\n\ts_mov_b32 m0, %1\n\ts_nop 0\n\t"                                           \
                 "buffer_load_dwordx4 %4, %2, %3 offen " AUX " lds\n\ts_mov_b32 m0, %0"                          \
                 : "=&s"(keep_) : "s"(ldsaddr), "s"(srd), "s"(soff), "v"(voff) : "memory"); } while (0)
#define DMA4(AUX, srd, v0, v1, v2, v3, soff, ldsaddr)                                                            \
  do { u32 keep_;                                                                                                \
    asm volatile("s_mov_b32 %0, m0\n\ts_mov_b32 m0, %1\n\ts_nop 0\n\t"                                           \
                 "buffer_load_dwordx4 %4, %2, %3 offen " AUX " lds\n\t"                                          \
                 "buffer_load_dwordx4 %5, %2, %3 offen offset:1024 " AUX " lds\n\t"                              \
                 "buffer_load_dwordx4 %6, %2, %3 offen offset:2048 " AUX " lds\n\t"                              \
                 "buffer_load_dwordx4 %7, %2, %3 offen offset:3072 " AUX " lds\n\ts_mov_b32 m0, %0"              \
                 : "=&s"(keep_) : "s"(ldsaddr), "s"(srd), "s"(soff), "v"(v0), "v"(v1), "v"(v2), "v"(v3) : "memory"); } while (0)

#define DMA4W(AUX, srd, voff, so, step, ldsaddr)                                                                 \
  do { u32 keep_; const u32 d_ = (step) - 1024u, s0_ = (so), s1_ = s0_ + d_, s2_ = s1_ + d_, s3_ = s2_ + d_;     \
    asm volatile("s_mov_b32 %0, m0\n\ts_mov_b32 m0, %1\n\ts_nop 0\n\t"                                          \
                 "buffer_load_dwordx4 %7, %2, %3 offen " AUX " lds\n\t"                                          \
                 "buffer_load_dwordx4 %7, %2, %4 offen offset:1024 " AUX " lds\n\t"                              \
                 "buffer_load_dwordx4 %7, %2, %5 offen offset:2048 " AUX " lds\n\t"                              \
                 "buffer_load_dwordx4 %7, %2, %6 offen offset:3072 " AUX " lds\n\ts_mov_b32 m0, %0"              \
                 : "=&s"(keep_) : "s"(ldsaddr), "s"(srd), "s"(s0_), "s"(s1_), "s"(s2_), "s"(s3_), "v"(voff) : "memory"); } while (0)

template <int CTRL> __device__ __forceinline__ float dpp(float x) {
  return __builtin_bit_cast(float, __builtin_amdgcn_mov_dpp(__builtin_bit_cast(int, x), CTRL, 0xf, 0xf, true));
}
constexpr int ROR8 = 0x128, ROR4 = 0x124, ROR2 = 0x122, ROR1 = 0x121;
__device__ __forceinline__ float xrow_max(float x) {
  auto s = __builtin_amdgcn_permlane16_swap(__float_as_uint(x), __float_as_uint(x), false, false);
  x = fmaxf(__uint_as_float(s[0]), __uint_as_float(s[1]));
  auto t = __builtin_amdgcn_permlane32_swap(__float_as_uint(x), __float_as_uint(x), false, false);
  return fmaxf(__uint_as_float(t[0]), __uint_as_float(t[1]));
}
__device__ __forceinline__ float xrow_sum(float x) {
  auto s = __builtin_amdgcn_permlane16_swap(__float_as_uint(x), __float_as_uint(x), false, false);
  x = __uint_as_float(s[0]) + __uint_as_float(s[1]);
  auto t = __builtin_amdgcn_permlane32_swap(__float_as_uint(x), __float_as_uint(x), false, false);
  return __uint_as_float(t[0]) + __uint_as_float(t[1]);
}
__device__ __forceinline__ float wave_max(float x) {
  x = fmaxf(x, dpp<ROR8>(x)); x = fmaxf(x, dpp<ROR4>(x)); x = fmaxf(x, dpp<ROR2>(x)); x = fmaxf(x, dpp<ROR1>(x));
  return xrow_max(x);
}
__device__ __forceinline__ float wave_sum(float x) {
  x += dpp<ROR8>(x); x += dpp<ROR4>(x); x += dpp<ROR2>(x); x += dpp<ROR1>(x);
  return xrow_sum(x);
}
__device__ __forceinline__ float sum_x8_16_32(float x) {
  x += dpp<ROR8>(x);
  return xrow_sum(x);
}

typedef volatile LAS u32* flagp;
__device__ __forceinline__ void lds_wait_ge(flagp F, int idx, u32 need) {
  (void)F;
  const u32 a = L_FLG + idx * 4, ab = L_FLG + F_ABORT * 4;
  u32 v, z, cnt;
  asm volatile("s_mov_b32 %2, 0\n"
               "1:\n\tds_read_b32 %0, %3\n\tds_read_b32 %1, %4\n\ts_waitcnt lgkmcnt(0)\n\t"
               "v_cmp_le_u32 vcc, %5, %0\n\ts_cbranch_vccnz 2f\n\t"
               "v_cmp_ne_u32 vcc, 0, %1\n\ts_cbranch_vccnz 2f\n\t"
               "s_sleep 1\n\ts_add_u32 %2, %2, 1\n\ts_cmp_lt_u32 %2, %6\n\ts_cbranch_scc1 1b\n\t"
               "v_mov_b32 %0, 1\n\tds_write_b32 %4, %0\n"
               "2:"
               : "=&v"(v), "=&v"(z), "=&s"(cnt) : "v"(a), "v"(ab), "v"(need), "s"(LDS_SPIN_CAP) : "vcc", "scc", "memory");
}
__device__ __forceinline__ void lds_add(int idx, u32 n) {
  const u32 a = L_FLG + idx * 4;
  asm volatile("s_waitcnt lgkmcnt(0)\n\ts_mov_b64 vcc, exec\n\ts_mov_b64 exec, 1\n\tds_add_u32 %0, %1\n\ts_mov_b64 exec, vcc"
               :: "v"(a), "v"(n) : "vcc", "memory");
}
__device__ __forceinline__ bool spin_ok(flagp F, u32& spins) {
  if (++spins > GLB_SPIN_CAP) { F[F_ABORT] = 2u; return false; }
  if ((spins & 255u) == 0u && F[F_ABORT] != 0u) return false;
  return true;
}

constexpr int SC1 = 16;
typedef __amdgpu_buffer_rsrc_t rsrc_t;
__device__ __forceinline__ u32x4 ld128(rsrc_t r, int off) { return __builtin_amdgcn_raw_buffer_load_b128(r, off, 0, SC1); }
__device__ __forceinline__ u32x2 ld64(rsrc_t r, int off) { return __builtin_amdgcn_raw_buffer_load_b64(r, off, 0, SC1); }
__device__ __forceinline__ u32 ld32(rsrc_t r, int off) { return __builtin_amdgcn_raw_buffer_load_b32(r, off, 0, SC1); }
__device__ __forceinline__ void st128(rsrc_t r, int off, u32x4 v) { __builtin_amdgcn_raw_buffer_store_b128(v, r, off, 0, SC1); }
__device__ __forceinline__ void st64(rsrc_t r, int off, u32x2 v) { __builtin_amdgcn_raw_buffer_store_b64(v, r, off, 0, SC1); }

__device__ __forceinline__ u32x4 tr_frag(LAS unsigned char* L, u32 addr, u32 stride4) {
  const s16x4 a = __builtin_amdgcn_ds_read_tr16_b64_v4i16((LAS s16x4*)(L + addr));
  const s16x4 b = __builtin_amdgcn_ds_read_tr16_b64_v4i16((LAS s16x4*)(L + addr + stride4));
  const u32x2 a2 = __builtin_bit_cast(u32x2, a), b2 = __builtin_bit_cast(u32x2, b);
  return u32x4{a2.x, a2.y, b2.x, b2.y};
}
__device__ __forceinline__ u32x4 lds128(LAS unsigned char* L, u32 addr) { return *(LAS u32x4*)(L + addr); }

__device__ __forceinline__ void ln_stats(const float (&v)[10], bool tail, float& mean_r, float& sd_r) {
  float s = 0.f;
#pragma unroll
  for (int i = 0; i < 8; ++i) s += v[i];
  if (tail) s += v[8] + v[9];
  const float mean = wave_sum(s) * (1.0f / DM);
  float q = 0.f;
#pragma unroll
  for (int i = 0; i < 8; ++i) { const float d = v[i] - mean; q += d * d; }
  if (tail) { const float d0 = v[8] - mean, d1 = v[9] - mean; q += d0 * d0 + d1 * d1; }
  const float var = wave_sum(q) * (1.0f / DM);
  mean_r = rb(mean);
  sd_r = rb(sqrtf(rb(rb(var) + 1e-5f)));
}

__global__ void __launch_bounds__(NT, 2) fused_block(Params p) {
  extern __shared__ __attribute__((aligned(16))) unsigned char smem[];
  LAS unsigned char* L = (LAS unsigned char*)smem;
  flagp F = (flagp)(L + L_FLG);
  const int tid = threadIdx.x, lane = tid & 63, w = __builtin_amdgcn_readfirstlane(tid >> 6);
  const u32 t_start = (u32)__builtin_amdgcn_s_memrealtime();
  const int c = blockIdx.x, xg = c & 7, ci = c >> 3;
  const int hkv = 2 * xg + (ci >> 4), cj = ci & 15, bb = cj >> 1, half = cj & 1;
  const int pj = ci >> 1;
  const bool isF = half == 0 || pj >= 14, isO = !isF && pj < 9;
  const int hu = xg * 5 + (pj - 9);
  const bool isH = !isF && !isO && hu < 36;
  const int f = xg * 18 + (half == 0 ? pj : pj + 2);
  const int u = pj;
  const int mu = xg * 9 + u;
  const bool isQ = isO && u < 8;
  const int shift = (pj < 8) * SHIFT_O + (pj >= 9 && pj < 14 && hu < 36) * SHIFT_H;
  const int nsl = half == 0 ? 64 + shift : 64 - shift;
  const int key0 = half == 0 ? 0 : (64 + shift) * 64;
  const u32 Wtot = isF ? 72u : isO ? 136u : isH ? 108u : 0u;
  const int roff = isF ? (shift == 1 ? 6 : 5) : isH ? 7 : 0;
  const int posd = (2 * nsl + roff) & 7;
  const int g = lane >> 4, r16 = lane & 15, qp = (lane & 15) >> 2, pp = lane & 3;
  const rsrc_t rs = __builtin_amdgcn_make_buffer_rsrc((void*)p.ws, 0, WS_END, 0x00020000);

  u32x4 wf[18];
  u32 cbp[5], sbp[5];
  u32x4 pre4a, pre4b;
  float osA = 0.f, osB = 0.f;
  u32 nfin = 0;

  auto load_cbsb = [&](int row, u32 (&cb)[5], u32 (&sb)[5]) {
    u32 spins = 0;
    for (;;) {
      MEMBAR();
      const int base = WS_MODT + row * 4608;
#pragma unroll
      for (int i = 0; i < 4; ++i) { sb[i] = ld32(rs, base + (288 + lane + 64 * i) * 4); cb[i] = ld32(rs, base + (576 + lane + 64 * i) * 4); }
      sb[4] = ld32(rs, base + (288 + 256 + (lane & 31)) * 4); cb[4] = ld32(rs, base + (576 + 256 + (lane & 31)) * 4);
      u32 mx = 0;
#pragma unroll
      for (int i = 0; i < 5; ++i) mx = umax(mx, umax(sb[i], cb[i]));
      if (__all(mx != SENT) || !spin_ok(F, spins)) break;
    }
#pragma unroll
    for (int i = 0; i < 5; ++i) cb[i] = pack2(1.0f + bflo(cb[i]), 1.0f + bfhi(cb[i]));
  };
  auto get_tag = [&]() { lds_wait_ge(F, F_TAGOK, 1u); return (u32)F[F_TAG]; };
  auto q_partial = [&](int kt, f32x4 (&acc)[4]) {
    lds_wait_ge(F, F_LANDED, 72u);
    u32x4 pq[24];
    const u32 a0 = L_RING + (kt * 192 + 8 * g + qp) * 128 + 8 * pp;
#pragma unroll
    for (int s = 0; s < 6; ++s)
#pragma unroll
      for (int ct = 0; ct < 4; ++ct) pq[s * 4 + ct] = tr_frag(L, a0 + s * 4096 + ct * 32, 4 * 128);
    lds_add(F_WREL + 0, 1);
    const u32 tag = get_tag();
    u32x4 hv[6];
    u32 spins = 0;
    const int hb = WS_H + xg * 18432 + kt * 6144 + lane * 16;
    int lo[6];
#pragma unroll
    for (int i = 0; i < 6; ++i) { const int e = i * 64 + lane; lo[i] = L_ACT + ((e >> 2) & 7) * ACT_STRIDE + kt * 384 + ((e >> 5) * 4 + (e & 3)) * 8; }
    while ((u32)__builtin_amdgcn_s_memrealtime() - t_start < (u32)HGATE) __builtin_amdgcn_s_sleep(2);
    for (;;) {
      MEMBAR();
#pragma unroll
      for (int i = 0; i < 6; ++i) hv[i] = ld128(rs, hb + i * 1024);
      u32 bad = 0;
#pragma unroll
      for (int i = 0; i < 6; ++i) bad |= (hv[i].y ^ tag) | (hv[i].w ^ tag);
      if (__all(bad == 0u) || !spin_ok(F, spins)) break;
      __builtin_amdgcn_s_sleep(HPOLL_SLEEP);
    }
#pragma unroll
    for (int i = 0; i < 6; ++i) *(LAS u32x2*)(L + lo[i]) = u32x2{hv[i].x, hv[i].z};
    WAIT_LGKM0();
    const u32 ab = L_ACT + (lane & 7) * ACT_STRIDE + kt * 384 + g * 16;
#pragma unroll
    for (int ct = 0; ct < 4; ++ct) acc[ct] = f32x4{0, 0, 0, 0};
#pragma unroll
    for (int s = 0; s < 6; ++s) {
      const u32x4 bfr = lds128(L, ab + s * 64);
#pragma unroll
      for (int ct = 0; ct < 4; ++ct) acc[ct] = mfma(pq[s * 4 + ct], bfr, acc[ct]);
    }
  };

  if (w == 0) {
    __builtin_amdgcn_s_setprio(3);
    if (lane < 32) F[lane] = 0u;
    u32 n = 0;
    u32x4 srd0; u32 v0, step0, pre;
    if (isF) { srd0 = mk_srd(p.wg + f * 64, DM * 9216 - f * 64); v0 = (lane >> 2) * 9216 + (lane & 3) * 16; step0 = 16 * 9216; pre = 16; }
    else if (isQ) { srd0 = mk_srd(p.wq + (xg * 512 + u * 64) * 2, DM * 8192 - (xg * 512 + u * 64) * 2); v0 = (lane >> 3) * 8192 + (lane & 7) * 16; step0 = 8 * 8192; pre = WQW - 4; }
    else if (isH) { srd0 = mk_srd(p.wmod + hu * 32, DM * 6912 - hu * 32); v0 = (lane >> 1) * 6912 + (lane & 1) * 16; step0 = 32 * 6912; pre = 8; }
    else { srd0 = mk_srd(p.wq, 0); v0 = 0; step0 = 1024; pre = 0; }
#pragma unroll 1
    for (; n < pre; n += 4) DMA4W(AUX_W, srd0, v0, n * step0, step0, L_RING + n * 1024);
    BARRIER();
    int pub = 0;
    auto run = [&](auto inflp, const u32x4& srd, u32 voff, u32 step, u32 cnt, u32 lds0, u32 first = 0) {
      constexpr int NP = decltype(inflp)::value;
      constexpr int KEEP = NP >= 8 ? NP - 4 : NP / 2;
      u32 s = first;
#pragma unroll 1
      for (; s + 4 <= cnt; s += 4) {
        DMA4W(AUX_W, srd, voff, s * step, step, lds0 + s * 1024);
        n += 4;
        asm volatile("s_waitcnt vmcnt(%0)" :: "i"(KEEP) : "memory");
        pub = max(pub, (int)n - KEEP); F[F_LANDED] = (u32)pub;
      }
#pragma unroll 1
      for (; s < cnt; ++s) { DMA1(AUX_W, srd, voff, s * step, lds0 + s * 1024); ++n; }
    };
    typedef std::integral_constant<int, INFLW / 16> P16;
    typedef std::integral_constant<int, (INFLW / 8 > 32 ? 32 : INFLW / 8)> P8;
    if (isF) {
      run(P16(), srd0, v0, step0, 36, L_RING, pre);
      run(P16(), mk_srd(p.wu + f * 64, DM * 9216 - f * 64), v0, step0, 36, L_RING + 36 * 1024);
    } else if (isO) {
      if (isQ) run(std::integral_constant<int, WQW>(), srd0, v0, step0, 72, L_RING, pre);
      else n = 72;
      run(std::integral_constant<int, WOW>(), mk_srd(p.wo + xg * 512 * 1152 + u * 128, 512 * 1152 - u * 128), (lane >> 3) * 1152 + (lane & 7) * 16, 8 * 1152, 64, L_RING + 72 * 1024);
    } else if (isH) {
      const u32 vm32 = (lane >> 1) * 6912 + (lane & 1) * 16;
      run(std::integral_constant<int, P1W>(), srd0, v0, step0, 18, L_RING, pre);
      run(std::integral_constant<int, P1W>(), mk_srd(p.wmod + 1152 + hu * 32, DM * 6912 - 1152 - hu * 32), vm32, 32 * 6912, 18, L_RING + 18 * 1024);
      run(P8(), mk_srd(p.wmod + 2304 + hu * 128, DM * 6912 - 2304 - hu * 128), (lane >> 3) * 6912 + (lane & 7) * 16, 8 * 6912, 72, L_RING + 36 * 1024);
    }
    const size_t kvoff = ((size_t)(bb * HKV + hkv) * SK + (size_t)key0) * 256;
    const u32x4 srd_k = mk_srd(p.kc + kvoff, (u32)nsl * 16384u), srd_v = mk_srd(p.vc + kvoff, (u32)nsl * 16384u);
    u32 vk[4], vv[2];
#pragma unroll
    for (int j = 0; j < 4; ++j) vk[j] = g * 256 + ((r16 ^ (4 * j + g)) << 4);
#pragma unroll
    for (int j = 0; j < 2; ++j) vv[j] = g * 256 + ((r16 ^ ((g | (j << 2)) << 1)) << 4);
    const u32 wneed = isF ? 0x00022211u : isO ? 0x33221111u : isH ? 0x02222211u : 0u;
    auto wait_window = [&](u32 N) {
      asm volatile("s_waitcnt vmcnt(28)\n\ts_cmp_ge_u32 %0, 28\n\ts_cbranch_scc1 9f\n\t"
                   "s_waitcnt vmcnt(24)\n\ts_cmp_ge_u32 %0, 24\n\ts_cbranch_scc1 9f\n\t"
                   "s_waitcnt vmcnt(20)\n\ts_cmp_ge_u32 %0, 20\n\ts_cbranch_scc1 9f\n\t"
                   "s_waitcnt vmcnt(16)\n9:" :: "s"(N) : "scc", "memory");
    };
    u32 fr_next = 0;
    u32 t_prev = t_start, t_ref = t_start;
    auto wait_free = [&](int pos, u32 need, u32 seen) {
      if (seen < need && F[F_FREE + pos] < need) { WAIT_VM0(); F[F_LANDED] = n; pub = (int)n; lds_wait_ge(F, F_FREE + pos, need); }
    };
#pragma unroll 1
    for (int q = 0; q < 2 * nsl; ++q) {
      const int pos = (q + roff) & 7;
      if (q >= 8) wait_free(pos, (u32)(q - 7), fr_next);
      else {
        const int nb = (wneed >> (4 * pos)) & 15;
#pragma unroll 1
        for (int i = 0; i < nb; ++i)
          if (F[F_WREL + i] < 7u) { WAIT_VM0(); F[F_LANDED] = n; pub = (int)n; lds_wait_ge(F, F_WREL + i, 7u); }
      }
      int lvl = q < EARLY_Q ? EARLY_LVL : BASE_LVL;
      if (q == SCHED_Q0) { const u32 t0_ = __builtin_amdgcn_readfirstlane((u32)F[F_T0]); if (t0_) t_ref = t0_; }
      if (q >= SCHED_Q0) {
        const u32 eown = t_prev + (u32)(((2 * nsl - q + 1) * SCHED_TAU) >> 4);
        const int lag = (int)(eown - t_ref) - SCHED_TE - SCHED_L1;
        if (lag > 0) lvl = BASE_LVL + BOOST1;
        if (lag > SCHED_L2) lvl = BASE_LVL + BOOST2;
        if (lag < -SCHED_L3) lvl = BASE_LVL - SLOW1;
      }
      t_prev = (u32)__builtin_amdgcn_s_memrealtime();
      const u32 N2 = 16u + 4u * (u32)((lvl + 1) >> 1), N = 16u + 4u * (u32)(lvl >> 1);
      fr_next = F[F_FREE + ((pos + 1) & 7)];
      const u32 lb = L_RING + pos * 16384;
      const bool isk = q < nsl;
      const u32x4 srd = isk ? srd_k : srd_v;
      const u32 v0_ = isk ? vk[0] : vv[0], v1_ = isk ? vk[1] : vv[0], v2_ = isk ? vk[2] : vv[1], v3_ = isk ? vk[3] : vv[1];
      const u32 so = (u32)(isk ? q : q - nsl) * 16384u;
#pragma unroll
      for (int gq = 0; gq < 4; ++gq) {
        DMA4(AUX_KV, srd, v0_, v1_, v2_, v3_, so + gq * 4096, lb + gq * 4096);
        n += 4;
        const u32 Ng = gq < 2 ? N2 : N;
        wait_window(Ng);
        pub = max(pub, (int)n - (int)Ng); F[F_LANDED] = (u32)pub;
      }
    }
    if (isF) {
      for (int i = 0; i < 3; ++i) wait_free(posd + i, (u32)(2 * nsl - 7 + i), 0u);
      run(P8(), mk_srd(p.wd + f * 36864, 36864), (u32)(lane * 16), 1024, 36, L_RING + posd * 16384);
    }
    if (!isF) { WAIT_VM0(); F[F_LANDED] = n; }
    nfin = n;
    __builtin_amdgcn_s_setprio(0);
    if (isF) {
      lds_wait_ge(F, F_CB0, 1u);
#pragma unroll
      for (int i = 0; i < 5; ++i) { cbp[i] = *(LAS u32*)(L + L_RED + i * 256 + lane * 4); sbp[i] = *(LAS u32*)(L + L_RED + 1280 + i * 256 + lane * 4); }
    }
  } else if (w == 7) {
    BARRIER();
    const u64 ctrl = __hip_atomic_load((GAS u64*)(p.ws + WS_CTRL), __ATOMIC_RELAXED, __HIP_MEMORY_SCOPE_AGENT);
    const u64 epoch = (((u64)__builtin_amdgcn_readfirstlane((u32)(ctrl >> 32)) << 32) | (u32)__builtin_amdgcn_readfirstlane((u32)ctrl)) + 1ull;
    const u32 tag = (u32)epoch;
    F[F_TAG] = tag;
    WAIT_LGKM0();
    F[F_TAGOK] = 1u;
    if (c == 0 && lane < 8) st64(rs, WS_T0 + lane * 128, u32x2{t_start, tag});
    const u32x4 z4 = {0u, 0u, 0u, 0u}, s4 = {SENT, SENT, SENT, SENT};
    if (isH) {
#pragma unroll
      for (int i = 0; i < 2; ++i) st64(rs, WS_MODT + (lane & 7) * 4608 + hu * 128 + (2 * i + (r16 >> 3)) * 32 + g * 8, u32x2{SENT, SENT});
    }
    st128(rs, WS_OP + c * 1024 + lane * 16, z4);
    if (lane == 0) st128(rs, WS_ST + c * 128, z4);
    if (isO) {
#pragma unroll
      for (int i = 0; i < 2; ++i) st128(rs, WS_WP + (xg * 144 + u * 16) * 128 + i * 1024 + lane * 16, z4);
      st128(rs, WS_X1 + (lane >> 3) * 9216 + mu * 128 + (lane & 7) * 16, s4);
    }
    if (isF) {
#pragma unroll 1
      for (int i = 0; i < 9; ++i) st128(rs, WS_FP + f * 9216 + i * 1024 + lane * 16, s4);
    }
    WAIT_VM0();
    F[F_INIT] = 1u;
    if (lane == 0) __hip_atomic_store((GAS u64*)(p.ws + WS_ALIVE) + xg * 32 + ci, epoch, __ATOMIC_RELAXED, __HIP_MEMORY_SCOPE_AGENT);
    if (isQ) {
      f32x4 acc[4];
      q_partial(2, acc);
      lds_add(F_WREL + 1, 1); lds_add(F_WREL + 2, 1);
      lds_wait_ge(F, F_P2, 2u);
      const int ls = (g * 8 + (lane & 7)) * 16;
#pragma unroll
      for (int ct = 0; ct < 4; ++ct) {
        const f32x4 a = (*(LAS f32x4*)(L + L_P2 + ct * 512 + ls) + *(LAS f32x4*)(L + L_P2 + 2048 + ct * 512 + ls)) + acc[ct];
        if (r16 < 8) st128(rs, WS_Q + ((2 * xg + (u >> 2)) * 8 + r16) * 1024 + ((u & 3) * 16 + ct * 4 + g) * 16, u32x4{pack2(a[0], a[1]), tag, pack2(a[2], a[3]), tag});
      }
    } else {
      lds_add(F_WREL + 0, 1); lds_add(F_WREL + 1, 1); lds_add(F_WREL + 2, 1);
    }
    {
      u32x4 qv; u32x2 t0v = {0u, 0u}; u32 spins = 0;
      while ((u32)__builtin_amdgcn_s_memrealtime() - t_start < (u32)QGATE) __builtin_amdgcn_s_sleep(2);
      for (;;) {
        MEMBAR();
        qv = ld128(rs, WS_Q + (hkv * 8 + bb) * 1024 + lane * 16);
        t0v = ld64(rs, WS_T0 + xg * 128);
        if (__all(((qv.y ^ tag) | (qv.w ^ tag)) == 0u) || !spin_ok(F, spins)) break;
        __builtin_amdgcn_s_sleep(QPOLL_SLEEP);
      }
      *(LAS u32x2*)(L + L_QS + lane * 8) = u32x2{qv.x, qv.z};
      F[F_T0] = t0v.y == tag ? t0v.x : t_start;
      WAIT_LGKM0();
      F[F_Q] = 1u;
    }
    lds_wait_ge(F, F_LANDED, Wtot + 16u * (u32)(nsl - 8));
    {
      const u32 ehi = (u32)(epoch >> 32);
      u32 spins = 0;
      for (;;) {
        MEMBAR();
        const u32x4 a = ld128(rs, WS_ALIVE + lane * 32), b2 = ld128(rs, WS_ALIVE + lane * 32 + 16);
        const u32 bad = (a.x ^ tag) | (a.y ^ ehi) | (a.z ^ tag) | (a.w ^ ehi) | (b2.x ^ tag) | (b2.y ^ ehi) | (b2.z ^ tag) | (b2.w ^ ehi);
        if (__all(bad == 0u) || !spin_ok(F, spins)) break;
        __builtin_amdgcn_s_sleep(64);
      }
    }
    F[F_ALIVE] = 1u;
    if (c == 0 && lane == 0) __hip_atomic_store((GAS u64*)(p.ws + WS_CTRL), epoch, __ATOMIC_RELAXED, __HIP_MEMORY_SCOPE_AGENT);
    {
      lds_wait_ge(F, F_L, 6u);
      const int hd = lane & 1;
      float M = *(LAS float*)(L + L_MX + hd * 4), Ls = *(LAS float*)(L + L_LW + hd * 4);
#pragma unroll
      for (int i = 1; i < 6; ++i) { M = fmaxf(M, *(LAS float*)(L + L_MX + i * 8 + hd * 4)); Ls += *(LAS float*)(L + L_LW + i * 8 + hd * 4); }
      if (lane < 2) st64(rs, WS_ST + c * 128 + hd * 8, u32x2{vld(M), vld(Ls)});
      u32x2 o; u32 spins = 0;
      for (;;) {
        MEMBAR();
        o = ld64(rs, WS_ST + (c ^ 8) * 128 + hd * 8);
        if (__all((o.x & o.y & 1u) != 0u) || !spin_ok(F, spins)) break;
        __builtin_amdgcn_s_sleep(8);
      }
      const float m0 = __uint_as_float(vld(M)), l0 = __uint_as_float(vld(Ls)), m1 = __uint_as_float(o.x), l1 = __uint_as_float(o.y);
      const float Mg = fmaxf(m0, m1);
      const float e0 = __builtin_amdgcn_exp2f((m0 - Mg) * LOG2E), e1 = __builtin_amdgcn_exp2f((m1 - Mg) * LOG2E);
      const float sc = e0 / (e0 * l0 + e1 * l1);
      osA = __builtin_bit_cast(float, __builtin_amdgcn_readlane(__builtin_bit_cast(int, sc), 0));
      osB = __builtin_bit_cast(float, __builtin_amdgcn_readlane(__builtin_bit_cast(int, sc), 1));
    }
    if (isF) {
      load_cbsb(0, cbp, sbp);
#pragma unroll
      for (int i = 0; i < 5; ++i) { *(LAS u32*)(L + L_RED + i * 256 + lane * 4) = cbp[i]; *(LAS u32*)(L + L_RED + 1280 + i * 256 + lane * 4) = sbp[i]; }
      WAIT_LGKM0();
      F[F_CB0] = 1u;
      load_cbsb(7, cbp, sbp);
      u32x2 gb; u32 spins = 0;
      for (;;) {
        MEMBAR();
        gb = ld64(rs, WS_MODT + (lane & 7) * 4608 + 864 * 4 + f * 8);
        if (__all(umax(gb.x, gb.y) != SENT) || !spin_ok(F, spins)) break;
      }
      pre4a = u32x4{gb.x, gb.y, 0u, 0u};
    } else if (isO) {
      u32 spins = 0;
      for (;;) {
        MEMBAR();
        pre4a = ld128(rs, WS_MODT + (lane & 7) * 4608 + mu * 16);
        if (__all(umax4(pre4a) != SENT) || !spin_ok(F, spins)) break;
      }
      pre4b = *(const GAS u32x4*)(p.x + (lane & 7) * DM + mu * 8);
    }
  } else {
    const int cw = w - 1;
    if (isF) {
      BARRIER();
      const int m = cw & 1, kt = cw >> 1;
      const u32 a0 = L_RING + 36 * m * 1024 + (kt * 192 + 8 * g + qp) * 64 + 8 * pp;
      if (m == 1) lds_add(F_WREL + 0, 1);
      lds_wait_ge(F, F_LANDED, m == 0 ? 36u : 72u);
#pragma unroll
      for (int s = 0; s < 6; ++s) { wf[2 * s] = tr_frag(L, a0 + s * 2048, 4 * 64); wf[2 * s + 1] = tr_frag(L, a0 + s * 2048 + 32, 4 * 64); }
      if (m == 0) lds_add(F_WREL + 0, 1);
      lds_add(F_WREL + 1, 1);
      lds_add(F_WREL + 2, 1);
    } else if (isO) {
      BARRIER();
      if (cw < 4) {
        lds_add(F_WREL + 0, 1);
        const u32 a0 = L_RING + 72 * 1024 + (8 * g + qp) * 128 + cw * 32 + 8 * pp;
        lds_wait_ge(F, F_LANDED, 104u);
#pragma unroll
        for (int s = 0; s < 8; ++s) wf[s] = tr_frag(L, a0 + s * 4096, 4 * 128);
        lds_add(F_WREL + 1, 1);
        lds_wait_ge(F, F_LANDED, 136u);
#pragma unroll
        for (int s = 8; s < 16; ++s) wf[s] = tr_frag(L, a0 + s * 4096, 4 * 128);
        lds_add(F_WREL + 2, 1);
      } else {
        if (isQ) {
          f32x4 acc[4];
          q_partial(cw - 4, acc);
          if (r16 < 8) {
#pragma unroll
            for (int ct = 0; ct < 4; ++ct) *(LAS f32x4*)(L + L_P2 + (cw - 4) * 2048 + ct * 512 + (g * 8 + r16) * 16) = acc[ct];
          }
          lds_add(F_P2, 1);
        } else lds_add(F_WREL + 0, 1);
        lds_add(F_WREL + 1, 1); lds_add(F_WREL + 2, 1);
      }
    } else if (isH) {
      u32 cv[6], xa[5], xb[5];
#pragma unroll
      for (int i = 0; i < 6; ++i) cv[i] = ((const GAS u32*)p.c)[cw * 64 + lane + 384 * i];
      const int rowb = 6 + (cw & 1);
      const GAS u32* xr = (const GAS u32*)p.x + cw * 288;
      const GAS u32* xs = (const GAS u32*)p.x + rowb * 288;
#pragma unroll
      for (int i = 0; i < 4; ++i) { xa[i] = xr[lane + 64 * i]; xb[i] = xs[lane + 64 * i]; }
      xa[4] = xr[256 + (lane & 31)]; xb[4] = xs[256 + (lane & 31)];
      const u32x2 xq = *(const GAS u32x2*)(p.x + (lane & 7) * DM + hu * 16 + g * 4);
      BARRIER();
#pragma unroll
      for (int i = 0; i < 6; ++i) {
        const int e = cw * 64 + lane + 384 * i;
        const float a = bflo(cv[i]), b2 = bfhi(cv[i]);
        *(LAS u32*)(L + L_SC + (e / 288) * ACT_STRIDE + (e % 288) * 4) = pack2(a / (1.0f + __expf(-a)), b2 / (1.0f + __expf(-b2)));
      }
      {
        float v[10], mean_r, sd_r;
#pragma unroll
        for (int i = 0; i < 5; ++i) { v[2 * i] = bflo(xa[i]); v[2 * i + 1] = bfhi(xa[i]); }
        ln_stats(v, lane < 32, mean_r, sd_r);
        if (lane == 0) { *(LAS float*)(L + L_STAT + cw * 8) = mean_r; *(LAS float*)(L + L_STAT + cw * 8 + 4) = sd_r; }
        if (cw < 2) {
#pragma unroll
          for (int i = 0; i < 5; ++i) { v[2 * i] = bflo(xb[i]); v[2 * i + 1] = bfhi(xb[i]); }
          ln_stats(v, lane < 32, mean_r, sd_r);
          if (lane == 0) { *(LAS float*)(L + L_STAT + rowb * 8) = mean_r; *(LAS float*)(L + L_STAT + rowb * 8 + 4) = sd_r; }
        }
      }
      lds_add(F_SC, 1);
      const u32 sb0 = L_SC + (lane & 7) * ACT_STRIDE + g * 16;
      {
        const int m = cw & 1, kt = cw >> 1;
        lds_wait_ge(F, F_SC, 6u);
        const float mean_r = *(LAS float*)(L + L_STAT + (lane & 7) * 8), sd_r = *(LAS float*)(L + L_STAT + (lane & 7) * 8 + 4);
        float n0 = rb(rb(bflo(xq.x) - mean_r) / sd_r), n1 = rb(rb(bfhi(xq.x) - mean_r) / sd_r);
        float n2 = rb(rb(bflo(xq.y) - mean_r) / sd_r), n3 = rb(rb(bfhi(xq.y) - mean_r) / sd_r);
        asm volatile("" : "+v"(n0), "+v"(n1), "+v"(n2), "+v"(n3));
        u32 tag = 0;
        if (cw == 5) tag = get_tag();
        u32x4 bfr[6];
#pragma unroll
        for (int s = 0; s < 6; ++s) bfr[s] = lds128(L, sb0 + (kt * 6 + s) * 64);
        lds_wait_ge(F, F_LANDED, 36u);
        const u32 a0 = L_RING + (m * 18 + kt * 6) * 1024 + (8 * g + qp) * 32 + 8 * pp;
        f32x4 acc = {0, 0, 0, 0}, acc2 = {0, 0, 0, 0};
#pragma unroll
        for (int s = 0; s < 6; s += 2) { acc = mfma(tr_frag(L, a0 + s * 1024, 4 * 32), bfr[s], acc); acc2 = mfma(tr_frag(L, a0 + (s + 1) * 1024, 4 * 32), bfr[s + 1], acc2); }
        acc += acc2;
        if (r16 < 8) *(LAS f32x4*)(L + L_RED + cw * 512 + (g * 8 + r16) * 16) = acc;
        lds_add(F_P1, 1);
        lds_add(F_WREL + 0, 1);
        if (cw == 5) {
          lds_wait_ge(F, F_P1, 6u);
          const int ls = (g * 8 + (lane & 7)) * 16;
          const f32x4 sa = *(LAS f32x4*)(L + L_RED + 0 * 512 + ls) + *(LAS f32x4*)(L + L_RED + 2 * 512 + ls) + *(LAS f32x4*)(L + L_RED + 4 * 512 + ls);
          const f32x4 ca = *(LAS f32x4*)(L + L_RED + 1 * 512 + ls) + *(LAS f32x4*)(L + L_RED + 3 * 512 + ls) + *(LAS f32x4*)(L + L_RED + 5 * 512 + ls);
          const float h0 = rb(rb(n0 * rb(1.0f + rb(ca[0]))) + rb(sa[0])), h1 = rb(rb(n1 * rb(1.0f + rb(ca[1]))) + rb(sa[1]));
          const float h2 = rb(rb(n2 * rb(1.0f + rb(ca[2]))) + rb(sa[2])), h3 = rb(rb(n3 * rb(1.0f + rb(ca[3]))) + rb(sa[3]));
          const u32x4 hu4 = {pack2(h0, h1), tag, pack2(h2, h3), tag};
#pragma unroll
          for (int i = 0; i < REP_H / 2; ++i)
            st128(rs, WS_H + (2 * i + (r16 >> 3)) * 18432 + hu * 512 + (lane & 7) * 64 + g * 16, hu4);
        }
      }
      if (cw < 4) {
        lds_wait_ge(F, F_LANDED, 108u);
        const u32 a0 = L_RING + 36 * 1024 + (8 * g + qp) * 128 + cw * 32 + 8 * pp;
        f32x4 a0c = {0, 0, 0, 0}, a1c = {0, 0, 0, 0};
#pragma unroll 3
        for (int s = 0; s < 18; s += 2) {
          a0c = mfma(tr_frag(L, a0 + s * 4096, 4 * 128), lds128(L, sb0 + s * 64), a0c);
          a1c = mfma(tr_frag(L, a0 + (s + 1) * 4096, 4 * 128), lds128(L, sb0 + (s + 1) * 64), a1c);
        }
        const f32x4 a = a0c + a1c;
        lds_add(F_WREL + 1, 1);
        lds_wait_ge(F, F_INIT, 1u);
        if (r16 < 8) st64(rs, WS_MODT + r16 * 4608 + hu * 128 + cw * 32 + g * 8, u32x2{nosent(pack2(a[0], a[1])), nosent(pack2(a[2], a[3]))});
      } else lds_add(F_WREL + 1, 1);
      lds_add(F_WREL + 2, 1);
    } else {
      BARRIER();
      lds_add(F_WREL + 0, 1); lds_add(F_WREL + 1, 1); lds_add(F_WREL + 2, 1);
    }
    u32x4 qf[4];
    u32 ka[4];
#pragma unroll
    for (int s = 0; s < 4; ++s) ka[s] = r16 * 256 + (((4 * s + g) ^ r16) << 4);
    float mx0 = -INFINITY, mx1 = -INFINITY;
    auto k_read = [&](int q, u32x4* kf) {
      const int pos = (q + roff) & 7;
      lds_wait_ge(F, F_LANDED, Wtot + 16u * (u32)(q + 1));
      const u32 sb = L_RING + pos * 16384;
#pragma unroll
      for (int t = 0; t < 4; ++t)
#pragma unroll
        for (int s = 0; s < 4; ++s) kf[t * 4 + s] = lds128(L, sb + ka[s] + t * 4096);
      WAIT_LGKM0();
      if (lane == 0) F[F_FREE + pos] = (u32)(q + 1);
    };
    auto k_scores = [&](int q, const u32x4* kf) {
      f32x4 acc[4];
#pragma unroll
      for (int t = 0; t < 4; ++t) {
        acc[t] = f32x4{0, 0, 0, 0};
#pragma unroll
        for (int s = 0; s < 4; ++s) acc[t] = mfma(qf[s], kf[t * 4 + s], acc[t]);
      }
      float s0 = g == 0 ? acc[0][0] : g == 1 ? acc[1][0] : g == 2 ? acc[2][0] : acc[3][0];
      float s1 = g == 0 ? acc[0][1] : g == 1 ? acc[1][1] : g == 2 ? acc[2][1] : acc[3][1];
      s0 = rb(rb(s0) * 0.08838834764831845f);
      s1 = rb(rb(s1) * 0.08838834764831845f);
      mx0 = fmaxf(mx0, s0); mx1 = fmaxf(mx1, s1);
      *(LAS u16*)(L + L_SC + q * 256 + lane * 2) = (u16)(__float_as_uint(s0) >> 16);
      *(LAS u16*)(L + L_SC + q * 256 + 128 + lane * 2) = (u16)(__float_as_uint(s1) >> 16);
    };
    {
      u32x4 kf[16];
      k_read(cw, kf);
      lds_wait_ge(F, F_Q, 1u);
      lds_wait_ge(F, F_WREL + 1, 7u); lds_wait_ge(F, F_WREL + 2, 7u);
#pragma unroll
      for (int s = 0; s < 4; ++s) qf[s] = lds128(L, L_QS + (lane & 1) * 256 + s * 64 + g * 16);
      k_scores(cw, kf);
    }
#pragma unroll 1
    for (int q = cw + 6; q < nsl; q += 6) {
      u32x4 kf[16];
      k_read(q, kf);
      k_scores(q, kf);
    }
    {
      mx0 = wave_max(mx0); mx1 = wave_max(mx1);
      if (lane == 0) *(LAS u32x2*)(L + L_MX + cw * 8) = u32x2{__float_as_uint(mx0), __float_as_uint(mx1)};
      lds_add(F_MX, 1);
      lds_wait_ge(F, F_MX, 6u);
#pragma unroll
      for (int i = 0; i < 6; ++i) { mx0 = fmaxf(mx0, *(LAS float*)(L + L_MX + i * 8)); mx1 = fmaxf(mx1, *(LAS float*)(L + L_MX + i * 8 + 4)); }
      float l0 = 0.f, l1 = 0.f;
#pragma unroll 1
      for (int q = cw; q < nsl; q += 6) {
        const float s0 = bf2f(*(LAS u16*)(L + L_SC + q * 256 + lane * 2)), s1 = bf2f(*(LAS u16*)(L + L_SC + q * 256 + 128 + lane * 2));
        const float p0 = __builtin_amdgcn_exp2f((s0 - mx0) * LOG2E), p1 = __builtin_amdgcn_exp2f((s1 - mx1) * LOG2E);
        l0 += p0; l1 += p1;
        *(LAS u16*)(L + L_SC + q * 256 + lane * 2) = (u16)f2bf(p0);
        *(LAS u16*)(L + L_SC + q * 256 + 128 + lane * 2) = (u16)f2bf(p1);
      }
      l0 = wave_sum(l0); l1 = wave_sum(l1);
      if (lane == 0) *(LAS u32x2*)(L + L_LW + cw * 8) = u32x2{__float_as_uint(l0), __float_as_uint(l1)};
      lds_add(F_L, 1);
    }
    f32x4 o[8];
#pragma unroll
    for (int cb = 0; cb < 8; ++cb) o[cb] = f32x4{0, 0, 0, 0};
    const u32 va0 = (8 * g + qp) * 256 + ((pp >> 1) << 4) + 8 * (pp & 1) + (((qp | ((g & 1) << 2)) << 1) << 4);
#pragma unroll 1
    for (int q = cw; q < nsl; q += 6) {
      const int pos = (q + nsl + roff) & 7;
      if (isF && q >= 24 && q < 30) { lds_wait_ge(F, F_ALIVE, 1u); load_cbsb(w, cbp, sbp); }
      const u32x4 pf0 = lds128(L, L_SC + q * 256 + (lane & 1) * 128 + g * 16), pf1 = lds128(L, L_SC + q * 256 + (lane & 1) * 128 + 64 + g * 16);
      lds_wait_ge(F, F_LANDED, Wtot + 16u * (u32)(q + nsl + 1));
      const u32 sb = L_RING + pos * 16384 + va0;
      u32x4 vf[8], vg2[8];
#pragma unroll
      for (int cb = 0; cb < 8; ++cb) vf[cb] = tr_frag(L, sb ^ (cb << 5), 1024);
#pragma unroll
      for (int cb = 0; cb < 8; ++cb) vg2[cb] = tr_frag(L, (sb ^ (cb << 5)) + 8192, 1024);
      WAIT_LGKM0();
      if (lane == 0) F[F_FREE + pos] = (u32)(q + nsl + 1);
#pragma unroll
      for (int cb = 0; cb < 8; ++cb) o[cb] = mfma(pf0, vf[cb], o[cb]);
#pragma unroll
      for (int cb = 0; cb < 8; ++cb) o[cb] = mfma(pf1, vg2[cb], o[cb]);
    }
    if (g == 0) {
#pragma unroll
      for (int cb = 0; cb < 8; ++cb) *(LAS u32x2*)(L + L_ACT + cw * 1024 + (cb * 16 + r16) * 8) = u32x2{__float_as_uint(o[cb][0]), __float_as_uint(o[cb][1])};
    }
    lds_add(F_OPART, 1);
  }
  if (w == 7) {
    lds_wait_ge(F, F_OPART, 6u);
    f32x4 O = *(LAS f32x4*)(L + L_ACT + lane * 16);
#pragma unroll
    for (int i = 1; i < 6; ++i) O += *(LAS f32x4*)(L + L_ACT + i * 1024 + lane * 16);
    st128(rs, WS_OP + c * 1024 + lane * 16, u32x4{vld(O[0] * osA), vld(O[1] * osB), vld(O[2] * osA), vld(O[3] * osB)});
    F[F_OPUB] = 1u;
  }
  if (isO) {
    constexpr int L_OS = L_SC;
    u32x4 ov[4];
    bool got[4] = {false, false, false, false};
    u32 spins = 0;
    lds_wait_ge(F, F_ALIVE, 1u);
    lds_wait_ge(F, F_OPUB, 1u); __builtin_amdgcn_s_sleep(DELAY_A);
    for (;;) {
      MEMBAR();
#pragma unroll
      for (int i = 0; i < 4; ++i) if (!got[i]) ov[i] = ld128(rs, WS_OP + (xg + 8 * ((i >> 1) * 16 + 2 * w + (i & 1))) * 1024 + lane * 16);
      bool all = true;
#pragma unroll
      for (int i = 0; i < 4; ++i) if (!got[i]) {
        got[i] = __all((ov[i].x & ov[i].y & ov[i].z & ov[i].w & 1u) != 0u);
        all = all && got[i];
      }
      if (all || !spin_ok(F, spins)) break;
    }
#pragma unroll
    for (int hk = 0; hk < 2; ++hk) {
      const f32x4 s = __builtin_bit_cast(f32x4, ov[2 * hk]) + __builtin_bit_cast(f32x4, ov[2 * hk + 1]);
      *(LAS u32*)(L + L_OS + w * 1040 + hk * 512 + lane * 4) = pack2(s[0], s[2]);
      *(LAS u32*)(L + L_OS + w * 1040 + hk * 512 + 256 + lane * 4) = pack2(s[1], s[3]);
    }
    BARRIER();
    if (w >= 1 && w <= 4) {
      const int ct = w - 1;
      const u32 ob = L_OS + (lane & 7) * 1040 + g * 16;
      u32x4 bfr[16];
#pragma unroll
      for (int s = 0; s < 16; ++s) bfr[s] = lds128(L, ob + s * 64);
      f32x4 a0 = {0, 0, 0, 0}, a1 = {0, 0, 0, 0}, a2 = {0, 0, 0, 0}, a3 = {0, 0, 0, 0};
#pragma unroll
      for (int s = 0; s < 16; s += 4) { a0 = mfma(wf[s], bfr[s], a0); a1 = mfma(wf[s + 1], bfr[s + 1], a1); a2 = mfma(wf[s + 2], bfr[s + 2], a2); a3 = mfma(wf[s + 3], bfr[s + 3], a3); }
      const f32x4 a = (a0 + a1) + (a2 + a3);
      if (r16 < 8) st128(rs, WS_WP + (xg * 144 + (u * 4 + ct) * 4 + g) * 128 + r16 * 16, u32x4{vld(a[0]), vld(a[1]), vld(a[2]), vld(a[3])});
    } else if (w == 7) {
      u32x4 pa, pb; u32 spins2 = 0;
      __builtin_amdgcn_s_sleep(DELAY_B);
      const int wo_ = WS_WP + ((lane >> 3) * 144 + 2 * mu) * 128 + (lane & 7) * 16;
      for (;;) {
        MEMBAR();
        pa = ld128(rs, wo_); pb = ld128(rs, wo_ + 128);
        if (__all((pa.x & pa.y & pa.z & pa.w & pb.x & pb.y & pb.z & pb.w & 1u) != 0u) || !spin_ok(F, spins2)) break;
      }
      float a[8];
      a[0] = __uint_as_float(pa.x); a[1] = __uint_as_float(pa.y); a[2] = __uint_as_float(pa.z); a[3] = __uint_as_float(pa.w);
      a[4] = __uint_as_float(pb.x); a[5] = __uint_as_float(pb.y); a[6] = __uint_as_float(pb.z); a[7] = __uint_as_float(pb.w);
#pragma unroll
      for (int i = 0; i < 8; ++i) a[i] = sum_x8_16_32(a[i]);
      u32x4 xo;
#pragma unroll
      for (int i = 0; i < 4; ++i) {
        const u32 ap = pack2(a[2 * i], a[2 * i + 1]);
        const u32 tp = pack2(bflo(pre4a[i]) * bflo(ap), bfhi(pre4a[i]) * bfhi(ap));
        xo[i] = nosent(pack2(bflo(pre4b[i]) + bflo(tp), bfhi(pre4b[i]) + bfhi(tp)));
      }
      st128(rs, WS_X1 + (lane >> 3) * 9216 + mu * 128 + (lane & 7) * 16, xo);
    }
  } else if (isF) {
    const u32 n0 = Wtot + 32u * (u32)nsl;
    if (w == 7) {
      u32x4 pv; u32 spins = 0;
      __builtin_amdgcn_s_sleep(DELAY_W);
      for (;;) {
        MEMBAR();
        pv = ld128(rs, WS_WP + ((lane >> 3) * 144 + f) * 128 + (lane & 7) * 16);
        if (__all((pv.x & pv.y & pv.z & pv.w & 1u) != 0u) || !spin_ok(F, spins)) break;
      }
      F[F_X1] = 1u;
    } else lds_wait_ge(F, F_X1, 1u);
    __builtin_amdgcn_s_sleep(DELAY_C);
    u32x2 x1p = {0u, 0u};
    {
      if (w != 0) {
        const int e0 = (w - 1) * 64 + lane, xb = WS_X1 + xg * 9216;
        u32x4 xv, xw = {0u, 0u, 0u, 0u}; u32 spins = 0;
        for (;;) {
          MEMBAR();
          xv = ld128(rs, xb + e0 * 16);
          if (w <= 2) xw = ld128(rs, xb + (448 + e0) * 16);
          if (__all(umax(umax4(xv), umax4(xw)) != SENT) || !spin_ok(F, spins)) break;
        }
        const u32 a = L_ACT + (e0 & 7) * ACT_STRIDE + (e0 >> 3) * 16;
        *(LAS u32x4*)(L + a) = xv;
        if (w <= 2) *(LAS u32x4*)(L + a + 56 * 16) = xw;
      }
      BARRIER();
      u32 xr[5];
#pragma unroll
      for (int i = 0; i < 4; ++i) xr[i] = *(LAS u32*)(L + L_ACT + w * ACT_STRIDE + (lane + 64 * i) * 4);
      xr[4] = *(LAS u32*)(L + L_ACT + w * ACT_STRIDE + (256 + (lane & 31)) * 4);
      if (w == 7) x1p = *(LAS u32x2*)(L + L_ACT + (lane & 7) * ACT_STRIDE + f * 8);
      float v[10];
#pragma unroll
      for (int i = 0; i < 5; ++i) { v[2 * i] = bflo(xr[i]); v[2 * i + 1] = bfhi(xr[i]); }
      float s = (v[0] + v[1]) + (v[2] + v[3]) + ((v[4] + v[5]) + (v[6] + v[7]));
      if (lane < 32) s += v[8] + v[9];
      const float mean = wave_sum(s) * (1.0f / DM);
      float q2 = 0.f, q3 = 0.f;
#pragma unroll
      for (int i = 0; i < 4; ++i) { const float d0 = v[2 * i] - mean, d1 = v[2 * i + 1] - mean; q2 += d0 * d0; q3 += d1 * d1; }
      if (lane < 32) { const float d0 = v[8] - mean, d1 = v[9] - mean; q2 += d0 * d0; q3 += d1 * d1; }
      const float var = wave_sum(q2 + q3) * (1.0f / DM);
      const float mean_r = rb(mean);
      const float rinv = __builtin_amdgcn_rcpf(rb(__builtin_amdgcn_sqrtf(rb(rb(var) + 1e-5f))));
#pragma unroll
      for (int i = 0; i < 5; ++i) {
        const u32 dp = pack2(v[2 * i] - mean_r, v[2 * i + 1] - mean_r);
        const u32 np = pack2(bflo(dp) * rinv, bfhi(dp) * rinv);
        const u32 tp = pack2(bflo(np) * bflo(cbp[i]), bfhi(np) * bfhi(cbp[i]));
        const u32 hp = pack2(bflo(tp) + bflo(sbp[i]), bfhi(tp) + bfhi(sbp[i]));
        if (i < 4) *(LAS u32*)(L + L_H2 + w * ACT_STRIDE + (lane + 64 * i) * 4) = hp;
        else if (lane < 32) *(LAS u32*)(L + L_H2 + w * ACT_STRIDE + (256 + lane) * 4) = hp;
      }
    }
    if (w == 0) { WAIT_VM0(); F[F_LANDED] = nfin; }
    BARRIER();
    if (w == 7) {
      BARRIER();
      BARRIER();
      const int off = WS_FP + (lane >> 3) * 9216 + ((f >> 3) * 4 + (f & 3)) * 128 + (lane & 7) * 16 + ((f >> 2) & 1) * 8;
      u32x2 e[18]; u32 spins = 0;
      u32 slo = 0x00003f80u, shi = 0x3f800000u;
      asm volatile("" : "+v"(slo), "+v"(shi));
      __builtin_amdgcn_s_sleep(DELAY_E);
      for (;;) {
        MEMBAR();
#pragma unroll
        for (int i = 0; i < 18; ++i) e[i] = ld64(rs, off + i * 8 * 9216);
        u32 mx = 0;
#pragma unroll
        for (int i = 0; i < 18; ++i) mx = umax(mx, umax(e[i].x, e[i].y));
        if (__all(mx != SENT) || !spin_ok(F, spins)) break;
      }
      f32x4 s = {0, 0, 0, 0}, s1 = {0, 0, 0, 0};
#pragma unroll
      for (int i = 0; i < 18; i += 2) {
        s[0] = add_sel(e[i].x, slo, s[0]); s[1] = add_sel(e[i].x, shi, s[1]); s[2] = add_sel(e[i].y, slo, s[2]); s[3] = add_sel(e[i].y, shi, s[3]);
        s1[0] = add_sel(e[i + 1].x, slo, s1[0]); s1[1] = add_sel(e[i + 1].x, shi, s1[1]); s1[2] = add_sel(e[i + 1].y, slo, s1[2]); s1[3] = add_sel(e[i + 1].y, shi, s1[3]);
      }
      s += s1;
      const u32 dA = pack2(sum_x8_16_32(s[0]), sum_x8_16_32(s[1])), dB = pack2(sum_x8_16_32(s[2]), sum_x8_16_32(s[3]));
      const u32 tA = pack2(bflo(pre4a.x) * bflo(dA), bfhi(pre4a.x) * bfhi(dA)), tB = pack2(bflo(pre4a.y) * bflo(dB), bfhi(pre4a.y) * bfhi(dB));
      if (lane < 8) *(GAS u32x2*)(p.out + lane * DM + f * 4) = u32x2{pack2(bflo(x1p.x) + bflo(tA), bfhi(x1p.x) + bfhi(tA)), pack2(bflo(x1p.y) + bflo(tB), bfhi(x1p.y) + bfhi(tB))};
    } else {
      const int cw = w - 1;
      if (w >= 1) {
        const int kt = cw >> 1;
        const u32 hb = L_H2 + (lane & 7) * ACT_STRIDE + kt * 384 + g * 16;
        u32x4 bfr[6];
#pragma unroll
        for (int s = 0; s < 6; ++s) bfr[s] = lds128(L, hb + s * 64);
        f32x4 a0 = {0, 0, 0, 0}, a1 = {0, 0, 0, 0}, a2 = {0, 0, 0, 0}, a3 = {0, 0, 0, 0};
#pragma unroll
        for (int s = 0; s < 6; s += 2) { a0 = mfma(wf[2 * s], bfr[s], a0); a1 = mfma(wf[2 * s + 1], bfr[s], a1); a2 = mfma(wf[2 * s + 2], bfr[s + 1], a2); a3 = mfma(wf[2 * s + 3], bfr[s + 1], a3); }
        a0 += a2; a1 += a3;
        if (r16 < 8) { *(LAS f32x4*)(L + L_R2 + cw * 1024 + (g * 8 + r16) * 16) = a0; *(LAS f32x4*)(L + L_R2 + cw * 1024 + 512 + (g * 8 + r16) * 16) = a1; }
        lds_wait_ge(F, F_LANDED, n0 + 36u);
        const u32 d0 = L_RING + posd * 16384 + (8 * g + qp) * 1152 + cw * 192 + 8 * pp;
#pragma unroll
        for (int t = 0; t < 6; ++t) wf[12 + t] = tr_frag(L, d0 + t * 32, 4 * 1152);
      }
      BARRIER();
      if (w < 4) {
        const int ct = w >> 1, e = (w & 1) * 64 + lane;
        const u32 a = L_R2 + ct * 512 + e * 4;
        const float gg = rb(*(LAS float*)(L + a) + *(LAS float*)(L + a + 2048) + *(LAS float*)(L + a + 4096));
        const float uu = rb(*(LAS float*)(L + a + 1024) + *(LAS float*)(L + a + 3072) + *(LAS float*)(L + a + 5120));
        const float inner = 0.7978845608028654f * (gg + 0.044715f * gg * gg * gg);
        const float th = 1.0f - 2.0f * __builtin_amdgcn_rcpf(1.0f + __expf(2.0f * inner));
        const float ge = rb(0.5f * gg * (1.0f + th));
        const int ls = e >> 2, rr = e & 3, b_ = ls & 7, g_ = ls >> 3;
        *(LAS u16*)(L + L_HH + b_ * 80 + (ct * 16 + g_ * 4 + rr) * 2) = (u16)f2bf(ge * uu);
      }
      BARRIER();
      if (w >= 1) {
        const u32x4 bfr = lds128(L, L_HH + (lane & 7) * 80 + g * 16);
        f32x4 a[6];
#pragma unroll
        for (int t = 0; t < 6; ++t) a[t] = mfma(wf[12 + t], bfr, f32x4{0, 0, 0, 0});
#pragma unroll
        for (int t = 0; t < 6; t += 2)
          if (r16 < 8) st128(rs, WS_FP + f * 9216 + ((cw * 3 + (t >> 1)) * 4 + g) * 128 + r16 * 16,
                             u32x4{nosent(pack2(a[t][0], a[t][1])), nosent(pack2(a[t][2], a[t][3])), nosent(pack2(a[t + 1][0], a[t + 1][1])), nosent(pack2(a[t + 1][2], a[t + 1][3]))});
      }
    }
  }
}

}

extern "C" void kernel_launch(void* const* d_in, const int* in_sizes, int n_in, void* d_out, int out_size, void* d_ws, size_t ws_size,
                              hipStream_t stream) {
  (void)in_sizes; (void)n_in; (void)out_size; (void)ws_size;
  Params p;
  p.x = (const u16*)d_in[0];
  p.kc = (const unsigned char*)d_in[1];
  p.vc = (const unsigned char*)d_in[2];
  p.wq = (const unsigned char*)d_in[3];
  p.wo = (const unsigned char*)d_in[4];
  p.wg = (const unsigned char*)d_in[6];
  p.wu = (const unsigned char*)d_in[7];
  p.wd = (const unsigned char*)d_in[8];
  p.c = (const u16*)d_in[9];
  p.wmod = (const unsigned char*)d_in[10];
  p.out = (u16*)d_out;
  p.ws = (unsigned char*)d_ws;
  (void)hipFuncSetAttribute((const void*)fused_block, hipFuncAttributeMaxDynamicSharedMemorySize, LDS_BYTES);
  hipLaunchKernelGGL(fused_block, dim3(NCU), dim3(NT), LDS_BYTES, stream, p);
}
```

```cpp
#include <hip/hip_runtime.h>
#include <stdint.h>
#include <type_traits>
#pragma clang fp contract(off)

#define LAS __attribute__((address_space(3)))
#define GAS __attribute__((address_space(1)))
typedef unsigned u32;
typedef unsigned long long u64;
typedef unsigned short u16;
typedef u32 u32x4 __attribute__((ext_vector_type(4)));
typedef u32 u32x2 __attribute__((ext_vector_type(2)));
typedef float f32x4 __attribute__((ext_vector_type(4)));
typedef __bf16 bf16x8 __attribute__((ext_vector_type(8)));
typedef short s16x4 __attribute__((ext_vector_type(4)));

namespace {

constexpr int DM = 576, HKV = 16, SK = 8192;
constexpr int NCU = 256, NT = 512;

constexpr int INFLW = 320;
constexpr int P1W = 12;
constexpr int WQW = 16, WOW = 16;
constexpr int BASE_LVL = 2, EARLY_LVL = 4, EARLY_Q = 16;
constexpr int BOOST1 = 2, BOOST2 = 4, SLOW1 = 1;
constexpr int SCHED_TE = 7700;
constexpr int SCHED_TAU = 876;
constexpr int SCHED_Q0 = 17, SCHED_L1 = 30, SCHED_L2 = 70, SCHED_L3 = 120;
constexpr int SHIFT_O = 1, SHIFT_H = 2;
constexpr int HGATE = 800, QGATE = 1000;
constexpr int DELAY_A = 12, DELAY_B = 14, DELAY_W = 36, DELAY_C = 16, DELAY_E = 24;
constexpr int HPOLL_SLEEP = 2, QPOLL_SLEEP = 2;
#define AUX_KV "nt"
#define AUX_W "nt"

constexpr int REP_H = 8;
constexpr int WS_CTRL = 0;
constexpr int WS_ALIVE = 4096;
constexpr int WS_ST = 8192;
constexpr int WS_MODT = WS_ST + 256 * 128;
constexpr int WS_X1 = WS_MODT + 36864;
constexpr int WS_H = WS_X1 + 8 * 9216;
constexpr int WS_Q = WS_H + REP_H * 18432;
constexpr int WS_OP = WS_Q + 131072;
constexpr int WS_WP = WS_OP + 262144;
constexpr int WS_FP = WS_WP + 147456;
constexpr int WS_T0 = WS_FP + 144 * 9216;
constexpr int WS_END = WS_T0 + 8 * 128;

constexpr int MAXSL = 64 + (SHIFT_O > SHIFT_H ? SHIFT_O : SHIFT_H);
constexpr int L_RING = 0;
constexpr int L_SC = 131072;
constexpr int ACT_STRIDE = 1168;
constexpr int L_P2 = L_SC + 8192;
constexpr int L_H2 = L_SC;
constexpr int L_R2 = L_SC + 9344;
constexpr int L_HH = L_R2 + 6144;
constexpr int L_ACT = L_SC + (MAXSL * 256 > 16384 ? MAXSL * 256 : 16384);
constexpr int L_QS = L_ACT + 9344;
constexpr int L_RED = L_QS + 512;
constexpr int L_STAT = L_RED + 3072;
constexpr int L_MX = L_STAT + 64;
constexpr int L_LW = L_MX + 64;
constexpr int L_FLG = L_LW + 64;
constexpr int LDS_BYTES = 163840;
static_assert(L_FLG + 256 <= LDS_BYTES && L_HH + 640 <= L_ACT, "lds");
constexpr int F_LANDED = 0, F_FREE = 1  , F_WREL = 9  , F_SC = 12, F_P1 = 13, F_INIT = 14, F_Q = 15, F_ABORT = 16, F_ALIVE = 17,
              F_P2 = 18, F_MX = 19, F_L = 20, F_X1 = 21, F_TAGOK = 22, F_TAG = 23, F_OPART = 24, F_CB0 = 25, F_T0 = 26, F_OPUB = 27;

constexpr u32 SENT = 0xFFFFFFFFu;
constexpr u32 LDS_SPIN_CAP = 1u << 21;
constexpr u32 GLB_SPIN_CAP = 1u << 17;
constexpr float LOG2E = 1.4426950408889634f;

struct Params {
  const u16* x; const unsigned char* kc; const unsigned char* vc; const unsigned char* wq; const unsigned char* wo;
  const unsigned char* wg; const unsigned char* wu; const unsigned char* wd; const u16* c; const unsigned char* wmod;
  u16* out; unsigned char* ws;
};

__device__ __forceinline__ float bf2f(u32 v16) { return __uint_as_float(v16 << 16); }
__device__ __forceinline__ float bflo(u32 d) { return __uint_as_float(d << 16); }
__device__ __forceinline__ float bfhi(u32 d) { return __uint_as_float(d & 0xffff0000u); }
__device__ __forceinline__ float rb(float f) { return (float)(__bf16)f; }
__device__ __forceinline__ u32 f2bf(float f) { return (u32)__builtin_bit_cast(u16, (__bf16)f); }
typedef float f32x2 __attribute__((ext_vector_type(2)));
typedef __bf16 bf16x2 __attribute__((ext_vector_type(2)));
__device__ __forceinline__ u32 pack2(float lo, float hi) { return __builtin_bit_cast(u32, __builtin_convertvector(f32x2{lo, hi}, bf16x2)); }
__device__ __forceinline__ u32 nosent(u32 p) { return p < 0xFFFEFFFFu ? p : 0xFFFEFFFFu; }
__device__ __forceinline__ u32 umax(u32 a, u32 b) { return a > b ? a : b; }
__device__ __forceinline__ u32 umax4(u32x4 v) { return umax(umax(v.x, v.y), umax(v.z, v.w)); }
__device__ __forceinline__ float add_sel(u32 v, u32 sel, float acc) { return __builtin_amdgcn_fdot2_f32_bf16(__builtin_bit_cast(bf16x2, v), __builtin_bit_cast(bf16x2, sel), acc, false); }
__device__ __forceinline__ u32 vld(float f) { return __float_as_uint(f) | 1u; }
__device__ __forceinline__ f32x4 mfma(u32x4 a, u32x4 b, f32x4 c) {
  return __builtin_amdgcn_mfma_f32_16x16x32_bf16(__builtin_bit_cast(bf16x8, a), __builtin_bit_cast(bf16x8, b), c, 0, 0, 0);
}
__device__ __forceinline__ u32x4 mk_srd(const void* p, u32 bytes) {
  const u64 a = (u64)p;
  u32x4 s;
  s.x = __builtin_amdgcn_readfirstlane((u32)a);
  s.y = __builtin_amdgcn_readfirstlane((u32)(a >> 32) & 0xffffu);
  s.z = __builtin_amdgcn_readfirstlane(bytes);
  s.w = 0x00020000u;
  return s;
}
#define MEMBAR() asm volatile("" ::: "memory")
#define WAIT_LGKM0() asm volatile("s_waitcnt lgkmcnt(0)" ::: "memory")
#define WAIT_VM0() asm volatile("s_waitcnt vmcnt(0)" ::: "memory")
#define BARRIER() do { asm volatile("s_waitcnt lgkmcnt(0)" ::: "memory"); __builtin_amdgcn_s_barrier(); asm volatile("" ::: "memory"); } while (0)

#define DMA1(AUX, srd, voff, soff, ldsaddr)                                                                      \
  do { u32 keep_;                                                                                                \
    asm volatile("s_mov_b32 %0, m0\n\ts_mov_b32 m0, %1\n\ts_nop 0\n\t"                                           \
                 "buffer_load_dwordx4 %4, %2, %3 offen " AUX " lds\n\ts_mov_b32 m0, %0"                          \
                 : "=&s"(keep_) : "s"(ldsaddr), "s"(srd), "s"(soff), "v"(voff) : "memory"); } while (0)
#define DMA4(AUX, srd, v0, v1, v2, v3, soff, ldsaddr)                                                            \
  do { u32 keep_;                                                                                                \
    asm volatile("s_mov_b32 %0, m0\n\ts_mov_b32 m0, %1\n\ts_nop 0\n\t"                                           \
                 "buffer_load_dwordx4 %4, %2, %3 offen " AUX " lds\n\t"                                          \
                 "buffer_load_dwordx4 %5, %2, %3 offen offset:1024 " AUX " lds\n\t"                              \
                 "buffer_load_dwordx4 %6, %2, %3 offen offset:2048 " AUX " lds\n\t"                              \
                 "buffer_load_dwordx4 %7, %2, %3 offen offset:3072 " AUX " lds\n\ts_mov_b32 m0, %0"              \
                 : "=&s"(keep_) : "s"(ldsaddr), "s"(srd), "s"(soff), "v"(v0), "v"(v1), "v"(v2), "v"(v3) : "memory"); } while (0)

#define DMA4W(AUX, srd, voff, so, step, ldsaddr)                                                                 \
  do { u32 keep_; const u32 d_ = (step) - 1024u, s0_ = (so), s1_ = s0_ + d_, s2_ = s1_ + d_, s3_ = s2_ + d_;     \
    asm volatile("s_mov_b32 %0, m0\n\ts_mov_b32 m0, %1\n\ts_nop 0\n\t"                                          \
                 "buffer_load_dwordx4 %7, %2, %3 offen " AUX " lds\n\t"                                          \
                 "buffer_load_dwordx4 %7, %2, %4 offen offset:1024 " AUX " lds\n\t"                              \
                 "buffer_load_dwordx4 %7, %2, %5 offen offset:2048 " AUX " lds\n\t"                              \
                 "buffer_load_dwordx4 %7, %2, %6 offen offset:3072 " AUX " lds\n\ts_mov_b32 m0, %0"              \
                 : "=&s"(keep_) : "s"(ldsaddr), "s"(srd), "s"(s0_), "s"(s1_), "s"(s2_), "s"(s3_), "v"(voff) : "memory"); } while (0)

template <int CTRL> __device__ __forceinline__ float dpp(float x) {
  return __builtin_bit_cast(float, __builtin_amdgcn_mov_dpp(__builtin_bit_cast(int, x), CTRL, 0xf, 0xf, true));
}
constexpr int ROR8 = 0x128, ROR4 = 0x124, ROR2 = 0x122, ROR1 = 0x121;
__device__ __forceinline__ float xrow_max(float x) {
  auto s = __builtin_amdgcn_permlane16_swap(__float_as_uint(x), __float_as_uint(x), false, false);
  x = fmaxf(__uint_as_float(s[0]), __uint_as_float(s[1]));
  auto t = __builtin_amdgcn_permlane32_swap(__float_as_uint(x), __float_as_uint(x), false, false);
  return fmaxf(__uint_as_float(t[0]), __uint_as_float(t[1]));
}
__device__ __forceinline__ float xrow_sum(float x) {
  auto s = __builtin_amdgcn_permlane16_swap(__float_as_uint(x), __float_as_uint(x), false, false);
  x = __uint_as_float(s[0]) + __uint_as_float(s[1]);
  auto t = __builtin_amdgcn_permlane32_swap(__float_as_uint(x), __float_as_uint(x), false, false);
  return __uint_as_float(t[0]) + __uint_as_float(t[1]);
}
__device__ __forceinline__ float wave_max(float x) {
  x = fmaxf(x, dpp<ROR8>(x)); x = fmaxf(x, dpp<ROR4>(x)); x = fmaxf(x, dpp<ROR2>(x)); x = fmaxf(x, dpp<ROR1>(x));
  return xrow_max(x);
}
__device__ __forceinline__ float wave_sum(float x) {
  x += dpp<ROR8>(x); x += dpp<ROR4>(x); x += dpp<ROR2>(x); x += dpp<ROR1>(x);
  return xrow_sum(x);
}
__device__ __forceinline__ float sum_x8_16_32(float x) {
  x += dpp<ROR8>(x);
  return xrow_sum(x);
}

typedef volatile LAS u32* flagp;
__device__ __forceinline__ void lds_wait_ge(flagp F, int idx, u32 need) {
  (void)F;
  const u32 a = L_FLG + idx * 4, ab = L_FLG + F_ABORT * 4;
  u32 v, z, cnt;
  asm volatile("s_mov_b32 %2, 0\n"
               "1:\n\tds_read_b32 %0, %3\n\tds_read_b32 %1, %4\n\ts_waitcnt lgkmcnt(0)\n\t"
               "v_cmp_le_u32 vcc, %5, %0\n\ts_cbranch_vccnz 2f\n\t"
               "v_cmp_ne_u32 vcc, 0, %1\n\ts_cbranch_vccnz 2f\n\t"
               "s_sleep 1\n\ts_add_u32 %2, %2, 1\n\ts_cmp_lt_u32 %2, %6\n\ts_cbranch_scc1 1b\n\t"
               "v_mov_b32 %0, 1\n\tds_write_b32 %4, %0\n"
               "2:"
               : "=&v"(v), "=&v"(z), "=&s"(cnt) : "v"(a), "v"(ab), "v"(need), "s"(LDS_SPIN_CAP) : "vcc", "scc", "memory");
}
__device__ __forceinline__ void lds_add(int idx, u32 n) {
  const u32 a = L_FLG + idx * 4;
  asm volatile("s_waitcnt lgkmcnt(0)\n\ts_mov_b64 vcc, exec\n\ts_mov_b64 exec, 1\n\tds_add_u32 %0, %1\n\ts_mov_b64 exec, vcc"
               :: "v"(a), "v"(n) : "vcc", "memory");
}
__device__ __forceinline__ bool spin_ok(flagp F, u32& spins) {
  if (++spins > GLB_SPIN_CAP) { F[F_ABORT] = 2u; return false; }
  if ((spins & 255u) == 0u && F[F_ABORT] != 0u) return false;
  return true;
}

constexpr int SC1 = 16;
typedef __amdgpu_buffer_rsrc_t rsrc_t;
__device__ __forceinline__ u32x4 ld128(rsrc_t r, int off) { return __builtin_amdgcn_raw_buffer_load_b128(r, off, 0, SC1); }
__device__ __forceinline__ u32x2 ld64(rsrc_t r, int off) { return __builtin_amdgcn_raw_buffer_load_b64(r, off, 0, SC1); }
__device__ __forceinline__ u32 ld32(rsrc_t r, int off) { return __builtin_amdgcn_raw_buffer_load_b32(r, off, 0, SC1); }
__device__ __forceinline__ void st128(rsrc_t r, int off, u32x4 v) { __builtin_amdgcn_raw_buffer_store_b128(v, r, off, 0, SC1); }
__device__ __forceinline__ void st64(rsrc_t r, int off, u32x2 v) { __builtin_amdgcn_raw_buffer_store_b64(v, r, off, 0, SC1); }

__device__ __forceinline__ u32x4 tr_frag(LAS unsigned char* L, u32 addr, u32 stride4) {
  const s16x4 a = __builtin_amdgcn_ds_read_tr16_b64_v4i16((LAS s16x4*)(L + addr));
  const s16x4 b = __builtin_amdgcn_ds_read_tr16_b64_v4i16((LAS s16x4*)(L + addr + stride4));
  const u32x2 a2 = __builtin_bit_cast(u32x2, a), b2 = __builtin_bit_cast(u32x2, b);
  return u32x4{a2.x, a2.y, b2.x, b2.y};
}
__device__ __forceinline__ u32x4 lds128(LAS unsigned char* L, u32 addr) { return *(LAS u32x4*)(L + addr); }

__device__ __forceinline__ void ln_stats(const float (&v)[10], bool tail, float& mean_r, float& sd_r) {
  float s = 0.f;
#pragma unroll
  for (int i = 0; i < 8; ++i) s += v[i];
  if (tail) s += v[8] + v[9];
  const float mean = wave_sum(s) * (1.0f / DM);
  float q = 0.f;
#pragma unroll
  for (int i = 0; i < 8; ++i) { const float d = v[i] - mean; q += d * d; }
  if (tail) { const float d0 = v[8] - mean, d1 = v[9] - mean; q += d0 * d0 + d1 * d1; }
  const float var = wave_sum(q) * (1.0f / DM);
  mean_r = rb(mean);
  sd_r = rb(sqrtf(rb(rb(var) + 1e-5f)));
}

__global__ void __launch_bounds__(NT, 2) fused_block(Params p) {
  extern __shared__ __attribute__((aligned(16))) unsigned char smem[];
  LAS unsigned char* L = (LAS unsigned char*)smem;
  flagp F = (flagp)(L + L_FLG);
  const int tid = threadIdx.x, lane = tid & 63, w = __builtin_amdgcn_readfirstlane(tid >> 6);
  const u32 t_start = (u32)__builtin_amdgcn_s_memrealtime();
  const int c = blockIdx.x, xg = c & 7, ci = c >> 3;
  const int hkv = 2 * xg + (ci >> 4), cj = ci & 15, bb = cj >> 1, half = cj & 1;
  const int pj = ci >> 1;
  const bool isF = (half == 0) | (pj >= 14), isO = !isF & (pj < 9);
  const int hu = xg * 5 + (pj - 9);
  const bool isH = !isF & !isO & (hu < 36);
  const int f = xg * 18 + pj + 2 * half;
  const int u = pj;
  const int mu = xg * 9 + u;
  const bool isQ = isO & (u < 8);
  const int shift = (int)(pj < 8) * SHIFT_O + (int)((pj >= 9) & (pj < 14) & (hu < 36)) * SHIFT_H;
  const int nsl = 64 + shift - 2 * shift * half;
  const int key0 = half * (64 + shift) * 64;
  const u32 Wtot = 72u * (u32)isF + 136u * (u32)isO + 108u * (u32)isH;
  const int roff = (int)isF * (5 + (int)(shift == 1)) + (int)isH * 7;
  const int posd = (2 * nsl + roff) & 7;
  const int g = lane >> 4, r16 = lane & 15, qp = (lane & 15) >> 2, pp = lane & 3;
  const rsrc_t rs = __builtin_amdgcn_make_buffer_rsrc((void*)p.ws, 0, WS_END, 0x00020000);

  u32x4 wf[18];
  u32 cbp[5], sbp[5];
  u32x4 pre4a, pre4b;
  float osA = 0.f, osB = 0.f;
  u32 nfin = 0;

  auto load_cbsb = [&](int row, u32 (&cb)[5], u32 (&sb)[5]) {
    u32 spins = 0;
    for (;;) {
      MEMBAR();
      const int base = WS_MODT + row * 4608;
#pragma unroll
      for (int i = 0; i < 4; ++i) { sb[i] = ld32(rs, base + (288 + lane + 64 * i) * 4); cb[i] = ld32(rs, base + (576 + lane + 64 * i) * 4); }
      sb[4] = ld32(rs, base + (288 + 256 + (lane & 31)) * 4); cb[4] = ld32(rs, base + (576 + 256 + (lane & 31)) * 4);
      u32 mx = 0;
#pragma unroll
      for (int i = 0; i < 5; ++i) mx = umax(mx, umax(sb[i], cb[i]));
      if (__all(mx != SENT) || !spin_ok(F, spins)) break;
    }
#pragma unroll
    for (int i = 0; i < 5; ++i) cb[i] = pack2(1.0f + bflo(cb[i]), 1.0f + bfhi(cb[i]));
  };
  auto get_tag = [&]() { lds_wait_ge(F, F_TAGOK, 1u); return (u32)F[F_TAG]; };
  auto q_partial = [&](int kt, f32x4 (&acc)[4]) {
    lds_wait_ge(F, F_LANDED, 72u);
    u32x4 pq[24];
    const u32 a0 = L_RING + (kt * 192 + 8 * g + qp) * 128 + 8 * pp;
#pragma unroll
    for (int s = 0; s < 6; ++s)
#pragma unroll
      for (int ct = 0; ct < 4; ++ct) pq[s * 4 + ct] = tr_frag(L, a0 + s * 4096 + ct * 32, 4 * 128);
    lds_add(F_WREL + 0, 1);
    const u32 tag = get_tag();
    u32x4 hv[6];
    u32 spins = 0;
    const int hb = WS_H + xg * 18432 + kt * 6144 + lane * 16;
    int lo[6];
#pragma unroll
    for (int i = 0; i < 6; ++i) { const int e = i * 64 + lane; lo[i] = L_ACT + ((e >> 2) & 7) * ACT_STRIDE + kt * 384 + ((e >> 5) * 4 + (e & 3)) * 8; }
    while ((u32)__builtin_amdgcn_s_memrealtime() - t_start < (u32)HGATE) __builtin_amdgcn_s_sleep(2);
    for (;;) {
      MEMBAR();
#pragma unroll
      for (int i = 0; i < 6; ++i) hv[i] = ld128(rs, hb + i * 1024);
      u32 bad = 0;
#pragma unroll
      for (int i = 0; i < 6; ++i) bad |= (hv[i].y ^ tag) | (hv[i].w ^ tag);
      if (__all(bad == 0u) || !spin_ok(F, spins)) break;
      __builtin_amdgcn_s_sleep(HPOLL_SLEEP);
    }
#pragma unroll
    for (int i = 0; i < 6; ++i) *(LAS u32x2*)(L + lo[i]) = u32x2{hv[i].x, hv[i].z};
    WAIT_LGKM0();
    const u32 ab = L_ACT + (lane & 7) * ACT_STRIDE + kt * 384 + g * 16;
#pragma unroll
    for (int ct = 0; ct < 4; ++ct) acc[ct] = f32x4{0, 0, 0, 0};
#pragma unroll
    for (int s = 0; s < 6; ++s) {
      const u32x4 bfr = lds128(L, ab + s * 64);
#pragma unroll
      for (int ct = 0; ct < 4; ++ct) acc[ct] = mfma(pq[s * 4 + ct], bfr, acc[ct]);
    }
  };

  if (w == 0) {
    __builtin_amdgcn_s_setprio(3);
    if (lane < 32) F[lane] = 0u;
    u32 n = 0;
    u32x4 srd0; u32 v0, step0, pre;
    if (isF) { srd0 = mk_srd(p.wg + f * 64, DM * 9216 - f * 64); v0 = (lane >> 2) * 9216 + (lane & 3) * 16; step0 = 16 * 9216; pre = 16; }
    else if (isQ) { srd0 = mk_srd(p.wq + (xg * 512 + u * 64) * 2, DM * 8192 - (xg * 512 + u * 64) * 2); v0 = (lane >> 3) * 8192 + (lane & 7) * 16; step0 = 8 * 8192; pre = WQW - 4; }
    else if (isH) { srd0 = mk_srd(p.wmod + hu * 32, DM * 6912 - hu * 32); v0 = (lane >> 1) * 6912 + (lane & 1) * 16; step0 = 32 * 6912; pre = 8; }
    else { srd0 = mk_srd(p.wq, 0); v0 = 0; step0 = 1024; pre = 0; }
#pragma unroll 1
    for (; n < pre; n += 4) DMA4W(AUX_W, srd0, v0, n * step0, step0, L_RING + n * 1024);
    BARRIER();
    int pub = 0;
    auto run = [&](auto inflp, const u32x4& srd, u32 voff, u32 step, u32 cnt, u32 lds0, u32 first = 0) {
      constexpr int NP = decltype(inflp)::value;
      constexpr int KEEP = NP >= 8 ? NP - 4 : NP / 2;
      u32 s = first;
#pragma unroll 1
      for (; s + 4 <= cnt; s += 4) {
        DMA4W(AUX_W, srd, voff, s * step, step, lds0 + s * 1024);
        n += 4;
        asm volatile("s_waitcnt vmcnt(%0)" :: "i"(KEEP) : "memory");
        pub = max(pub, (int)n - KEEP); F[F_LANDED] = (u32)pub;
      }
#pragma unroll 1
      for (; s < cnt; ++s) { DMA1(AUX_W, srd, voff, s * step, lds0 + s * 1024); ++n; }
    };
    typedef std::integral_constant<int, INFLW / 16> P16;
    typedef std::integral_constant<int, (INFLW / 8 > 32 ? 32 : INFLW / 8)> P8;
    if (isF) {
      run(P16(), srd0, v0, step0, 36, L_RING, pre);
      run(P16(), mk_srd(p.wu + f * 64, DM * 9216 - f * 64), v0, step0, 36, L_RING + 36 * 1024);
    } else if (isO) {
      if (isQ) run(std::integral_constant<int, WQW>(), srd0, v0, step0, 72, L_RING, pre);
      else n = 72;
      run(std::integral_constant<int, WOW>(), mk_srd(p.wo + xg * 512 * 1152 + u * 128, 512 * 1152 - u * 128), (lane >> 3) * 1152 + (lane & 7) * 16, 8 * 1152, 64, L_RING + 72 * 1024);
    } else if (isH) {
      const u32 vm32 = (lane >> 1) * 6912 + (lane & 1) * 16;
      run(std::integral_constant<int, P1W>(), srd0, v0, step0, 18, L_RING, pre);
      run(std::integral_constant<int, P1W>(), mk_srd(p.wmod + 1152 + hu * 32, DM * 6912 - 1152 - hu * 32), vm32, 32 * 6912, 18, L_RING + 18 * 1024);
      run(P8(), mk_srd(p.wmod + 2304 + hu * 128, DM * 6912 - 2304 - hu * 128), (lane >> 3) * 6912 + (lane & 7) * 16, 8 * 6912, 72, L_RING + 36 * 1024);
    }
    const size_t kvoff = ((size_t)(bb * HKV + hkv) * SK + (size_t)key0) * 256;
    const u32x4 srd_k = mk_srd(p.kc + kvoff, (u32)nsl * 16384u), srd_v = mk_srd(p.vc + kvoff, (u32)nsl * 16384u);
    u32 vk[4], vv[2];
#pragma unroll
    for (int j = 0; j < 4; ++j) vk[j] = g * 256 + ((r16 ^ (4 * j + g)) << 4);
#pragma unroll
    for (int j = 0; j < 2; ++j) vv[j] = g * 256 + ((r16 ^ ((g | (j << 2)) << 1)) << 4);
    const u32 wneed = isF ? 0x00022211u : isO ? 0x33221111u : isH ? 0x02222211u : 0u;
    auto wait_window = [&](u32 N) {
      asm volatile("s_waitcnt vmcnt(28)\n\ts_cmp_ge_u32 %0, 28\n\ts_cbranch_scc1 9f\n\t"
                   "s_waitcnt vmcnt(24)\n\ts_cmp_ge_u32 %0, 24\n\ts_cbranch_scc1 9f\n\t"
                   "s_waitcnt vmcnt(20)\n\ts_cmp_ge_u32 %0, 20\n\ts_cbranch_scc1 9f\n\t"
                   "s_waitcnt vmcnt(16)\n9:" :: "s"(N) : "scc", "memory");
    };
    u32 fr_next = 0;
    u32 t_prev = t_start, t_ref = t_start;
    auto wait_free = [&](int pos, u32 need, u32 seen) {
      if (seen < need && F[F_FREE + pos] < need) { WAIT_VM0(); F[F_LANDED] = n; pub = (int)n; lds_wait_ge(F, F_FREE + pos, need); }
    };
#pragma unroll 1
    for (int q = 0; q < 2 * nsl; ++q) {
      const int pos = (q + roff) & 7;
      if (q >= 8) wait_free(pos, (u32)(q - 7), fr_next);
      else {
        const int nb = (wneed >> (4 * pos)) & 15;
#pragma unroll 1
        for (int i = 0; i < nb; ++i)
          if (F[F_WREL + i] < 7u) { WAIT_VM0(); F[F_LANDED] = n; pub = (int)n; lds_wait_ge(F, F_WREL + i, 7u); }
      }
      int lvl = q < EARLY_Q ? EARLY_LVL : BASE_LVL;
      if (q == SCHED_Q0) { const u32 t0_ = __builtin_amdgcn_readfirstlane((u32)F[F_T0]); if (t0_) t_ref = t0_; }
      if (q >= SCHED_Q0) {
        const u32 eown = t_prev + (u32)(((2 * nsl - q + 1) * SCHED_TAU) >> 4);
        const int lag = (int)(eown - t_ref) - SCHED_TE - SCHED_L1;
        if (lag > 0) lvl = BASE_LVL + BOOST1;
        if (lag > SCHED_L2) lvl = BASE_LVL + BOOST2;
        if (lag < -SCHED_L3) lvl = BASE_LVL - SLOW1;
      }
      t_prev = (u32)__builtin_amdgcn_s_memrealtime();
      const u32 N2 = 16u + 4u * (u32)((lvl + 1) >> 1), N = 16u + 4u * (u32)(lvl >> 1);
      fr_next = F[F_FREE + ((pos + 1) & 7)];
      const u32 lb = L_RING + pos * 16384;
      const bool isk = q < nsl;
      const u32x4 srd = isk ? srd_k : srd_v;
      const u32 v0_ = isk ? vk[0] : vv[0], v1_ = isk ? vk[1] : vv[0], v2_ = isk ? vk[2] : vv[1], v3_ = isk ? vk[3] : vv[1];
      const u32 so = (u32)(isk ? q : q - nsl) * 16384u;
#pragma unroll
      for (int gq = 0; gq < 4; ++gq) {
        DMA4(AUX_KV, srd, v0_, v1_, v2_, v3_, so + gq * 4096, lb + gq * 4096);
        n += 4;
        const u32 Ng = gq < 2 ? N2 : N;
        wait_window(Ng);
        pub = max(pub, (int)n - (int)Ng); F[F_LANDED] = (u32)pub;
      }
    }
    if (isF) {
      for (int i = 0; i < 3; ++i) wait_free(posd + i, (u32)(2 * nsl - 7 + i), 0u);
      run(P8(), mk_srd(p.wd + f * 36864, 36864), (u32)(lane * 16), 1024, 36, L_RING + posd * 16384);
    }
    if (!isF) { WAIT_VM0(); F[F_LANDED] = n; }
    nfin = n;
    __builtin_amdgcn_s_setprio(0);
    if (isF) {
      lds_wait_ge(F, F_CB0, 1u);
#pragma unroll
      for (int i = 0; i < 5; ++i) { cbp[i] = *(LAS u32*)(L + L_RED + i * 256 + lane * 4); sbp[i] = *(LAS u32*)(L + L_RED + 1280 + i * 256 + lane * 4); }
    }
  } else if (w == 7) {
    BARRIER();
    const u64 ctrl = __hip_atomic_load((GAS u64*)(p.ws + WS_CTRL), __ATOMIC_RELAXED, __HIP_MEMORY_SCOPE_AGENT);
    const u64 epoch = (((u64)__builtin_amdgcn_readfirstlane((u32)(ctrl >> 32)) << 32) | (u32)__builtin_amdgcn_readfirstlane((u32)ctrl)) + 1ull;
    const u32 tag = (u32)epoch;
    F[F_TAG] = tag;
    WAIT_LGKM0();
    F[F_TAGOK] = 1u;
    if (c == 0 && lane < 8) st64(rs, WS_T0 + lane * 128, u32x2{t_start, tag});
    const u32x4 z4 = {0u, 0u, 0u, 0u}, s4 = {SENT, SENT, SENT, SENT};
    if (isH) {
#pragma unroll
      for (int i = 0; i < 2; ++i) st64(rs, WS_MODT + (lane & 7) * 4608 + hu * 128 + (2 * i + (r16 >> 3)) * 32 + g * 8, u32x2{SENT, SENT});
    }
    st128(rs, WS_OP + c * 1024 + lane * 16, z4);
    if (lane == 0) st128(rs, WS_ST + c * 128, z4);
    if (isO) {
#pragma unroll
      for (int i = 0; i < 2; ++i) st128(rs, WS_WP + (xg * 144 + u * 16) * 128 + i * 1024 + lane * 16, z4);
      st128(rs, WS_X1 + (lane >> 3) * 9216 + mu * 128 + (lane & 7) * 16, s4);
    }
    if (isF) {
#pragma unroll 1
      for (int i = 0; i < 9; ++i) st128(rs, WS_FP + f * 9216 + i * 1024 + lane * 16, s4);
    }
    WAIT_VM0();
    F[F_INIT] = 1u;
    if (lane == 0) __hip_atomic_store((GAS u64*)(p.ws + WS_ALIVE) + xg * 32 + ci, epoch, __ATOMIC_RELAXED, __HIP_MEMORY_SCOPE_AGENT);
    if (isQ) {
      f32x4 acc[4];
      q_partial(2, acc);
      lds_add(F_WREL + 1, 1); lds_add(F_WREL + 2, 1);
      lds_wait_ge(F, F_P2, 2u);
      const int ls = (g * 8 + (lane & 7)) * 16;
#pragma unroll
      for (int ct = 0; ct < 4; ++ct) {
        const f32x4 a = (*(LAS f32x4*)(L + L_P2 + ct * 512 + ls) + *(LAS f32x4*)(L + L_P2 + 2048 + ct * 512 + ls)) + acc[ct];
        if (r16 < 8) st128(rs, WS_Q + ((2 * xg + (u >> 2)) * 8 + r16) * 1024 + ((u & 3) * 16 + ct * 4 + g) * 16, u32x4{pack2(a[0], a[1]), tag, pack2(a[2], a[3]), tag});
      }
    } else {
      lds_add(F_WREL + 0, 1); lds_add(F_WREL + 1, 1); lds_add(F_WREL + 2, 1);
    }
    {
      u32x4 qv; u32x2 t0v = {0u, 0u}; u32 spins = 0;
      while ((u32)__builtin_amdgcn_s_memrealtime() - t_start < (u32)QGATE) __builtin_amdgcn_s_sleep(2);
      for (;;) {
        MEMBAR();
        qv = ld128(rs, WS_Q + (hkv * 8 + bb) * 1024 + lane * 16);
        t0v = ld64(rs, WS_T0 + xg * 128);
        if (__all(((qv.y ^ tag) | (qv.w ^ tag)) == 0u) || !spin_ok(F, spins)) break;
        __builtin_amdgcn_s_sleep(QPOLL_SLEEP);
      }
      *(LAS u32x2*)(L + L_QS + lane * 8) = u32x2{qv.x, qv.z};
      F[F_T0] = t0v.y == tag ? t0v.x : t_start;
      WAIT_LGKM0();
      F[F_Q] = 1u;
    }
    lds_wait_ge(F, F_LANDED, Wtot + 16u * (u32)(nsl - 8));
    {
      const u32 ehi = (u32)(epoch >> 32);
      u32 spins = 0;
      for (;;) {
        MEMBAR();
        const u32x4 a = ld128(rs, WS_ALIVE + lane * 32), b2 = ld128(rs, WS_ALIVE + lane * 32 + 16);
        const u32 bad = (a.x ^ tag) | (a.y ^ ehi) | (a.z ^ tag) | (a.w ^ ehi) | (b2.x ^ tag) | (b2.y ^ ehi) | (b2.z ^ tag) | (b2.w ^ ehi);
        if (__all(bad == 0u) || !spin_ok(F, spins)) break;
        __builtin_amdgcn_s_sleep(64);
      }
    }
    F[F_ALIVE] = 1u;
    if (c == 0 && lane == 0) __hip_atomic_store((GAS u64*)(p.ws + WS_CTRL), epoch, __ATOMIC_RELAXED, __HIP_MEMORY_SCOPE_AGENT);
    {
      lds_wait_ge(F, F_L, 6u);
      const int hd = lane & 1;
      float M = *(LAS float*)(L + L_MX + hd * 4), Ls = *(LAS float*)(L + L_LW + hd * 4);
#pragma unroll
      for (int i = 1; i < 6; ++i) { M = fmaxf(M, *(LAS float*)(L + L_MX + i * 8 + hd * 4)); Ls += *(LAS float*)(L + L_LW + i * 8 + hd * 4); }
      if (lane < 2) st64(rs, WS_ST + c * 128 + hd * 8, u32x2{vld(M), vld(Ls)});
      u32x2 o; u32 spins = 0;
      for (;;) {
        MEMBAR();
        o = ld64(rs, WS_ST + (c ^ 8) * 128 + hd * 8);
        if (__all((o.x & o.y & 1u) != 0u) || !spin_ok(F, spins)) break;
        __builtin_amdgcn_s_sleep(8);
      }
      const float m0 = __uint_as_float(vld(M)), l0 = __uint_as_float(vld(Ls)), m1 = __uint_as_float(o.x), l1 = __uint_as_float(o.y);
      const float Mg = fmaxf(m0, m1);
      const float e0 = __builtin_amdgcn_exp2f((m0 - Mg) * LOG2E), e1 = __builtin_amdgcn_exp2f((m1 - Mg) * LOG2E);
      const float sc = e0 / (e0 * l0 + e1 * l1);
      osA = __builtin_bit_cast(float, __builtin_amdgcn_readlane(__builtin_bit_cast(int, sc), 0));
      osB = __builtin_bit_cast(float, __builtin_amdgcn_readlane(__builtin_bit_cast(int, sc), 1));
    }
    if (isF) {
      load_cbsb(0, cbp, sbp);
#pragma unroll
      for (int i = 0; i < 5; ++i) { *(LAS u32*)(L + L_RED + i * 256 + lane * 4) = cbp[i]; *(LAS u32*)(L + L_RED + 1280 + i * 256 + lane * 4) = sbp[i]; }
      WAIT_LGKM0();
      F[F_CB0] = 1u;
      load_cbsb(7, cbp, sbp);
      u32x2 gb; u32 spins = 0;
      for (;;) {
        MEMBAR();
        gb = ld64(rs, WS_MODT + (lane & 7) * 4608 + 864 * 4 + f * 8);
        if (__all(umax(gb.x, gb.y) != SENT) || !spin_ok(F, spins)) break;
      }
      pre4a = u32x4{gb.x, gb.y, 0u, 0u};
    } else if (isO) {
      u32 spins = 0;
      for (;;) {
        MEMBAR();
        pre4a = ld128(rs, WS_MODT + (lane & 7) * 4608 + mu * 16);
        if (__all(umax4(pre4a) != SENT) || !spin_ok(F, spins)) break;
      }
      pre4b = *(const GAS u32x4*)(p.x + (lane & 7) * DM + mu * 8);
    }
  } else {
    const int cw = w - 1;
    if (isF) {
      BARRIER();
      const int m = cw & 1, kt = cw >> 1;
      const u32 a0 = L_RING + 36 * m * 1024 + (kt * 192 + 8 * g + qp) * 64 + 8 * pp;
      if (m == 1) lds_add(F_WREL + 0, 1);
      lds_wait_ge(F, F_LANDED, m == 0 ? 36u : 72u);
#pragma unroll
      for (int s = 0; s < 6; ++s) { wf[2 * s] = tr_frag(L, a0 + s * 2048, 4 * 64); wf[2 * s + 1] = tr_frag(L, a0 + s * 2048 + 32, 4 * 64); }
      if (m == 0) lds_add(F_WREL + 0, 1);
      lds_add(F_WREL + 1, 1);
      lds_add(F_WREL + 2, 1);
    } else if (isO) {
      BARRIER();
      if (cw < 4) {
        lds_add(F_WREL + 0, 1);
        const u32 a0 = L_RING + 72 * 1024 + (8 * g + qp) * 128 + cw * 32 + 8 * pp;
        lds_wait_ge(F, F_LANDED, 104u);
#pragma unroll
        for (int s = 0; s < 8; ++s) wf[s] = tr_frag(L, a0 + s * 4096, 4 * 128);
        lds_add(F_WREL + 1, 1);
        lds_wait_ge(F, F_LANDED, 136u);
#pragma unroll
        for (int s = 8; s < 16; ++s) wf[s] = tr_frag(L, a0 + s * 4096, 4 * 128);
        lds_add(F_WREL + 2, 1);
      } else {
        if (isQ) {
          f32x4 acc[4];
          q_partial(cw - 4, acc);
          if (r16 < 8) {
#pragma unroll
            for (int ct = 0; ct < 4; ++ct) *(LAS f32x4*)(L + L_P2 + (cw - 4) * 2048 + ct * 512 + (g * 8 + r16) * 16) = acc[ct];
          }
          lds_add(F_P2, 1);
        } else lds_add(F_WREL + 0, 1);
        lds_add(F_WREL + 1, 1); lds_add(F_WREL + 2, 1);
      }
    } else if (isH) {
      u32 cv[6], xa[5], xb[5];
#pragma unroll
      for (int i = 0; i < 6; ++i) cv[i] = ((const GAS u32*)p.c)[cw * 64 + lane + 384 * i];
      const int rowb = 6 + (cw & 1);
      const GAS u32* xr = (const GAS u32*)p.x + cw * 288;
      const GAS u32* xs = (const GAS u32*)p.x + rowb * 288;
#pragma unroll
      for (int i = 0; i < 4; ++i) { xa[i] = xr[lane + 64 * i]; xb[i] = xs[lane + 64 * i]; }
      xa[4] = xr[256 + (lane & 31)]; xb[4] = xs[256 + (lane & 31)];
      const u32x2 xq = *(const GAS u32x2*)(p.x + (lane & 7) * DM + hu * 16 + g * 4);
      BARRIER();
#pragma unroll
      for (int i = 0; i < 6; ++i) {
        const int e = cw * 64 + lane + 384 * i;
        const float a = bflo(cv[i]), b2 = bfhi(cv[i]);
        *(LAS u32*)(L + L_SC + (e / 288) * ACT_STRIDE + (e % 288) * 4) = pack2(a / (1.0f + __expf(-a)), b2 / (1.0f + __expf(-b2)));
      }
      {
        float v[10], mean_r, sd_r;
#pragma unroll
        for (int i = 0; i < 5; ++i) { v[2 * i] = bflo(xa[i]); v[2 * i + 1] = bfhi(xa[i]); }
        ln_stats(v, lane < 32, mean_r, sd_r);
        if (lane == 0) { *(LAS float*)(L + L_STAT + cw * 8) = mean_r; *(LAS float*)(L + L_STAT + cw * 8 + 4) = sd_r; }
        if (cw < 2) {
#pragma unroll
          for (int i = 0; i < 5; ++i) { v[2 * i] = bflo(xb[i]); v[2 * i + 1] = bfhi(xb[i]); }
          ln_stats(v, lane < 32, mean_r, sd_r);
          if (lane == 0) { *(LAS float*)(L + L_STAT + rowb * 8) = mean_r; *(LAS float*)(L + L_STAT + rowb * 8 + 4) = sd_r; }
        }
      }
      lds_add(F_SC, 1);
      const u32 sb0 = L_SC + (lane & 7) * ACT_STRIDE + g * 16;
      {
        const int m = cw & 1, kt = cw >> 1;
        lds_wait_ge(F, F_SC, 6u);
        const float mean_r = *(LAS float*)(L + L_STAT + (lane & 7) * 8), sd_r = *(LAS float*)(L + L_STAT + (lane & 7) * 8 + 4);
        float n0 = rb(rb(bflo(xq.x) - mean_r) / sd_r), n1 = rb(rb(bfhi(xq.x) - mean_r) / sd_r);
        float n2 = rb(rb(bflo(xq.y) - mean_r) / sd_r), n3 = rb(rb(bfhi(xq.y) - mean_r) / sd_r);
        asm volatile("" : "+v"(n0), "+v"(n1), "+v"(n2), "+v"(n3));
        u32 tag = 0;
        if (cw == 5) tag = get_tag();
        u32x4 bfr[6];
#pragma unroll
        for (int s = 0; s < 6; ++s) bfr[s] = lds128(L, sb0 + (kt * 6 + s) * 64);
        lds_wait_ge(F, F_LANDED, 36u);
        const u32 a0 = L_RING + (m * 18 + kt * 6) * 1024 + (8 * g + qp) * 32 + 8 * pp;
        f32x4 acc = {0, 0, 0, 0}, acc2 = {0, 0, 0, 0};
#pragma unroll
        for (int s = 0; s < 6; s += 2) { acc = mfma(tr_frag(L, a0 + s * 1024, 4 * 32), bfr[s], acc); acc2 = mfma(tr_frag(L, a0 + (s + 1) * 1024, 4 * 32), bfr[s + 1], acc2); }
        acc += acc2;
        if (r16 < 8) *(LAS f32x4*)(L + L_RED + cw * 512 + (g * 8 + r16) * 16) = acc;
        lds_add(F_P1, 1);
        lds_add(F_WREL + 0, 1);
        if (cw == 5) {
          lds_wait_ge(F, F_P1, 6u);
          const int ls = (g * 8 + (lane & 7)) * 16;
          const f32x4 sa = *(LAS f32x4*)(L + L_RED + 0 * 512 + ls) + *(LAS f32x4*)(L + L_RED + 2 * 512 + ls) + *(LAS f32x4*)(L + L_RED + 4 * 512 + ls);
          const f32x4 ca = *(LAS f32x4*)(L + L_RED + 1 * 512 + ls) + *(LAS f32x4*)(L + L_RED + 3 * 512 + ls) + *(LAS f32x4*)(L + L_RED + 5 * 512 + ls);
          const float h0 = rb(rb(n0 * rb(1.0f + rb(ca[0]))) + rb(sa[0])), h1 = rb(rb(n1 * rb(1.0f + rb(ca[1]))) + rb(sa[1]));
          const float h2 = rb(rb(n2 * rb(1.0f + rb(ca[2]))) + rb(sa[2])), h3 = rb(rb(n3 * rb(1.0f + rb(ca[3]))) + rb(sa[3]));
          const u32x4 hu4 = {pack2(h0, h1), tag, pack2(h2, h3), tag};
#pragma unroll
          for (int i = 0; i < REP_H / 2; ++i)
            st128(rs, WS_H + (2 * i + (r16 >> 3)) * 18432 + hu * 512 + (lane & 7) * 64 + g * 16, hu4);
        }
      }
      if (cw < 4) {
        lds_wait_ge(F, F_LANDED, 108u);
        const u32 a0 = L_RING + 36 * 1024 + (8 * g + qp) * 128 + cw * 32 + 8 * pp;
        f32x4 a0c = {0, 0, 0, 0}, a1c = {0, 0, 0, 0};
#pragma unroll 3
        for (int s = 0; s < 18; s += 2) {
          a0c = mfma(tr_frag(L, a0 + s * 4096, 4 * 128), lds128(L, sb0 + s * 64), a0c);
          a1c = mfma(tr_frag(L, a0 + (s + 1) * 4096, 4 * 128), lds128(L, sb0 + (s + 1) * 64), a1c);
        }
        const f32x4 a = a0c + a1c;
        lds_add(F_WREL + 1, 1);
        lds_wait_ge(F, F_INIT, 1u);
        if (r16 < 8) st64(rs, WS_MODT + r16 * 4608 + hu * 128 + cw * 32 + g * 8, u32x2{nosent(pack2(a[0], a[1])), nosent(pack2(a[2], a[3]))});
      } else lds_add(F_WREL + 1, 1);
      lds_add(F_WREL + 2, 1);
    } else {
      BARRIER();
      lds_add(F_WREL + 0, 1); lds_add(F_WREL + 1, 1); lds_add(F_WREL + 2, 1);
    }
    u32x4 qf[4];
    u32 ka[4];
#pragma unroll
    for (int s = 0; s < 4; ++s) ka[s] = r16 * 256 + (((4 * s + g) ^ r16) << 4);
    float mx0 = -INFINITY, mx1 = -INFINITY;
    auto k_read = [&](int q, u32x4* kf) {
      const int pos = (q + roff) & 7;
      lds_wait_ge(F, F_LANDED, Wtot + 16u * (u32)(q + 1));
      const u32 sb = L_RING + pos * 16384;
#pragma unroll
      for (int t = 0; t < 4; ++t)
#pragma unroll
        for (int s = 0; s < 4; ++s) kf[t * 4 + s] = lds128(L, sb + ka[s] + t * 4096);
      WAIT_LGKM0();
      if (lane == 0) F[F_FREE + pos] = (u32)(q + 1);
    };
    auto k_scores = [&](int q, const u32x4* kf) {
      f32x4 acc[4];
#pragma unroll
      for (int t = 0; t < 4; ++t) {
        acc[t] = f32x4{0, 0, 0, 0};
#pragma unroll
        for (int s = 0; s < 4; ++s) acc[t] = mfma(qf[s], kf[t * 4 + s], acc[t]);
      }
      float s0 = g == 0 ? acc[0][0] : g == 1 ? acc[1][0] : g == 2 ? acc[2][0] : acc[3][0];
      float s1 = g == 0 ? acc[0][1] : g == 1 ? acc[1][1] : g == 2 ? acc[2][1] : acc[3][1];
      s0 = rb(rb(s0) * 0.08838834764831845f);
      s1 = rb(rb(s1) * 0.08838834764831845f);
      mx0 = fmaxf(mx0, s0); mx1 = fmaxf(mx1, s1);
      *(LAS u16*)(L + L_SC + q * 256 + lane * 2) = (u16)(__float_as_uint(s0) >> 16);
      *(LAS u16*)(L + L_SC + q * 256 + 128 + lane * 2) = (u16)(__float_as_uint(s1) >> 16);
    };
    {
      u32x4 kf[16];
      k_read(cw, kf);
      lds_wait_ge(F, F_Q, 1u);
      lds_wait_ge(F, F_WREL + 1, 7u); lds_wait_ge(F, F_WREL + 2, 7u);
#pragma unroll
      for (int s = 0; s < 4; ++s) qf[s] = lds128(L, L_QS + (lane & 1) * 256 + s * 64 + g * 16);
      k_scores(cw, kf);
    }
#pragma unroll 1
    for (int q = cw + 6; q < nsl; q += 6) {
      u32x4 kf[16];
      k_read(q, kf);
      k_scores(q, kf);
    }
    {
      mx0 = wave_max(mx0); mx1 = wave_max(mx1);
      if (lane == 0) *(LAS u32x2*)(L + L_MX + cw * 8) = u32x2{__float_as_uint(mx0), __float_as_uint(mx1)};
      lds_add(F_MX, 1);
      lds_wait_ge(F, F_MX, 6u);
#pragma unroll
      for (int i = 0; i < 6; ++i) { mx0 = fmaxf(mx0, *(LAS float*)(L + L_MX + i * 8)); mx1 = fmaxf(mx1, *(LAS float*)(L + L_MX + i * 8 + 4)); }
      float l0 = 0.f, l1 = 0.f;
#pragma unroll 1
      for (int q = cw; q < nsl; q += 6) {
        const float s0 = bf2f(*(LAS u16*)(L + L_SC + q * 256 + lane * 2)), s1 = bf2f(*(LAS u16*)(L + L_SC + q * 256 + 128 + lane * 2));
        const float p0 = __builtin_amdgcn_exp2f((s0 - mx0) * LOG2E), p1 = __builtin_amdgcn_exp2f((s1 - mx1) * LOG2E);
        l0 += p0; l1 += p1;
        *(LAS u16*)(L + L_SC + q * 256 + lane * 2) = (u16)f2bf(p0);
        *(LAS u16*)(L + L_SC + q * 256 + 128 + lane * 2) = (u16)f2bf(p1);
      }
      l0 = wave_sum(l0); l1 = wave_sum(l1);
      if (lane == 0) *(LAS u32x2*)(L + L_LW + cw * 8) = u32x2{__float_as_uint(l0), __float_as_uint(l1)};
      lds_add(F_L, 1);
    }
    f32x4 o[8];
#pragma unroll
    for (int cb = 0; cb < 8; ++cb) o[cb] = f32x4{0, 0, 0, 0};
    const u32 va0 = (8 * g + qp) * 256 + ((pp >> 1) << 4) + 8 * (pp & 1) + (((qp | ((g & 1) << 2)) << 1) << 4);
#pragma unroll 1
    for (int q = cw; q < nsl; q += 6) {
      const int pos = (q + nsl + roff) & 7;
      if (isF && q >= 24 && q < 30) { lds_wait_ge(F, F_ALIVE, 1u); load_cbsb(w, cbp, sbp); }
      const u32x4 pf0 = lds128(L, L_SC + q * 256 + (lane & 1) * 128 + g * 16), pf1 = lds128(L, L_SC + q * 256 + (lane & 1) * 128 + 64 + g * 16);
      lds_wait_ge(F, F_LANDED, Wtot + 16u * (u32)(q + nsl + 1));
      const u32 sb = L_RING + pos * 16384 + va0;
      u32x4 vf[8], vg2[8];
#pragma unroll
      for (int cb = 0; cb < 8; ++cb) vf[cb] = tr_frag(L, sb ^ (cb << 5), 1024);
#pragma unroll
      for (int cb = 0; cb < 8; ++cb) vg2[cb] = tr_frag(L, (sb ^ (cb << 5)) + 8192, 1024);
      WAIT_LGKM0();
      if (lane == 0) F[F_FREE + pos] = (u32)(q + nsl + 1);
#pragma unroll
      for (int cb = 0; cb < 8; ++cb) o[cb] = mfma(pf0, vf[cb], o[cb]);
#pragma unroll
      for (int cb = 0; cb < 8; ++cb) o[cb] = mfma(pf1, vg2[cb], o[cb]);
    }
    if (g == 0) {
#pragma unroll
      for (int cb = 0; cb < 8; ++cb) *(LAS u32x2*)(L + L_ACT + cw * 1024 + (cb * 16 + r16) * 8) = u32x2{__float_as_uint(o[cb][0]), __float_as_uint(o[cb][1])};
    }
    lds_add(F_OPART, 1);
  }
  if (w == 7) {
    lds_wait_ge(F, F_OPART, 6u);
    f32x4 O = *(LAS f32x4*)(L + L_ACT + lane * 16);
#pragma unroll
    for (int i = 1; i < 6; ++i) O += *(LAS f32x4*)(L + L_ACT + i * 1024 + lane * 16);
    st128(rs, WS_OP + c * 1024 + lane * 16, u32x4{vld(O[0] * osA), vld(O[1] * osB), vld(O[2] * osA), vld(O[3] * osB)});
    F[F_OPUB] = 1u;
  }
  if (isO) {
    constexpr int L_OS = L_SC;
    u32x4 ov[4];
    bool got[4] = {false, false, false, false};
    u32 spins = 0;
    lds_wait_ge(F, F_ALIVE, 1u);
    lds_wait_ge(F, F_OPUB, 1u); __builtin_amdgcn_s_sleep(DELAY_A);
    for (;;) {
      MEMBAR();
#pragma unroll
      for (int i = 0; i < 4; ++i) if (!got[i]) ov[i] = ld128(rs, WS_OP + (xg + 8 * ((i >> 1) * 16 + 2 * w + (i & 1))) * 1024 + lane * 16);
      bool all = true;
#pragma unroll
      for (int i = 0; i < 4; ++i) if (!got[i]) {
        got[i] = __all((ov[i].x & ov[i].y & ov[i].z & ov[i].w & 1u) != 0u);
        all = all && got[i];
      }
      if (all || !spin_ok(F, spins)) break;
    }
#pragma unroll
    for (int hk = 0; hk < 2; ++hk) {
      const f32x4 s = __builtin_bit_cast(f32x4, ov[2 * hk]) + __builtin_bit_cast(f32x4, ov[2 * hk + 1]);
      *(LAS u32*)(L + L_OS + w * 1040 + hk * 512 + lane * 4) = pack2(s[0], s[2]);
      *(LAS u32*)(L + L_OS + w * 1040 + hk * 512 + 256 + lane * 4) = pack2(s[1], s[3]);
    }
    BARRIER();
    if (w >= 1 && w <= 4) {
      const int ct = w - 1;
      const u32 ob = L_OS + (lane & 7) * 1040 + g * 16;
      u32x4 bfr[16];
#pragma unroll
      for (int s = 0; s < 16; ++s) bfr[s] = lds128(L, ob + s * 64);
      f32x4 a0 = {0, 0, 0, 0}, a1 = {0, 0, 0, 0}, a2 = {0, 0, 0, 0}, a3 = {0, 0, 0, 0};
#pragma unroll
      for (int s = 0; s < 16; s += 4) { a0 = mfma(wf[s], bfr[s], a0); a1 = mfma(wf[s + 1], bfr[s + 1], a1); a2 = mfma(wf[s + 2], bfr[s + 2], a2); a3 = mfma(wf[s + 3], bfr[s + 3], a3); }
      const f32x4 a = (a0 + a1) + (a2 + a3);
      if (r16 < 8) st128(rs, WS_WP + (xg * 144 + (u * 4 + ct) * 4 + g) * 128 + r16 * 16, u32x4{vld(a[0]), vld(a[1]), vld(a[2]), vld(a[3])});
    } else if (w == 7) {
      u32x4 pa, pb; u32 spins2 = 0;
      __builtin_amdgcn_s_sleep(DELAY_B);
      const int wo_ = WS_WP + ((lane >> 3) * 144 + 2 * mu) * 128 + (lane & 7) * 16;
      for (;;) {
        MEMBAR();
        pa = ld128(rs, wo_); pb = ld128(rs, wo_ + 128);
        if (__all((pa.x & pa.y & pa.z & pa.w & pb.x & pb.y & pb.z & pb.w & 1u) != 0u) || !spin_ok(F, spins2)) break;
      }
      float a[8];
      a[0] = __uint_as_float(pa.x); a[1] = __uint_as_float(pa.y); a[2] = __uint_as_float(pa.z); a[3] = __uint_as_float(pa.w);
      a[4] = __uint_as_float(pb.x); a[5] = __uint_as_float(pb.y); a[6] = __uint_as_float(pb.z); a[7] = __uint_as_float(pb.w);
#pragma unroll
      for (int i = 0; i < 8; ++i) a[i] = sum_x8_16_32(a[i]);
      u32x4 xo;
#pragma unroll
      for (int i = 0; i < 4; ++i) {
        const u32 ap = pack2(a[2 * i], a[2 * i + 1]);
        const u32 tp = pack2(bflo(pre4a[i]) * bflo(ap), bfhi(pre4a[i]) * bfhi(ap));
        xo[i] = nosent(pack2(bflo(pre4b[i]) + bflo(tp), bfhi(pre4b[i]) + bfhi(tp)));
      }
      st128(rs, WS_X1 + (lane >> 3) * 9216 + mu * 128 + (lane & 7) * 16, xo);
    }
  } else if (isF) {
    const u32 n0 = Wtot + 32u * (u32)nsl;
    if (w == 7) {
      u32x4 pv; u32 spins = 0;
      __builtin_amdgcn_s_sleep(DELAY_W);
      for (;;) {
        MEMBAR();
        pv = ld128(rs, WS_WP + ((lane >> 3) * 144 + f) * 128 + (lane & 7) * 16);
        if (__all((pv.x & pv.y & pv.z & pv.w & 1u) != 0u) || !spin_ok(F, spins)) break;
      }
      F[F_X1] = 1u;
    } else lds_wait_ge(F, F_X1, 1u);
    __builtin_amdgcn_s_sleep(DELAY_C);
    u32x2 x1p = {0u, 0u};
    {
      if (w != 0) {
        const int e0 = (w - 1) * 64 + lane, xb = WS_X1 + xg * 9216;
        u32x4 xv, xw = {0u, 0u, 0u, 0u}; u32 spins = 0;
        for (;;) {
          MEMBAR();
          xv = ld128(rs, xb + e0 * 16);
          if (w <= 2) xw = ld128(rs, xb + (448 + e0) * 16);
          if (__all(umax(umax4(xv), umax4(xw)) != SENT) || !spin_ok(F, spins)) break;
        }
        const u32 a = L_ACT + (e0 & 7) * ACT_STRIDE + (e0 >> 3) * 16;
        *(LAS u32x4*)(L + a) = xv;
        if (w <= 2) *(LAS u32x4*)(L + a + 56 * 16) = xw;
      }
      BARRIER();
      u32 xr[5];
#pragma unroll
      for (int i = 0; i < 4; ++i) xr[i] = *(LAS u32*)(L + L_ACT + w * ACT_STRIDE + (lane + 64 * i) * 4);
      xr[4] = *(LAS u32*)(L + L_ACT + w * ACT_STRIDE + (256 + (lane & 31)) * 4);
      if (w == 7) x1p = *(LAS u32x2*)(L + L_ACT + (lane & 7) * ACT_STRIDE + f * 8);
      float v[10];
#pragma unroll
      for (int i = 0; i < 5; ++i) { v[2 * i] = bflo(xr[i]); v[2 * i + 1] = bfhi(xr[i]); }
      float s = (v[0] + v[1]) + (v[2] + v[3]) + ((v[4] + v[5]) + (v[6] + v[7]));
      if (lane < 32) s += v[8] + v[9];
      const float mean = wave_sum(s) * (1.0f / DM);
      float q2 = 0.f, q3 = 0.f;
#pragma unroll
      for (int i = 0; i < 4; ++i) { const float d0 = v[2 * i] - mean, d1 = v[2 * i + 1] - mean; q2 += d0 * d0; q3 += d1 * d1; }
      if (lane < 32) { const float d0 = v[8] - mean, d1 = v[9] - mean; q2 += d0 * d0; q3 += d1 * d1; }
      const float var = wave_sum(q2 + q3) * (1.0f / DM);
      const float mean_r = rb(mean);
      const float rinv = __builtin_amdgcn_rcpf(rb(__builtin_amdgcn_sqrtf(rb(rb(var) + 1e-5f))));
#pragma unroll
      for (int i = 0; i < 5; ++i) {
        const u32 dp = pack2(v[2 * i] - mean_r, v[2 * i + 1] - mean_r);
        const u32 np = pack2(bflo(dp) * rinv, bfhi(dp) * rinv);
        const u32 tp = pack2(bflo(np) * bflo(cbp[i]), bfhi(np) * bfhi(cbp[i]));
        const u32 hp = pack2(bflo(tp) + bflo(sbp[i]), bfhi(tp) + bfhi(sbp[i]));
        if (i < 4) *(LAS u32*)(L + L_H2 + w * ACT_STRIDE + (lane + 64 * i) * 4) = hp;
        else if (lane < 32) *(LAS u32*)(L + L_H2 + w * ACT_STRIDE + (256 + lane) * 4) = hp;
      }
    }
    if (w == 0) { WAIT_VM0(); F[F_LANDED] = nfin; }
    BARRIER();
    if (w == 7) {
      BARRIER();
      BARRIER();
      const int off = WS_FP + (lane >> 3) * 9216 + ((f >> 3) * 4 + (f & 3)) * 128 + (lane & 7) * 16 + ((f >> 2) & 1) * 8;
      u32x2 e[18]; u32 spins = 0;
      u32 slo = 0x00003f80u, shi = 0x3f800000u;
      asm volatile("" : "+v"(slo), "+v"(shi));
      __builtin_amdgcn_s_sleep(DELAY_E);
      for (;;) {
        MEMBAR();
#pragma unroll
        for (int i = 0; i < 18; ++i) e[i] = ld64(rs, off + i * 8 * 9216);
        u32 mx = 0;
#pragma unroll
        for (int i = 0; i < 18; ++i) mx = umax(mx, umax(e[i].x, e[i].y));
        if (__all(mx != SENT) || !spin_ok(F, spins)) break;
      }
      f32x4 s = {0, 0, 0, 0}, s1 = {0, 0, 0, 0};
#pragma unroll
      for (int i = 0; i < 18; i += 2) {
        s[0] = add_sel(e[i].x, slo, s[0]); s[1] = add_sel(e[i].x, shi, s[1]); s[2] = add_sel(e[i].y, slo, s[2]); s[3] = add_sel(e[i].y, shi, s[3]);
        s1[0] = add_sel(e[i + 1].x, slo, s1[0]); s1[1] = add_sel(e[i + 1].x, shi, s1[1]); s1[2] = add_sel(e[i + 1].y, slo, s1[2]); s1[3] = add_sel(e[i + 1].y, shi, s1[3]);
      }
      s += s1;
      const u32 dA = pack2(sum_x8_16_32(s[0]), sum_x8_16_32(s[1])), dB = pack2(sum_x8_16_32(s[2]), sum_x8_16_32(s[3]));
      const u32 tA = pack2(bflo(pre4a.x) * bflo(dA), bfhi(pre4a.x) * bfhi(dA)), tB = pack2(bflo(pre4a.y) * bflo(dB), bfhi(pre4a.y) * bfhi(dB));
      if (lane < 8) *(GAS u32x2*)(p.out + lane * DM + f * 4) = u32x2{pack2(bflo(x1p.x) + bflo(tA), bfhi(x1p.x) + bfhi(tA)), pack2(bflo(x1p.y) + bflo(tB), bfhi(x1p.y) + bfhi(tB))};
    } else {
      const int cw = w - 1;
      if (w >= 1) {
        const int kt = cw >> 1;
        const u32 hb = L_H2 + (lane & 7) * ACT_STRIDE + kt * 384 + g * 16;
        u32x4 bfr[6];
#pragma unroll
        for (int s = 0; s < 6; ++s) bfr[s] = lds128(L, hb + s * 64);
        f32x4 a0 = {0, 0, 0, 0}, a1 = {0, 0, 0, 0}, a2 = {0, 0, 0, 0}, a3 = {0, 0, 0, 0};
#pragma unroll
        for (int s = 0; s < 6; s += 2) { a0 = mfma(wf[2 * s], bfr[s], a0); a1 = mfma(wf[2 * s + 1], bfr[s], a1); a2 = mfma(wf[2 * s + 2], bfr[s + 1], a2); a3 = mfma(wf[2 * s + 3], bfr[s + 1], a3); }
        a0 += a2; a1 += a3;
        if (r16 < 8) { *(LAS f32x4*)(L + L_R2 + cw * 1024 + (g * 8 + r16) * 16) = a0; *(LAS f32x4*)(L + L_R2 + cw * 1024 + 512 + (g * 8 + r16) * 16) = a1; }
        lds_wait_ge(F, F_LANDED, n0 + 36u);
        const u32 d0 = L_RING + posd * 16384 + (8 * g + qp) * 1152 + cw * 192 + 8 * pp;
#pragma unroll
        for (int t = 0; t < 6; ++t) wf[12 + t] = tr_frag(L, d0 + t * 32, 4 * 1152);
      }
      BARRIER();
      if (w < 4) {
        const int ct = w >> 1, e = (w & 1) * 64 + lane;
        const u32 a = L_R2 + ct * 512 + e * 4;
        const float gg = rb(*(LAS float*)(L + a) + *(LAS float*)(L + a + 2048) + *(LAS float*)(L + a + 4096));
        const float uu = rb(*(LAS float*)(L + a + 1024) + *(LAS float*)(L + a + 3072) + *(LAS float*)(L + a + 5120));
        const float inner = 0.7978845608028654f * (gg + 0.044715f * gg * gg * gg);
        const float th = 1.0f - 2.0f * __builtin_amdgcn_rcpf(1.0f + __expf(2.0f * inner));
        const float ge = rb(0.5f * gg * (1.0f + th));
        const int ls = e >> 2, rr = e & 3, b_ = ls & 7, g_ = ls >> 3;
        *(LAS u16*)(L + L_HH + b_ * 80 + (ct * 16 + g_ * 4 + rr) * 2) = (u16)f2bf(ge * uu);
      }
      BARRIER();
      if (w >= 1) {
        const u32x4 bfr = lds128(L, L_HH + (lane & 7) * 80 + g * 16);
        f32x4 a[6];
#pragma unroll
        for (int t = 0; t < 6; ++t) a[t] = mfma(wf[12 + t], bfr, f32x4{0, 0, 0, 0});
#pragma unroll
        for (int t = 0; t < 6; t += 2)
          if (r16 < 8) st128(rs, WS_FP + f * 9216 + ((cw * 3 + (t >> 1)) * 4 + g) * 128 + r16 * 16,
                             u32x4{nosent(pack2(a[t][0], a[t][1])), nosent(pack2(a[t][2], a[t][3])), nosent(pack2(a[t + 1][0], a[t + 1][1])), nosent(pack2(a[t + 1][2], a[t + 1][3]))});
      }
    }
  }
}

}

extern "C" void kernel_launch(void* const* d_in, const int* in_sizes, int n_in, void* d_out, int out_size, void* d_ws, size_t ws_size,
                              hipStream_t stream) {
  (void)in_sizes; (void)n_in; (void)out_size; (void)ws_size;
  Params p;
  p.x = (const u16*)d_in[0];
  p.kc = (const unsigned char*)d_in[1];
  p.vc = (const unsigned char*)d_in[2];
  p.wq = (const unsigned char*)d_in[3];
  p.wo = (const unsigned char*)d_in[4];
  p.wg = (const unsigned char*)d_in[6];
  p.wu = (const unsigned char*)d_in[7];
  p.wd = (const unsigned char*)d_in[8];
  p.c = (const u16*)d_in[9];
  p.wmod = (const unsigned char*)d_in[10];
  p.out = (u16*)d_out;
  p.ws = (unsigned char*)d_ws;
  (void)hipFuncSetAttribute((const void*)fused_block, hipFuncAttributeMaxDynamicSharedMemorySize, LDS_BYTES);
  hipLaunchKernelGGL(fused_block, dim3(NCU), dim3(NT), LDS_BYTES, stream, p);
}
```

```cpp
#include <hip/hip_runtime.h>
#include <stdint.h>
#include <type_traits>
#pragma clang fp contract(off)

#define LAS __attribute__((address_space(3)))
#define GAS __attribute__((address_space(1)))
typedef unsigned u32;
typedef unsigned long long u64;
typedef unsigned short u16;
typedef u32 u32x4 __attribute__((ext_vector_type(4)));
typedef u32 u32x2 __attribute__((ext_vector_type(2)));
typedef float f32x4 __attribute__((ext_vector_type(4)));
typedef __bf16 bf16x8 __attribute__((ext_vector_type(8)));
typedef short s16x4 __attribute__((ext_vector_type(4)));

namespace {

constexpr int DM = 576, HKV = 16, SK = 8192;
constexpr int NCU = 256, NT = 512;

constexpr int INFLW = 320;
constexpr int P1W = 12;
constexpr int WQW = 16, WOW = 16;
constexpr int BASE_LVL = 2, EARLY_LVL = 4, EARLY_Q = 16;
constexpr int BOOST1 = 2, BOOST2 = 4, SLOW1 = 1;
constexpr int SCHED_TE = 7750;
constexpr int SCHED_TAU = 876;
constexpr int SCHED_Q0 = 17, SCHED_L1 = 30, SCHED_L2 = 70, SCHED_L3 = 120;
constexpr int SHIFT_O = 1, SHIFT_H = 2;
constexpr int HGATE = 800, QGATE = 1000;
constexpr int DELAY_A = 12, DELAY_B = 14, DELAY_W = 36, DELAY_C = 16, DELAY_E = 24;
constexpr int HPOLL_SLEEP = 2, QPOLL_SLEEP = 2;
#define AUX_KV "nt"
#define AUX_W "nt"

constexpr int REP_H = 8;
constexpr int WS_CTRL = 0;
constexpr int WS_ALIVE = 4096;
constexpr int WS_ST = 8192;
constexpr int WS_MODT = WS_ST + 256 * 128;
constexpr int WS_X1 = WS_MODT + 36864;
constexpr int WS_H = WS_X1 + 8 * 9216;
constexpr int WS_Q = WS_H + REP_H * 18432;
constexpr int WS_OP = WS_Q + 131072;
constexpr int WS_WP = WS_OP + 262144;
constexpr int WS_FP = WS_WP + 147456;
constexpr int WS_T0 = WS_FP + 144 * 9216;
constexpr int WS_END = WS_T0 + 8 * 128;

constexpr int MAXSL = 64 + (SHIFT_O > SHIFT_H ? SHIFT_O : SHIFT_H);
constexpr int L_RING = 0;
constexpr int L_SC = 131072;
constexpr int ACT_STRIDE = 1168;
constexpr int L_P2 = L_SC + 8192;
constexpr int L_H2 = L_SC;
constexpr int L_R2 = L_SC + 9344;
constexpr int L_HH = L_R2 + 6144;
constexpr int L_ACT = L_SC + (MAXSL * 256 > 16384 ? MAXSL * 256 : 16384);
constexpr int L_QS = L_ACT + 9344;
constexpr int L_RED = L_QS + 512;
constexpr int L_STAT = L_RED + 3072;
constexpr int L_MX = L_STAT + 64;
constexpr int L_LW = L_MX + 64;
constexpr int L_FLG = L_LW + 64;
constexpr int LDS_BYTES = 163840;
static_assert(L_FLG + 256 <= LDS_BYTES && L_HH + 640 <= L_ACT, "lds");
constexpr int F_LANDED = 0, F_FREE = 1  , F_WREL = 9  , F_SC = 12, F_P1 = 13, F_INIT = 14, F_Q = 15, F_ABORT = 16, F_ALIVE = 17,
              F_P2 = 18, F_MX = 19, F_L = 20, F_X1 = 21, F_TAGOK = 22, F_TAG = 23, F_OPART = 24, F_CB0 = 25, F_T0 = 26, F_OPUB = 27;

constexpr u32 SENT = 0xFFFFFFFFu;
constexpr u32 LDS_SPIN_CAP = 1u << 21;
constexpr u32 GLB_SPIN_CAP = 1u << 15;
constexpr float LOG2E = 1.4426950408889634f;

struct Params {
  const u16* x; const unsigned char* kc; const unsigned char* vc; const unsigned char* wq; const unsigned char* wo;
  const unsigned char* wg; const unsigned char* wu; const unsigned char* wd; const u16* c; const unsigned char* wmod;
  u16* out; unsigned char* ws;
};

__device__ __forceinline__ float bf2f(u32 v16) { return __uint_as_float(v16 << 16); }
__device__ __forceinline__ float bflo(u32 d) { return __uint_as_float(d << 16); }
__device__ __forceinline__ float bfhi(u32 d) { return __uint_as_float(d & 0xffff0000u); }
__device__ __forceinline__ float rb(float f) { return (float)(__bf16)f; }
__device__ __forceinline__ u32 f2bf(float f) { return (u32)__builtin_bit_cast(u16, (__bf16)f); }
typedef float f32x2 __attribute__((ext_vector_type(2)));
typedef __bf16 bf16x2 __attribute__((ext_vector_type(2)));
__device__ __forceinline__ u32 pack2(float lo, float hi) { return __builtin_bit_cast(u32, __builtin_convertvector(f32x2{lo, hi}, bf16x2)); }
__device__ __forceinline__ u32 nosent(u32 p) { return p < 0xFFFEFFFFu ? p : 0xFFFEFFFFu; }
__device__ __forceinline__ u32 umax(u32 a, u32 b) { return a > b ? a : b; }
__device__ __forceinline__ u32 umax4(u32x4 v) { return umax(umax(v.x, v.y), umax(v.z, v.w)); }
__device__ __forceinline__ float add_sel(u32 v, u32 sel, float acc) { return __builtin_amdgcn_fdot2_f32_bf16(__builtin_bit_cast(bf16x2, v), __builtin_bit_cast(bf16x2, sel), acc, false); }
__device__ __forceinline__ u32 vld(float f) { return __float_as_uint(f) | 1u; }
__device__ __forceinline__ f32x4 mfma(u32x4 a, u32x4 b, f32x4 c) {
  return __builtin_amdgcn_mfma_f32_16x16x32_bf16(__builtin_bit_cast(bf16x8, a), __builtin_bit_cast(bf16x8, b), c, 0, 0, 0);
}
__device__ __forceinline__ u32x4 mk_srd(const void* p, u32 bytes) {
  const u64 a = (u64)p;
  u32x4 s;
  s.x = __builtin_amdgcn_readfirstlane((u32)a);
  s.y = __builtin_amdgcn_readfirstlane((u32)(a >> 32) & 0xffffu);
  s.z = __builtin_amdgcn_readfirstlane(bytes);
  s.w = 0x00020000u;
  return s;
}
#define MEMBAR() asm volatile("" ::: "memory")
#define WAIT_LGKM0() asm volatile("s_waitcnt lgkmcnt(0)" ::: "memory")
#define WAIT_VM0() asm volatile("s_waitcnt vmcnt(0)" ::: "memory")
#define BARRIER() do { asm volatile("s_waitcnt lgkmcnt(0)" ::: "memory"); __builtin_amdgcn_s_barrier(); asm volatile("" ::: "memory"); } while (0)

#define DMA1(AUX, srd, voff, soff, ldsaddr)                                                                      \
  do { u32 keep_;                                                                                                \
    asm volatile("s_mov_b32 %0, m0\n\ts_mov_b32 m0, %1\n\ts_nop 0\n\t"                                           \
                 "buffer_load_dwordx4 %4, %2, %3 offen " AUX " lds\n\ts_mov_b32 m0, %0"                          \
                 : "=&s"(keep_) : "s"(ldsaddr), "s"(srd), "s"(soff), "v"(voff) : "memory"); } while (0)
#define DMA4(AUX, srd, v0, v1, v2, v3, soff, ldsaddr)                                                            \
  do { u32 keep_;                                                                                                \
    asm volatile("s_mov_b32 %0, m0\n\ts_mov_b32 m0, %1\n\ts_nop 0\n\t"                                           \
                 "buffer_load_dwordx4 %4, %2, %3 offen " AUX " lds\n\t"                                          \
                 "buffer_load_dwordx4 %5, %2, %3 offen offset:1024 " AUX " lds\n\t"                              \
                 "buffer_load_dwordx4 %6, %2, %3 offen offset:2048 " AUX " lds\n\t"                              \
                 "buffer_load_dwordx4 %7, %2, %3 offen offset:3072 " AUX " lds\n\ts_mov_b32 m0, %0"              \
                 : "=&s"(keep_) : "s"(ldsaddr), "s"(srd), "s"(soff), "v"(v0), "v"(v1), "v"(v2), "v"(v3) : "memory"); } while (0)

#define DMA4W(AUX, srd, voff, so, step, ldsaddr)                                                                 \
  do { u32 keep_; const u32 d_ = (step) - 1024u, s0_ = (so), s1_ = s0_ + d_, s2_ = s1_ + d_, s3_ = s2_ + d_;     \
    asm volatile("s_mov_b32 %0, m0\n\ts_mov_b32 m0, %1\n\ts_nop 0\n\t"                                          \
                 "buffer_load_dwordx4 %7, %2, %3 offen " AUX " lds\n\t"                                          \
                 "buffer_load_dwordx4 %7, %2, %4 offen offset:1024 " AUX " lds\n\t"                              \
                 "buffer_load_dwordx4 %7, %2, %5 offen offset:2048 " AUX " lds\n\t"                              \
                 "buffer_load_dwordx4 %7, %2, %6 offen offset:3072 " AUX " lds\n\ts_mov_b32 m0, %0"              \
                 : "=&s"(keep_) : "s"(ldsaddr), "s"(srd), "s"(s0_), "s"(s1_), "s"(s2_), "s"(s3_), "v"(voff) : "memory"); } while (0)

template <int CTRL> __device__ __forceinline__ float dpp(float x) {
  return __builtin_bit_cast(float, __builtin_amdgcn_mov_dpp(__builtin_bit_cast(int, x), CTRL, 0xf, 0xf, true));
}
constexpr int ROR8 = 0x128, ROR4 = 0x124, ROR2 = 0x122, ROR1 = 0x121;
__device__ __forceinline__ float xrow_max(float x) {
  auto s = __builtin_amdgcn_permlane16_swap(__float_as_uint(x), __float_as_uint(x), false, false);
  x = fmaxf(__uint_as_float(s[0]), __uint_as_float(s[1]));
  auto t = __builtin_amdgcn_permlane32_swap(__float_as_uint(x), __float_as_uint(x), false, false);
  return fmaxf(__uint_as_float(t[0]), __uint_as_float(t[1]));
}
__device__ __forceinline__ float xrow_sum(float x) {
  auto s = __builtin_amdgcn_permlane16_swap(__float_as_uint(x), __float_as_uint(x), false, false);
  x = __uint_as_float(s[0]) + __uint_as_float(s[1]);
  auto t = __builtin_amdgcn_permlane32_swap(__float_as_uint(x), __float_as_uint(x), false, false);
  return __uint_as_float(t[0]) + __uint_as_float(t[1]);
}
__device__ __forceinline__ float wave_max(float x) {
  x = fmaxf(x, dpp<ROR8>(x)); x = fmaxf(x, dpp<ROR4>(x)); x = fmaxf(x, dpp<ROR2>(x)); x = fmaxf(x, dpp<ROR1>(x));
  return xrow_max(x);
}
__device__ __forceinline__ float wave_sum(float x) {
  x += dpp<ROR8>(x); x += dpp<ROR4>(x); x += dpp<ROR2>(x); x += dpp<ROR1>(x);
  return xrow_sum(x);
}
__device__ __forceinline__ float sum_x8_16_32(float x) {
  x += dpp<ROR8>(x);
  return xrow_sum(x);
}

typedef volatile LAS u32* flagp;
__device__ __forceinline__ void lds_wait_ge(flagp F, int idx, u32 need) {
  (void)F;
  const u32 a = L_FLG + idx * 4, ab = L_FLG + F_ABORT * 4;
  u32 v, z, cnt;
  asm volatile("s_mov_b32 %2, 0\n"
               "1:\n\tds_read_b32 %0, %3\n\tds_read_b32 %1, %4\n\ts_waitcnt lgkmcnt(0)\n\t"
               "v_cmp_le_u32 vcc, %5, %0\n\ts_cbranch_vccnz 2f\n\t"
               "v_cmp_ne_u32 vcc, 0, %1\n\ts_cbranch_vccnz 2f\n\t"
               "s_sleep 1\n\ts_add_u32 %2, %2, 1\n\ts_cmp_lt_u32 %2, %6\n\ts_cbranch_scc1 1b\n\t"
               "v_mov_b32 %0, 1\n\tds_write_b32 %4, %0\n"
               "2:"
               : "=&v"(v), "=&v"(z), "=&s"(cnt) : "v"(a), "v"(ab), "v"(need), "s"(LDS_SPIN_CAP) : "vcc", "scc", "memory");
}
__device__ __forceinline__ void lds_add(int idx, u32 n) {
  const u32 a = L_FLG + idx * 4;
  asm volatile("s_waitcnt lgkmcnt(0)\n\ts_mov_b64 vcc, exec\n\ts_mov_b64 exec, 1\n\tds_add_u32 %0, %1\n\ts_mov_b64 exec, vcc"
               :: "v"(a), "v"(n) : "vcc", "memory");
}
constexpr int SC1 = 16;
typedef __amdgpu_buffer_rsrc_t rsrc_t;
__device__ __forceinline__ u32x4 ld128(rsrc_t r, int off) { return __builtin_amdgcn_raw_buffer_load_b128(r, off, 0, SC1); }
__device__ __forceinline__ u32x2 ld64(rsrc_t r, int off) { return __builtin_amdgcn_raw_buffer_load_b64(r, off, 0, SC1); }
__device__ __forceinline__ u32 ld32(rsrc_t r, int off) { return __builtin_amdgcn_raw_buffer_load_b32(r, off, 0, SC1); }
__device__ __forceinline__ void st128(rsrc_t r, int off, u32x4 v) { __builtin_amdgcn_raw_buffer_store_b128(v, r, off, 0, SC1); }
__device__ __forceinline__ void st64(rsrc_t r, int off, u32x2 v) { __builtin_amdgcn_raw_buffer_store_b64(v, r, off, 0, SC1); }

__device__ __forceinline__ u32x4 tr_frag(LAS unsigned char* L, u32 addr, u32 stride4) {
  const s16x4 a = __builtin_amdgcn_ds_read_tr16_b64_v4i16((LAS s16x4*)(L + addr));
  const s16x4 b = __builtin_amdgcn_ds_read_tr16_b64_v4i16((LAS s16x4*)(L + addr + stride4));
  const u32x2 a2 = __builtin_bit_cast(u32x2, a), b2 = __builtin_bit_cast(u32x2, b);
  return u32x4{a2.x, a2.y, b2.x, b2.y};
}
__device__ __forceinline__ u32x4 lds128(LAS unsigned char* L, u32 addr) { return *(LAS u32x4*)(L + addr); }

__device__ __forceinline__ void ln_stats(const float (&v)[10], bool tail, float& mean_r, float& sd_r) {
  float s = 0.f;
#pragma unroll
  for (int i = 0; i < 8; ++i) s += v[i];
  if (tail) s += v[8] + v[9];
  const float mean = wave_sum(s) * (1.0f / DM);
  float q = 0.f;
#pragma unroll
  for (int i = 0; i < 8; ++i) { const float d = v[i] - mean; q += d * d; }
  if (tail) { const float d0 = v[8] - mean, d1 = v[9] - mean; q += d0 * d0 + d1 * d1; }
  const float var = wave_sum(q) * (1.0f / DM);
  mean_r = rb(mean);
  sd_r = rb(sqrtf(rb(rb(var) + 1e-5f)));
}

__global__ void __launch_bounds__(NT, 2) fused_block(Params p) {
  extern __shared__ __attribute__((aligned(16))) unsigned char smem[];
  LAS unsigned char* L = (LAS unsigned char*)smem;
  flagp F = (flagp)(L + L_FLG);
  const int tid = threadIdx.x, lane = tid & 63, w = __builtin_amdgcn_readfirstlane(tid >> 6);
  const u32 t_start = (u32)__builtin_amdgcn_s_memrealtime();
  const int c = blockIdx.x, xg = c & 7, ci = c >> 3;
  const int hkv = 2 * xg + (ci >> 4), cj = ci & 15, bb = cj >> 1, half = cj & 1;
  const int pj = ci >> 1;
  const bool isF = (half == 0) | (pj >= 14), isO = !isF & (pj < 9);
  const int hu = xg * 5 + (pj - 9);
  const bool isH = !isF & !isO & (hu < 36);
  const int f = xg * 18 + pj + 2 * half;
  const int u = pj;
  const int mu = xg * 9 + u;
  const bool isQ = isO & (u < 8);
  const int shift = (int)(pj < 8) * SHIFT_O + (int)((pj >= 9) & (pj < 14) & (hu < 36)) * SHIFT_H;
  const int nsl = 64 + shift - 2 * shift * half;
  const int key0 = half * (64 + shift) * 64;
  const u32 Wtot = 72u * (u32)isF + 136u * (u32)isO + 108u * (u32)isH;
  const int roff = (int)isF * (5 + (int)(shift == 1)) + (int)isH * 7;
  const int posd = (2 * nsl + roff) & 7;
  const int g = lane >> 4, r16 = lane & 15, qp = (lane & 15) >> 2, pp = lane & 3;
  const rsrc_t rs = __builtin_amdgcn_make_buffer_rsrc((void*)p.ws, 0, WS_END, 0x00020000);

  u32x4 wf[18];
  u32 cbp[5], sbp[5];
  u32x4 pre4a, pre4b;
  float osA = 0.f, osB = 0.f;
  u32 nfin = 0;

  auto load_cbsb = [&](int row, u32 (&cb)[5], u32 (&sb)[5]) {
    u32 spins = 0;
#pragma unroll 1
    for (;;) {
      MEMBAR();
      const int base = WS_MODT + row * 4608;
#pragma unroll
      for (int i = 0; i < 4; ++i) { sb[i] = ld32(rs, base + (288 + lane + 64 * i) * 4); cb[i] = ld32(rs, base + (576 + lane + 64 * i) * 4); }
      sb[4] = ld32(rs, base + (288 + 256 + (lane & 31)) * 4); cb[4] = ld32(rs, base + (576 + 256 + (lane & 31)) * 4);
      u32 mx = 0;
#pragma unroll
      for (int i = 0; i < 5; ++i) mx = umax(mx, umax(sb[i], cb[i]));
      if (!((u32)!__all(mx != SENT) & (u32)(++spins < GLB_SPIN_CAP))) break;
    }
#pragma unroll
    for (int i = 0; i < 5; ++i) cb[i] = pack2(1.0f + bflo(cb[i]), 1.0f + bfhi(cb[i]));
  };
  auto get_tag = [&]() { lds_wait_ge(F, F_TAGOK, 1u); return (u32)F[F_TAG]; };
  auto q_partial = [&](int kt, f32x4 (&acc)[4]) {
    lds_wait_ge(F, F_LANDED, 72u);
    u32x4 pq[24];
    const u32 a0 = L_RING + (kt * 192 + 8 * g + qp) * 128 + 8 * pp;
#pragma unroll
    for (int s = 0; s < 6; ++s)
#pragma unroll
      for (int ct = 0; ct < 4; ++ct) pq[s * 4 + ct] = tr_frag(L, a0 + s * 4096 + ct * 32, 4 * 128);
    lds_add(F_WREL + 0, 1);
    const u32 tag = get_tag();
    u32x4 hv[6];
    u32 spins = 0;
    const int hb = WS_H + xg * 18432 + kt * 6144 + lane * 16;
    int lo[6];
#pragma unroll
    for (int i = 0; i < 6; ++i) { const int e = i * 64 + lane; lo[i] = L_ACT + ((e >> 2) & 7) * ACT_STRIDE + kt * 384 + ((e >> 5) * 4 + (e & 3)) * 8; }
    while ((u32)__builtin_amdgcn_s_memrealtime() - t_start < (u32)HGATE) __builtin_amdgcn_s_sleep(2);
#pragma unroll 1
    for (;;) {
      MEMBAR();
#pragma unroll
      for (int i = 0; i < 6; ++i) hv[i] = ld128(rs, hb + i * 1024);
      u32 bad = 0;
#pragma unroll
      for (int i = 0; i < 6; ++i) bad |= (hv[i].y ^ tag) | (hv[i].w ^ tag);
      if (!((u32)!__all(bad == 0u) & (u32)(++spins < GLB_SPIN_CAP))) break;
      __builtin_amdgcn_s_sleep(HPOLL_SLEEP);
    }
#pragma unroll
    for (int i = 0; i < 6; ++i) *(LAS u32x2*)(L + lo[i]) = u32x2{hv[i].x, hv[i].z};
    WAIT_LGKM0();
    const u32 ab = L_ACT + (lane & 7) * ACT_STRIDE + kt * 384 + g * 16;
#pragma unroll
    for (int ct = 0; ct < 4; ++ct) acc[ct] = f32x4{0, 0, 0, 0};
#pragma unroll
    for (int s = 0; s < 6; ++s) {
      const u32x4 bfr = lds128(L, ab + s * 64);
#pragma unroll
      for (int ct = 0; ct < 4; ++ct) acc[ct] = mfma(pq[s * 4 + ct], bfr, acc[ct]);
    }
  };

  if (w == 0) {
    __builtin_amdgcn_s_setprio(3);
    if (lane < 32) F[lane] = 0u;
    u32 n = 0;
    u32x4 srd0; u32 v0, step0, pre;
    if (isF) { srd0 = mk_srd(p.wg + f * 64, DM * 9216 - f * 64); v0 = (lane >> 2) * 9216 + (lane & 3) * 16; step0 = 16 * 9216; pre = 16; }
    else if (isQ) { srd0 = mk_srd(p.wq + (xg * 512 + u * 64) * 2, DM * 8192 - (xg * 512 + u * 64) * 2); v0 = (lane >> 3) * 8192 + (lane & 7) * 16; step0 = 8 * 8192; pre = WQW - 4; }
    else if (isH) { srd0 = mk_srd(p.wmod + hu * 32, DM * 6912 - hu * 32); v0 = (lane >> 1) * 6912 + (lane & 1) * 16; step0 = 32 * 6912; pre = 8; }
    else { srd0 = mk_srd(p.wq, 0); v0 = 0; step0 = 1024; pre = 0; }
#pragma unroll 1
    for (; n < pre; n += 4) DMA4W(AUX_W, srd0, v0, n * step0, step0, L_RING + n * 1024);
    BARRIER();
    int pub = 0;
    auto run = [&](auto inflp, const u32x4& srd, u32 voff, u32 step, u32 cnt, u32 lds0, u32 first = 0) {
      constexpr int NP = decltype(inflp)::value;
      constexpr int KEEP = NP >= 8 ? NP - 4 : NP / 2;
      u32 s = first;
#pragma unroll 1
      for (; s + 4 <= cnt; s += 4) {
        DMA4W(AUX_W, srd, voff, s * step, step, lds0 + s * 1024);
        n += 4;
        asm volatile("s_waitcnt vmcnt(%0)" :: "i"(KEEP) : "memory");
        pub = max(pub, (int)n - KEEP); F[F_LANDED] = (u32)pub;
      }
#pragma unroll 1
      for (; s < cnt; ++s) { DMA1(AUX_W, srd, voff, s * step, lds0 + s * 1024); ++n; }
    };
    typedef std::integral_constant<int, INFLW / 16> P16;
    typedef std::integral_constant<int, (INFLW / 8 > 32 ? 32 : INFLW / 8)> P8;
    if (isF) {
      run(P16(), srd0, v0, step0, 36, L_RING, pre);
      run(P16(), mk_srd(p.wu + f * 64, DM * 9216 - f * 64), v0, step0, 36, L_RING + 36 * 1024);
    } else if (isO) {
      if (isQ) run(std::integral_constant<int, WQW>(), srd0, v0, step0, 72, L_RING, pre);
      else n = 72;
      run(std::integral_constant<int, WOW>(), mk_srd(p.wo + xg * 512 * 1152 + u * 128, 512 * 1152 - u * 128), (lane >> 3) * 1152 + (lane & 7) * 16, 8 * 1152, 64, L_RING + 72 * 1024);
    } else if (isH) {
      const u32 vm32 = (lane >> 1) * 6912 + (lane & 1) * 16;
      run(std::integral_constant<int, P1W>(), srd0, v0, step0, 18, L_RING, pre);
      run(std::integral_constant<int, P1W>(), mk_srd(p.wmod + 1152 + hu * 32, DM * 6912 - 1152 - hu * 32), vm32, 32 * 6912, 18, L_RING + 18 * 1024);
      run(P8(), mk_srd(p.wmod + 2304 + hu * 128, DM * 6912 - 2304 - hu * 128), (lane >> 3) * 6912 + (lane & 7) * 16, 8 * 6912, 72, L_RING + 36 * 1024);
    }
    const size_t kvoff = ((size_t)(bb * HKV + hkv) * SK + (size_t)key0) * 256;
    const u32x4 srd_k = mk_srd(p.kc + kvoff, (u32)nsl * 16384u), srd_v = mk_srd(p.vc + kvoff, (u32)nsl * 16384u);
    u32 vk[4], vv[2];
#pragma unroll
    for (int j = 0; j < 4; ++j) vk[j] = g * 256 + ((r16 ^ (4 * j + g)) << 4);
#pragma unroll
    for (int j = 0; j < 2; ++j) vv[j] = g * 256 + ((r16 ^ ((g | (j << 2)) << 1)) << 4);
    const u32 wneed = isF ? 0x00022211u : isO ? 0x33221111u : isH ? 0x02222211u : 0u;
    auto wait_window = [&](u32 N) {
      asm volatile("s_waitcnt vmcnt(28)\n\ts_cmp_ge_u32 %0, 28\n\ts_cbranch_scc1 9f\n\t"
                   "s_waitcnt vmcnt(24)\n\ts_cmp_ge_u32 %0, 24\n\ts_cbranch_scc1 9f\n\t"
                   "s_waitcnt vmcnt(20)\n\ts_cmp_ge_u32 %0, 20\n\ts_cbranch_scc1 9f\n\t"
                   "s_waitcnt vmcnt(16)\n9:" :: "s"(N) : "scc", "memory");
    };
    u32 fr_next = 0;
    u32 t_prev = t_start, t_ref = t_start;
    auto wait_free = [&](int pos, u32 need, u32 seen) {
      if (seen < need && F[F_FREE + pos] < need) { WAIT_VM0(); F[F_LANDED] = n; pub = (int)n; lds_wait_ge(F, F_FREE + pos, need); }
    };
#pragma unroll 1
    for (int q = 0; q < 2 * nsl; ++q) {
      const int pos = (q + roff) & 7;
      if (q >= 8) wait_free(pos, (u32)(q - 7), fr_next);
      else {
        const int nb = (wneed >> (4 * pos)) & 15;
#pragma unroll 1
        for (int i = 0; i < nb; ++i)
          if (F[F_WREL + i] < 7u) { WAIT_VM0(); F[F_LANDED] = n; pub = (int)n; lds_wait_ge(F, F_WREL + i, 7u); }
      }
      int lvl = q < EARLY_Q ? EARLY_LVL : BASE_LVL;
      if (q == SCHED_Q0) { const u32 t0_ = __builtin_amdgcn_readfirstlane((u32)F[F_T0]); if (t0_) t_ref = t0_; }
      if (q >= SCHED_Q0) {
        const u32 eown = t_prev + (u32)(((2 * nsl - q + 1) * SCHED_TAU) >> 4);
        const int lag = (int)(eown - t_ref) - SCHED_TE - SCHED_L1;
        if (lag > 0) lvl = BASE_LVL + BOOST1;
        if (lag > SCHED_L2) lvl = BASE_LVL + BOOST2;
        if (lag < -SCHED_L3) lvl = BASE_LVL - SLOW1;
      }
      t_prev = (u32)__builtin_amdgcn_s_memrealtime();
      const u32 N2 = 16u + 4u * (u32)((lvl + 1) >> 1), N = 16u + 4u * (u32)(lvl >> 1);
      fr_next = F[F_FREE + ((pos + 1) & 7)];
      const u32 lb = L_RING + pos * 16384;
      const bool isk = q < nsl;
      const u32x4 srd = isk ? srd_k : srd_v;
      const u32 v0_ = isk ? vk[0] : vv[0], v1_ = isk ? vk[1] : vv[0], v2_ = isk ? vk[2] : vv[1], v3_ = isk ? vk[3] : vv[1];
      const u32 so = (u32)(isk ? q : q - nsl) * 16384u;
#pragma unroll
      for (int gq = 0; gq < 4; ++gq) {
        DMA4(AUX_KV, srd, v0_, v1_, v2_, v3_, so + gq * 4096, lb + gq * 4096);
        n += 4;
        const u32 Ng = gq < 2 ? N2 : N;
        wait_window(Ng);
        pub = max(pub, (int)n - (int)Ng); F[F_LANDED] = (u32)pub;
      }
    }
    if (isF) {
      for (int i = 0; i < 3; ++i) wait_free(posd + i, (u32)(2 * nsl - 7 + i), 0u);
      run(P8(), mk_srd(p.wd + f * 36864, 36864), (u32)(lane * 16), 1024, 36, L_RING + posd * 16384);
    }
    if (!isF) { WAIT_VM0(); F[F_LANDED] = n; }
    nfin = n;
    __builtin_amdgcn_s_setprio(0);
    if (isF) {
      lds_wait_ge(F, F_CB0, 1u);
#pragma unroll
      for (int i = 0; i < 5; ++i) { cbp[i] = *(LAS u32*)(L + L_RED + i * 256 + lane * 4); sbp[i] = *(LAS u32*)(L + L_RED + 1280 + i * 256 + lane * 4); }
    }
  } else if (w == 7) {
    BARRIER();
    const u64 ctrl = __hip_atomic_load((GAS u64*)(p.ws + WS_CTRL), __ATOMIC_RELAXED, __HIP_MEMORY_SCOPE_AGENT);
    const u64 epoch = (((u64)__builtin_amdgcn_readfirstlane((u32)(ctrl >> 32)) << 32) | (u32)__builtin_amdgcn_readfirstlane((u32)ctrl)) + 1ull;
    const u32 tag = (u32)epoch;
    F[F_TAG] = tag;
    WAIT_LGKM0();
    F[F_TAGOK] = 1u;
    if (c == 0 && lane < 8) st64(rs, WS_T0 + lane * 128, u32x2{t_start, tag});
    const u32x4 z4 = {0u, 0u, 0u, 0u}, s4 = {SENT, SENT, SENT, SENT};
    if (isH) {
#pragma unroll
      for (int i = 0; i < 2; ++i) st64(rs, WS_MODT + (lane & 7) * 4608 + hu * 128 + (2 * i + (r16 >> 3)) * 32 + g * 8, u32x2{SENT, SENT});
    }
    st128(rs, WS_OP + c * 1024 + lane * 16, z4);
    if (lane == 0) st128(rs, WS_ST + c * 128, z4);
    if (isO) {
#pragma unroll
      for (int i = 0; i < 2; ++i) st128(rs, WS_WP + (xg * 144 + u * 16) * 128 + i * 1024 + lane * 16, z4);
      st128(rs, WS_X1 + (lane >> 3) * 9216 + mu * 128 + (lane & 7) * 16, s4);
    }
    if (isF) {
#pragma unroll 1
      for (int i = 0; i < 9; ++i) st128(rs, WS_FP + f * 9216 + i * 1024 + lane * 16, s4);
    }
    WAIT_VM0();
    F[F_INIT] = 1u;
    if (lane == 0) __hip_atomic_store((GAS u64*)(p.ws + WS_ALIVE) + xg * 32 + ci, epoch, __ATOMIC_RELAXED, __HIP_MEMORY_SCOPE_AGENT);
    if (isQ) {
      f32x4 acc[4];
      q_partial(2, acc);
      lds_add(F_WREL + 1, 1); lds_add(F_WREL + 2, 1);
      lds_wait_ge(F, F_P2, 2u);
      const int ls = (g * 8 + (lane & 7)) * 16;
#pragma unroll
      for (int ct = 0; ct < 4; ++ct) {
        const f32x4 a = (*(LAS f32x4*)(L + L_P2 + ct * 512 + ls) + *(LAS f32x4*)(L + L_P2 + 2048 + ct * 512 + ls)) + acc[ct];
        if (r16 < 8) st128(rs, WS_Q + ((2 * xg + (u >> 2)) * 8 + r16) * 1024 + ((u & 3) * 16 + ct * 4 + g) * 16, u32x4{pack2(a[0], a[1]), tag, pack2(a[2], a[3]), tag});
      }
    } else {
      lds_add(F_WREL + 0, 1); lds_add(F_WREL + 1, 1); lds_add(F_WREL + 2, 1);
    }
    {
      u32x4 qv; u32x2 t0v = {0u, 0u}; u32 spins = 0;
      while ((u32)__builtin_amdgcn_s_memrealtime() - t_start < (u32)QGATE) __builtin_amdgcn_s_sleep(2);
#pragma unroll 1
      for (;;) {
        MEMBAR();
        qv = ld128(rs, WS_Q + (hkv * 8 + bb) * 1024 + lane * 16);
        t0v = ld64(rs, WS_T0 + xg * 128);
        if (!((u32)!__all(((qv.y ^ tag) | (qv.w ^ tag)) == 0u) & (u32)(++spins < GLB_SPIN_CAP))) break;
        __builtin_amdgcn_s_sleep(QPOLL_SLEEP);
      }
      *(LAS u32x2*)(L + L_QS + lane * 8) = u32x2{qv.x, qv.z};
      F[F_T0] = t0v.y == tag ? t0v.x : t_start;
      WAIT_LGKM0();
      F[F_Q] = 1u;
    }
    lds_wait_ge(F, F_LANDED, Wtot + 16u * (u32)(nsl - 8));
    {
      const u32 ehi = (u32)(epoch >> 32);
      u32 spins = 0;
#pragma unroll 1
      for (;;) {
        MEMBAR();
        const u32x4 a = ld128(rs, WS_ALIVE + lane * 32), b2 = ld128(rs, WS_ALIVE + lane * 32 + 16);
        const u32 bad = (a.x ^ tag) | (a.y ^ ehi) | (a.z ^ tag) | (a.w ^ ehi) | (b2.x ^ tag) | (b2.y ^ ehi) | (b2.z ^ tag) | (b2.w ^ ehi);
        if (!((u32)!__all(bad == 0u) & (u32)(++spins < GLB_SPIN_CAP))) break;
        __builtin_amdgcn_s_sleep(64);
      }
    }
    F[F_ALIVE] = 1u;
    if (c == 0 && lane == 0) __hip_atomic_store((GAS u64*)(p.ws + WS_CTRL), epoch, __ATOMIC_RELAXED, __HIP_MEMORY_SCOPE_AGENT);
    {
      lds_wait_ge(F, F_L, 6u);
      const int hd = lane & 1;
      float M = *(LAS float*)(L + L_MX + hd * 4), Ls = *(LAS float*)(L + L_LW + hd * 4);
#pragma unroll
      for (int i = 1; i < 6; ++i) { M = fmaxf(M, *(LAS float*)(L + L_MX + i * 8 + hd * 4)); Ls += *(LAS float*)(L + L_LW + i * 8 + hd * 4); }
      if (lane < 2) st64(rs, WS_ST + c * 128 + hd * 8, u32x2{vld(M), vld(Ls)});
      u32x2 o; u32 spins = 0;
#pragma unroll 1
      for (;;) {
        MEMBAR();
        o = ld64(rs, WS_ST + (c ^ 8) * 128 + hd * 8);
        if (!((u32)!__all((o.x & o.y & 1u) != 0u) & (u32)(++spins < GLB_SPIN_CAP))) break;
        __builtin_amdgcn_s_sleep(8);
      }
      const float m0 = __uint_as_float(vld(M)), l0 = __uint_as_float(vld(Ls)), m1 = __uint_as_float(o.x), l1 = __uint_as_float(o.y);
      const float Mg = fmaxf(m0, m1);
      const float e0 = __builtin_amdgcn_exp2f((m0 - Mg) * LOG2E), e1 = __builtin_amdgcn_exp2f((m1 - Mg) * LOG2E);
      const float sc = e0 / (e0 * l0 + e1 * l1);
      osA = __builtin_bit_cast(float, __builtin_amdgcn_readlane(__builtin_bit_cast(int, sc), 0));
      osB = __builtin_bit_cast(float, __builtin_amdgcn_readlane(__builtin_bit_cast(int, sc), 1));
    }
    if (isF) {
      load_cbsb(0, cbp, sbp);
#pragma unroll
      for (int i = 0; i < 5; ++i) { *(LAS u32*)(L + L_RED + i * 256 + lane * 4) = cbp[i]; *(LAS u32*)(L + L_RED + 1280 + i * 256 + lane * 4) = sbp[i]; }
      WAIT_LGKM0();
      F[F_CB0] = 1u;
      load_cbsb(7, cbp, sbp);
      u32x2 gb; u32 spins = 0;
#pragma unroll 1
      for (;;) {
        MEMBAR();
        gb = ld64(rs, WS_MODT + (lane & 7) * 4608 + 864 * 4 + f * 8);
        if (!((u32)!__all(umax(gb.x, gb.y) != SENT) & (u32)(++spins < GLB_SPIN_CAP))) break;
      }
      pre4a = u32x4{gb.x, gb.y, 0u, 0u};
    } else if (isO) {
      u32 spins = 0;
#pragma unroll 1
      for (;;) {
        MEMBAR();
        pre4a = ld128(rs, WS_MODT + (lane & 7) * 4608 + mu * 16);
        if (!((u32)!__all(umax4(pre4a) != SENT) & (u32)(++spins < GLB_SPIN_CAP))) break;
      }
      pre4b = *(const GAS u32x4*)(p.x + (lane & 7) * DM + mu * 8);
    }
  } else {
    const int cw = w - 1;
    if (isF) {
      BARRIER();
      const int m = cw & 1, kt = cw >> 1;
      const u32 a0 = L_RING + 36 * m * 1024 + (kt * 192 + 8 * g + qp) * 64 + 8 * pp;
      if (m == 1) lds_add(F_WREL + 0, 1);
      lds_wait_ge(F, F_LANDED, m == 0 ? 36u : 72u);
#pragma unroll
      for (int s = 0; s < 6; ++s) { wf[2 * s] = tr_frag(L, a0 + s * 2048, 4 * 64); wf[2 * s + 1] = tr_frag(L, a0 + s * 2048 + 32, 4 * 64); }
      if (m == 0) lds_add(F_WREL + 0, 1);
      lds_add(F_WREL + 1, 1);
      lds_add(F_WREL + 2, 1);
    } else if (isO) {
      BARRIER();
      if (cw < 4) {
        lds_add(F_WREL + 0, 1);
        const u32 a0 = L_RING + 72 * 1024 + (8 * g + qp) * 128 + cw * 32 + 8 * pp;
        lds_wait_ge(F, F_LANDED, 104u);
#pragma unroll
        for (int s = 0; s < 8; ++s) wf[s] = tr_frag(L, a0 + s * 4096, 4 * 128);
        lds_add(F_WREL + 1, 1);
        lds_wait_ge(F, F_LANDED, 136u);
#pragma unroll
        for (int s = 8; s < 16; ++s) wf[s] = tr_frag(L, a0 + s * 4096, 4 * 128);
        lds_add(F_WREL + 2, 1);
      } else {
        if (isQ) {
          f32x4 acc[4];
          q_partial(cw - 4, acc);
          if (r16 < 8) {
#pragma unroll
            for (int ct = 0; ct < 4; ++ct) *(LAS f32x4*)(L + L_P2 + (cw - 4) * 2048 + ct * 512 + (g * 8 + r16) * 16) = acc[ct];
          }
          lds_add(F_P2, 1);
        } else lds_add(F_WREL + 0, 1);
        lds_add(F_WREL + 1, 1); lds_add(F_WREL + 2, 1);
      }
    } else if (isH) {
      u32 cv[6], xa[5], xb[5];
#pragma unroll
      for (int i = 0; i < 6; ++i) cv[i] = ((const GAS u32*)p.c)[cw * 64 + lane + 384 * i];
      const int rowb = 6 + (cw & 1);
      const GAS u32* xr = (const GAS u32*)p.x + cw * 288;
      const GAS u32* xs = (const GAS u32*)p.x + rowb * 288;
#pragma unroll
      for (int i = 0; i < 4; ++i) { xa[i] = xr[lane + 64 * i]; xb[i] = xs[lane + 64 * i]; }
      xa[4] = xr[256 + (lane & 31)]; xb[4] = xs[256 + (lane & 31)];
      const u32x2 xq = *(const GAS u32x2*)(p.x + (lane & 7) * DM + hu * 16 + g * 4);
      BARRIER();
#pragma unroll
      for (int i = 0; i < 6; ++i) {
        const int e = cw * 64 + lane + 384 * i;
        const float a = bflo(cv[i]), b2 = bfhi(cv[i]);
        *(LAS u32*)(L + L_SC + (e / 288) * ACT_STRIDE + (e % 288) * 4) = pack2(a / (1.0f + __expf(-a)), b2 / (1.0f + __expf(-b2)));
      }
      {
        float v[10], mean_r, sd_r;
#pragma unroll
        for (int i = 0; i < 5; ++i) { v[2 * i] = bflo(xa[i]); v[2 * i + 1] = bfhi(xa[i]); }
        ln_stats(v, lane < 32, mean_r, sd_r);
        if (lane == 0) { *(LAS float*)(L + L_STAT + cw * 8) = mean_r; *(LAS float*)(L + L_STAT + cw * 8 + 4) = sd_r; }
        if (cw < 2) {
#pragma unroll
          for (int i = 0; i < 5; ++i) { v[2 * i] = bflo(xb[i]); v[2 * i + 1] = bfhi(xb[i]); }
          ln_stats(v, lane < 32, mean_r, sd_r);
          if (lane == 0) { *(LAS float*)(L + L_STAT + rowb * 8) = mean_r; *(LAS float*)(L + L_STAT + rowb * 8 + 4) = sd_r; }
        }
      }
      lds_add(F_SC, 1);
      const u32 sb0 = L_SC + (lane & 7) * ACT_STRIDE + g * 16;
      {
        const int m = cw & 1, kt = cw >> 1;
        lds_wait_ge(F, F_SC, 6u);
        const float mean_r = *(LAS float*)(L + L_STAT + (lane & 7) * 8), sd_r = *(LAS float*)(L + L_STAT + (lane & 7) * 8 + 4);
        float n0 = rb(rb(bflo(xq.x) - mean_r) / sd_r), n1 = rb(rb(bfhi(xq.x) - mean_r) / sd_r);
        float n2 = rb(rb(bflo(xq.y) - mean_r) / sd_r), n3 = rb(rb(bfhi(xq.y) - mean_r) / sd_r);
        asm volatile("" : "+v"(n0), "+v"(n1), "+v"(n2), "+v"(n3));
        u32 tag = 0;
        if (cw == 5) tag = get_tag();
        u32x4 bfr[6];
#pragma unroll
        for (int s = 0; s < 6; ++s) bfr[s] = lds128(L, sb0 + (kt * 6 + s) * 64);
        lds_wait_ge(F, F_LANDED, 36u);
        const u32 a0 = L_RING + (m * 18 + kt * 6) * 1024 + (8 * g + qp) * 32 + 8 * pp;
        f32x4 acc = {0, 0, 0, 0}, acc2 = {0, 0, 0, 0};
#pragma unroll
        for (int s = 0; s < 6; s += 2) { acc = mfma(tr_frag(L, a0 + s * 1024, 4 * 32), bfr[s], acc); acc2 = mfma(tr_frag(L, a0 + (s + 1) * 1024, 4 * 32), bfr[s + 1], acc2); }
        acc += acc2;
        if (r16 < 8) *(LAS f32x4*)(L + L_RED + cw * 512 + (g * 8 + r16) * 16) = acc;
        lds_add(F_P1, 1);
        lds_add(F_WREL + 0, 1);
        if (cw == 5) {
          lds_wait_ge(F, F_P1, 6u);
          const int ls = (g * 8 + (lane & 7)) * 16;
          const f32x4 sa = *(LAS f32x4*)(L + L_RED + 0 * 512 + ls) + *(LAS f32x4*)(L + L_RED + 2 * 512 + ls) + *(LAS f32x4*)(L + L_RED + 4 * 512 + ls);
          const f32x4 ca = *(LAS f32x4*)(L + L_RED + 1 * 512 + ls) + *(LAS f32x4*)(L + L_RED + 3 * 512 + ls) + *(LAS f32x4*)(L + L_RED + 5 * 512 + ls);
          const float h0 = rb(rb(n0 * rb(1.0f + rb(ca[0]))) + rb(sa[0])), h1 = rb(rb(n1 * rb(1.0f + rb(ca[1]))) + rb(sa[1]));
          const float h2 = rb(rb(n2 * rb(1.0f + rb(ca[2]))) + rb(sa[2])), h3 = rb(rb(n3 * rb(1.0f + rb(ca[3]))) + rb(sa[3]));
          const u32x4 hu4 = {pack2(h0, h1), tag, pack2(h2, h3), tag};
#pragma unroll
          for (int i = 0; i < REP_H / 2; ++i)
            st128(rs, WS_H + (2 * i + (r16 >> 3)) * 18432 + hu * 512 + (lane & 7) * 64 + g * 16, hu4);
        }
      }
      if (cw < 4) {
        lds_wait_ge(F, F_LANDED, 108u);
        const u32 a0 = L_RING + 36 * 1024 + (8 * g + qp) * 128 + cw * 32 + 8 * pp;
        f32x4 a0c = {0, 0, 0, 0}, a1c = {0, 0, 0, 0};
#pragma unroll 3
        for (int s = 0; s < 18; s += 2) {
          a0c = mfma(tr_frag(L, a0 + s * 4096, 4 * 128), lds128(L, sb0 + s * 64), a0c);
          a1c = mfma(tr_frag(L, a0 + (s + 1) * 4096, 4 * 128), lds128(L, sb0 + (s + 1) * 64), a1c);
        }
        const f32x4 a = a0c + a1c;
        lds_add(F_WREL + 1, 1);
        lds_wait_ge(F, F_INIT, 1u);
        if (r16 < 8) st64(rs, WS_MODT + r16 * 4608 + hu * 128 + cw * 32 + g * 8, u32x2{nosent(pack2(a[0], a[1])), nosent(pack2(a[2], a[3]))});
      } else lds_add(F_WREL + 1, 1);
      lds_add(F_WREL + 2, 1);
    } else {
      BARRIER();
      lds_add(F_WREL + 0, 1); lds_add(F_WREL + 1, 1); lds_add(F_WREL + 2, 1);
    }
    u32x4 qf[4];
    u32 ka[4];
#pragma unroll
    for (int s = 0; s < 4; ++s) ka[s] = r16 * 256 + (((4 * s + g) ^ r16) << 4);
    float mx0 = -INFINITY, mx1 = -INFINITY;
    auto k_read = [&](int q, u32x4* kf) {
      const int pos = (q + roff) & 7;
      lds_wait_ge(F, F_LANDED, Wtot + 16u * (u32)(q + 1));
      const u32 sb = L_RING + pos * 16384;
#pragma unroll
      for (int t = 0; t < 4; ++t)
#pragma unroll
        for (int s = 0; s < 4; ++s) kf[t * 4 + s] = lds128(L, sb + ka[s] + t * 4096);
      WAIT_LGKM0();
      if (lane == 0) F[F_FREE + pos] = (u32)(q + 1);
    };
    auto k_scores = [&](int q, const u32x4* kf) {
      f32x4 acc[4];
#pragma unroll
      for (int t = 0; t < 4; ++t) {
        acc[t] = f32x4{0, 0, 0, 0};
#pragma unroll
        for (int s = 0; s < 4; ++s) acc[t] = mfma(qf[s], kf[t * 4 + s], acc[t]);
      }
      float s0 = g == 0 ? acc[0][0] : g == 1 ? acc[1][0] : g == 2 ? acc[2][0] : acc[3][0];
      float s1 = g == 0 ? acc[0][1] : g == 1 ? acc[1][1] : g == 2 ? acc[2][1] : acc[3][1];
      s0 = rb(rb(s0) * 0.08838834764831845f);
      s1 = rb(rb(s1) * 0.08838834764831845f);
      mx0 = fmaxf(mx0, s0); mx1 = fmaxf(mx1, s1);
      *(LAS u16*)(L + L_SC + q * 256 + lane * 2) = (u16)(__float_as_uint(s0) >> 16);
      *(LAS u16*)(L + L_SC + q * 256 + 128 + lane * 2) = (u16)(__float_as_uint(s1) >> 16);
    };
    {
      u32x4 kf[16];
      k_read(cw, kf);
      lds_wait_ge(F, F_Q, 1u);
      lds_wait_ge(F, F_WREL + 1, 7u); lds_wait_ge(F, F_WREL + 2, 7u);
#pragma unroll
      for (int s = 0; s < 4; ++s) qf[s] = lds128(L, L_QS + (lane & 1) * 256 + s * 64 + g * 16);
      k_scores(cw, kf);
    }
#pragma unroll 1
    for (int q = cw + 6; q < nsl; q += 6) {
      u32x4 kf[16];
      k_read(q, kf);
      k_scores(q, kf);
    }
    {
      mx0 = wave_max(mx0); mx1 = wave_max(mx1);
      if (lane == 0) *(LAS u32x2*)(L + L_MX + cw * 8) = u32x2{__float_as_uint(mx0), __float_as_uint(mx1)};
      lds_add(F_MX, 1);
      lds_wait_ge(F, F_MX, 6u);
#pragma unroll
      for (int i = 0; i < 6; ++i) { mx0 = fmaxf(mx0, *(LAS float*)(L + L_MX + i * 8)); mx1 = fmaxf(mx1, *(LAS float*)(L + L_MX + i * 8 + 4)); }
      float l0 = 0.f, l1 = 0.f;
#pragma unroll 1
      for (int q = cw; q < nsl; q += 6) {
        const float s0 = bf2f(*(LAS u16*)(L + L_SC + q * 256 + lane * 2)), s1 = bf2f(*(LAS u16*)(L + L_SC + q * 256 + 128 + lane * 2));
        const float p0 = __builtin_amdgcn_exp2f((s0 - mx0) * LOG2E), p1 = __builtin_amdgcn_exp2f((s1 - mx1) * LOG2E);
        l0 += p0; l1 += p1;
        *(LAS u16*)(L + L_SC + q * 256 + lane * 2) = (u16)f2bf(p0);
        *(LAS u16*)(L + L_SC + q * 256 + 128 + lane * 2) = (u16)f2bf(p1);
      }
      l0 = wave_sum(l0); l1 = wave_sum(l1);
      if (lane == 0) *(LAS u32x2*)(L + L_LW + cw * 8) = u32x2{__float_as_uint(l0), __float_as_uint(l1)};
      lds_add(F_L, 1);
    }
    f32x4 o[8];
#pragma unroll
    for (int cb = 0; cb < 8; ++cb) o[cb] = f32x4{0, 0, 0, 0};
    const u32 va0 = (8 * g + qp) * 256 + ((pp >> 1) << 4) + 8 * (pp & 1) + (((qp | ((g & 1) << 2)) << 1) << 4);
#pragma unroll 1
    for (int q = cw; q < nsl; q += 6) {
      const int pos = (q + nsl + roff) & 7;
      if (isF && q >= 24 && q < 30) { lds_wait_ge(F, F_ALIVE, 1u); load_cbsb(w, cbp, sbp); }
      const u32x4 pf0 = lds128(L, L_SC + q * 256 + (lane & 1) * 128 + g * 16), pf1 = lds128(L, L_SC + q * 256 + (lane & 1) * 128 + 64 + g * 16);
      lds_wait_ge(F, F_LANDED, Wtot + 16u * (u32)(q + nsl + 1));
      const u32 sb = L_RING + pos * 16384 + va0;
      u32x4 vf[8], vg2[8];
#pragma unroll
      for (int cb = 0; cb < 8; ++cb) vf[cb] = tr_frag(L, sb ^ (cb << 5), 1024);
#pragma unroll
      for (int cb = 0; cb < 8; ++cb) vg2[cb] = tr_frag(L, (sb ^ (cb << 5)) + 8192, 1024);
      WAIT_LGKM0();
      if (lane == 0) F[F_FREE + pos] = (u32)(q + nsl + 1);
#pragma unroll
      for (int cb = 0; cb < 8; ++cb) o[cb] = mfma(pf0, vf[cb], o[cb]);
#pragma unroll
      for (int cb = 0; cb < 8; ++cb) o[cb] = mfma(pf1, vg2[cb], o[cb]);
    }
    if (g == 0) {
#pragma unroll
      for (int cb = 0; cb < 8; ++cb) *(LAS u32x2*)(L + L_ACT + cw * 1024 + (cb * 16 + r16) * 8) = u32x2{__float_as_uint(o[cb][0]), __float_as_uint(o[cb][1])};
    }
    lds_add(F_OPART, 1);
  }
  if (w == 7) {
    lds_wait_ge(F, F_OPART, 6u);
    f32x4 O = *(LAS f32x4*)(L + L_ACT + lane * 16);
#pragma unroll
    for (int i = 1; i < 6; ++i) O += *(LAS f32x4*)(L + L_ACT + i * 1024 + lane * 16);
    st128(rs, WS_OP + c * 1024 + lane * 16, u32x4{vld(O[0] * osA), vld(O[1] * osB), vld(O[2] * osA), vld(O[3] * osB)});
    F[F_OPUB] = 1u;
  }
  if (isO) {
    constexpr int L_OS = L_SC;
    u32x4 ov[4];
    bool got[4] = {false, false, false, false};
    u32 spins = 0;
    lds_wait_ge(F, F_ALIVE, 1u);
    lds_wait_ge(F, F_OPUB, 1u); __builtin_amdgcn_s_sleep(DELAY_A);
#pragma unroll 1
    for (;;) {
      MEMBAR();
#pragma unroll
      for (int i = 0; i < 4; ++i) if (!got[i]) ov[i] = ld128(rs, WS_OP + (xg + 8 * ((i >> 1) * 16 + 2 * w + (i & 1))) * 1024 + lane * 16);
      bool all = true;
#pragma unroll
      for (int i = 0; i < 4; ++i) if (!got[i]) {
        got[i] = __all((ov[i].x & ov[i].y & ov[i].z & ov[i].w & 1u) != 0u);
        all = all && got[i];
      }
      if (!((u32)!all & (u32)(++spins < GLB_SPIN_CAP))) break;
    }
#pragma unroll
    for (int hk = 0; hk < 2; ++hk) {
      const f32x4 s = __builtin_bit_cast(f32x4, ov[2 * hk]) + __builtin_bit_cast(f32x4, ov[2 * hk + 1]);
      *(LAS u32*)(L + L_OS + w * 1040 + hk * 512 + lane * 4) = pack2(s[0], s[2]);
      *(LAS u32*)(L + L_OS + w * 1040 + hk * 512 + 256 + lane * 4) = pack2(s[1], s[3]);
    }
    BARRIER();
    if (w >= 1 && w <= 4) {
      const int ct = w - 1;
      const u32 ob = L_OS + (lane & 7) * 1040 + g * 16;
      u32x4 bfr[16];
#pragma unroll
      for (int s = 0; s < 16; ++s) bfr[s] = lds128(L, ob + s * 64);
      f32x4 a0 = {0, 0, 0, 0}, a1 = {0, 0, 0, 0}, a2 = {0, 0, 0, 0}, a3 = {0, 0, 0, 0};
#pragma unroll
      for (int s = 0; s < 16; s += 4) { a0 = mfma(wf[s], bfr[s], a0); a1 = mfma(wf[s + 1], bfr[s + 1], a1); a2 = mfma(wf[s + 2], bfr[s + 2], a2); a3 = mfma(wf[s + 3], bfr[s + 3], a3); }
      const f32x4 a = (a0 + a1) + (a2 + a3);
      if (r16 < 8) st128(rs, WS_WP + (xg * 144 + (u * 4 + ct) * 4 + g) * 128 + r16 * 16, u32x4{vld(a[0]), vld(a[1]), vld(a[2]), vld(a[3])});
    } else if (w == 7) {
      u32x4 pa, pb; u32 spins2 = 0;
      __builtin_amdgcn_s_sleep(DELAY_B);
      const int wo_ = WS_WP + ((lane >> 3) * 144 + 2 * mu) * 128 + (lane & 7) * 16;
#pragma unroll 1
      for (;;) {
        MEMBAR();
        pa = ld128(rs, wo_); pb = ld128(rs, wo_ + 128);
        if (!((u32)!__all((pa.x & pa.y & pa.z & pa.w & pb.x & pb.y & pb.z & pb.w & 1u) != 0u) & (u32)(++spins2 < GLB_SPIN_CAP))) break;
      }
      float a[8];
      a[0] = __uint_as_float(pa.x); a[1] = __uint_as_float(pa.y); a[2] = __uint_as_float(pa.z); a[3] = __uint_as_float(pa.w);
      a[4] = __uint_as_float(pb.x); a[5] = __uint_as_float(pb.y); a[6] = __uint_as_float(pb.z); a[7] = __uint_as_float(pb.w);
#pragma unroll
      for (int i = 0; i < 8; ++i) a[i] = sum_x8_16_32(a[i]);
      u32x4 xo;
#pragma unroll
      for (int i = 0; i < 4; ++i) {
        const u32 ap = pack2(a[2 * i], a[2 * i + 1]);
        const u32 tp = pack2(bflo(pre4a[i]) * bflo(ap), bfhi(pre4a[i]) * bfhi(ap));
        xo[i] = nosent(pack2(bflo(pre4b[i]) + bflo(tp), bfhi(pre4b[i]) + bfhi(tp)));
      }
      st128(rs, WS_X1 + (lane >> 3) * 9216 + mu * 128 + (lane & 7) * 16, xo);
    }
  } else if (isF) {
    const u32 n0 = Wtot + 32u * (u32)nsl;
    if (w == 7) {
      u32x4 pv; u32 spins = 0;
      __builtin_amdgcn_s_sleep(DELAY_W);
#pragma unroll 1
      for (;;) {
        MEMBAR();
        pv = ld128(rs, WS_WP + ((lane >> 3) * 144 + f) * 128 + (lane & 7) * 16);
        if (!((u32)!__all((pv.x & pv.y & pv.z & pv.w & 1u) != 0u) & (u32)(++spins < GLB_SPIN_CAP))) break;
      }
      F[F_X1] = 1u;
    } else lds_wait_ge(F, F_X1, 1u);
    __builtin_amdgcn_s_sleep(DELAY_C);
    u32x2 x1p = {0u, 0u};
    {
      if (w != 0) {
        const int e0 = (w - 1) * 64 + lane, xb = WS_X1 + xg * 9216;
        u32x4 xv, xw = {0u, 0u, 0u, 0u}; u32 spins = 0;
#pragma unroll 1
        for (;;) {
          MEMBAR();
          xv = ld128(rs, xb + e0 * 16);
          if (w <= 2) xw = ld128(rs, xb + (448 + e0) * 16);
          if (!((u32)!__all(umax(umax4(xv), umax4(xw)) != SENT) & (u32)(++spins < GLB_SPIN_CAP))) break;
        }
        const u32 a = L_ACT + (e0 & 7) * ACT_STRIDE + (e0 >> 3) * 16;
        *(LAS u32x4*)(L + a) = xv;
        if (w <= 2) *(LAS u32x4*)(L + a + 56 * 16) = xw;
      }
      BARRIER();
      u32 xr[5];
#pragma unroll
      for (int i = 0; i < 4; ++i) xr[i] = *(LAS u32*)(L + L_ACT + w * ACT_STRIDE + (lane + 64 * i) * 4);
      xr[4] = *(LAS u32*)(L + L_ACT + w * ACT_STRIDE + (256 + (lane & 31)) * 4);
      if (w == 7) x1p = *(LAS u32x2*)(L + L_ACT + (lane & 7) * ACT_STRIDE + f * 8);
      float v[10];
#pragma unroll
      for (int i = 0; i < 5; ++i) { v[2 * i] = bflo(xr[i]); v[2 * i + 1] = bfhi(xr[i]); }
      float s = (v[0] + v[1]) + (v[2] + v[3]) + ((v[4] + v[5]) + (v[6] + v[7]));
      if (lane < 32) s += v[8] + v[9];
      const float mean = wave_sum(s) * (1.0f / DM);
      float q2 = 0.f, q3 = 0.f;
#pragma unroll
      for (int i = 0; i < 4; ++i) { const float d0 = v[2 * i] - mean, d1 = v[2 * i + 1] - mean; q2 += d0 * d0; q3 += d1 * d1; }
      if (lane < 32) { const float d0 = v[8] - mean, d1 = v[9] - mean; q2 += d0 * d0; q3 += d1 * d1; }
      const float var = wave_sum(q2 + q3) * (1.0f / DM);
      const float mean_r = rb(mean);
      const float rinv = __builtin_amdgcn_rcpf(rb(__builtin_amdgcn_sqrtf(rb(rb(var) + 1e-5f))));
#pragma unroll
      for (int i = 0; i < 5; ++i) {
        const u32 dp = pack2(v[2 * i] - mean_r, v[2 * i + 1] - mean_r);
        const u32 np = pack2(bflo(dp) * rinv, bfhi(dp) * rinv);
        const u32 tp = pack2(bflo(np) * bflo(cbp[i]), bfhi(np) * bfhi(cbp[i]));
        const u32 hp = pack2(bflo(tp) + bflo(sbp[i]), bfhi(tp) + bfhi(sbp[i]));
        if (i < 4) *(LAS u32*)(L + L_H2 + w * ACT_STRIDE + (lane + 64 * i) * 4) = hp;
        else if (lane < 32) *(LAS u32*)(L + L_H2 + w * ACT_STRIDE + (256 + lane) * 4) = hp;
      }
    }
    if (w == 0) { WAIT_VM0(); F[F_LANDED] = nfin; }
    BARRIER();
    if (w == 7) {
      BARRIER();
      BARRIER();
      const int off = WS_FP + (lane >> 3) * 9216 + ((f >> 3) * 4 + (f & 3)) * 128 + (lane & 7) * 16 + ((f >> 2) & 1) * 8;
      u32x2 e[18]; u32 spins = 0;
      u32 slo = 0x00003f80u, shi = 0x3f800000u;
      asm volatile("" : "+v"(slo), "+v"(shi));
      __builtin_amdgcn_s_sleep(DELAY_E);
#pragma unroll 1
      for (;;) {
        MEMBAR();
#pragma unroll
        for (int i = 0; i < 18; ++i) e[i] = ld64(rs, off + i * 8 * 9216);
        u32 mx = 0;
#pragma unroll
        for (int i = 0; i < 18; ++i) mx = umax(mx, umax(e[i].x, e[i].y));
        if (!((u32)!__all(mx != SENT) & (u32)(++spins < GLB_SPIN_CAP))) break;
      }
      f32x4 s = {0, 0, 0, 0}, s1 = {0, 0, 0, 0};
#pragma unroll
      for (int i = 0; i < 18; i += 2) {
        s[0] = add_sel(e[i].x, slo, s[0]); s[1] = add_sel(e[i].x, shi, s[1]); s[2] = add_sel(e[i].y, slo, s[2]); s[3] = add_sel(e[i].y, shi, s[3]);
        s1[0] = add_sel(e[i + 1].x, slo, s1[0]); s1[1] = add_sel(e[i + 1].x, shi, s1[1]); s1[2] = add_sel(e[i + 1].y, slo, s1[2]); s1[3] = add_sel(e[i + 1].y, shi, s1[3]);
      }
      s += s1;
      const u32 dA = pack2(sum_x8_16_32(s[0]), sum_x8_16_32(s[1])), dB = pack2(sum_x8_16_32(s[2]), sum_x8_16_32(s[3]));
      const u32 tA = pack2(bflo(pre4a.x) * bflo(dA), bfhi(pre4a.x) * bfhi(dA)), tB = pack2(bflo(pre4a.y) * bflo(dB), bfhi(pre4a.y) * bfhi(dB));
      if (lane < 8) *(GAS u32x2*)(p.out + lane * DM + f * 4) = u32x2{pack2(bflo(x1p.x) + bflo(tA), bfhi(x1p.x) + bfhi(tA)), pack2(bflo(x1p.y) + bflo(tB), bfhi(x1p.y) + bfhi(tB))};
    } else {
      const int cw = w - 1;
      if (w >= 1) {
        const int kt = cw >> 1;
        const u32 hb = L_H2 + (lane & 7) * ACT_STRIDE + kt * 384 + g * 16;
        u32x4 bfr[6];
#pragma unroll
        for (int s = 0; s < 6; ++s) bfr[s] = lds128(L, hb + s * 64);
        f32x4 a0 = {0, 0, 0, 0}, a1 = {0, 0, 0, 0}, a2 = {0, 0, 0, 0}, a3 = {0, 0, 0, 0};
#pragma unroll
        for (int s = 0; s < 6; s += 2) { a0 = mfma(wf[2 * s], bfr[s], a0); a1 = mfma(wf[2 * s + 1], bfr[s], a1); a2 = mfma(wf[2 * s + 2], bfr[s + 1], a2); a3 = mfma(wf[2 * s + 3], bfr[s + 1], a3); }
        a0 += a2; a1 += a3;
        if (r16 < 8) { *(LAS f32x4*)(L + L_R2 + cw * 1024 + (g * 8 + r16) * 16) = a0; *(LAS f32x4*)(L + L_R2 + cw * 1024 + 512 + (g * 8 + r16) * 16) = a1; }
        lds_wait_ge(F, F_LANDED, n0 + 36u);
        const u32 d0 = L_RING + posd * 16384 + (8 * g + qp) * 1152 + cw * 192 + 8 * pp;
#pragma unroll
        for (int t = 0; t < 6; ++t) wf[12 + t] = tr_frag(L, d0 + t * 32, 4 * 1152);
      }
      BARRIER();
      if (w < 4) {
        const int ct = w >> 1, e = (w & 1) * 64 + lane;
        const u32 a = L_R2 + ct * 512 + e * 4;
        const float gg = rb(*(LAS float*)(L + a) + *(LAS float*)(L + a + 2048) + *(LAS float*)(L + a + 4096));
        const float uu = rb(*(LAS float*)(L + a + 1024) + *(LAS float*)(L + a + 3072) + *(LAS float*)(L + a + 5120));
        const float inner = 0.7978845608028654f * (gg + 0.044715f * gg * gg * gg);
        const float th = 1.0f - 2.0f * __builtin_amdgcn_rcpf(1.0f + __expf(2.0f * inner));
        const float ge = rb(0.5f * gg * (1.0f + th));
        const int ls = e >> 2, rr = e & 3, b_ = ls & 7, g_ = ls >> 3;
        *(LAS u16*)(L + L_HH + b_ * 80 + (ct * 16 + g_ * 4 + rr) * 2) = (u16)f2bf(ge * uu);
      }
      BARRIER();
      if (w >= 1) {
        const u32x4 bfr = lds128(L, L_HH + (lane & 7) * 80 + g * 16);
        f32x4 a[6];
#pragma unroll
        for (int t = 0; t < 6; ++t) a[t] = mfma(wf[12 + t], bfr, f32x4{0, 0, 0, 0});
#pragma unroll
        for (int t = 0; t < 6; t += 2)
          if (r16 < 8) st128(rs, WS_FP + f * 9216 + ((cw * 3 + (t >> 1)) * 4 + g) * 128 + r16 * 16,
                             u32x4{nosent(pack2(a[t][0], a[t][1])), nosent(pack2(a[t][2], a[t][3])), nosent(pack2(a[t + 1][0], a[t + 1][1])), nosent(pack2(a[t + 1][2], a[t + 1][3]))});
      }
    }
  }
}

}

extern "C" void kernel_launch(void* const* d_in, const int* in_sizes, int n_in, void* d_out, int out_size, void* d_ws, size_t ws_size,
                              hipStream_t stream) {
  (void)in_sizes; (void)n_in; (void)out_size; (void)ws_size;
  Params p;
  p.x = (const u16*)d_in[0];
  p.kc = (const unsigned char*)d_in[1];
  p.vc = (const unsigned char*)d_in[2];
  p.wq = (const unsigned char*)d_in[3];
  p.wo = (const unsigned char*)d_in[4];
  p.wg = (const unsigned char*)d_in[6];
  p.wu = (const unsigned char*)d_in[7];
  p.wd = (const unsigned char*)d_in[8];
  p.c = (const u16*)d_in[9];
  p.wmod = (const unsigned char*)d_in[10];
  p.out = (u16*)d_out;
  p.ws = (unsigned char*)d_ws;
  (void)hipFuncSetAttribute((const void*)fused_block, hipFuncAttributeMaxDynamicSharedMemorySize, LDS_BYTES);
  hipLaunchKernelGGL(fused_block, dim3(NCU), dim3(NT), LDS_BYTES, stream, p);
}
```
